```python
import torch

def _softmax(x, axis=-1):
    return torch.softmax(x, dim=axis)
def _gelu(x):
    return torch.nn.functional.gelu(x, approximate="tanh")
def _silu(x):
    return torch.nn.functional.silu(x)
def _sigmoid(x):
    return torch.sigmoid(x)
def _layernorm(x, axis=-1, eps=1e-5):
    m = x.mean(dim=axis, keepdim=True)
    v = x.var(dim=axis, keepdim=True, unbiased=False)
    return (x - m) / torch.sqrt(v + eps)
def _scatter_add(tgt, idx, src, axis=0):
    return tgt.clone().index_add_(axis, idx, src)
def _segment_sum(src, seg, n):
    out = torch.zeros((n,) + src.shape[1:], dtype=src.dtype)
    return out.index_add_(0, seg, src)
def _pad(x, pw):
    flat = tuple(v for pair in reversed(pw) for v in pair)
    return torch.nn.functional.pad(x, flat)

kernel_name = 'gated_mix__mha_block_cached__sliding_block_cached__decode'

def setup_inputs():

    _w = (lambda a: (lambda t: t.to(torch.bfloat16) if t.is_floating_point() else t)(torch.as_tensor(a)))
    def _A_setup():
        B, Sq, D = 4, 1, 512
        H, Sk, Dh = 32, 8192, 128
        return {
            "x": _w((lambda *s: torch.randn(*s, dtype=torch.bfloat16) * 0.05)(B, Sq, D)),
            "k_cache": _w((lambda *s: torch.randn(*s, dtype=torch.bfloat16) * 6.4)(B, H, Sk, Dh)),
            "v_cache": _w((lambda *s: torch.randn(*s, dtype=torch.bfloat16) * 0.05)(B, H, Sk, Dh)),
            "wq": _w((lambda *s: torch.randn(*s, dtype=torch.bfloat16) * 6.4)(D, H * Dh) * (D ** -0.5)),
            "wo": _w((lambda *s: torch.randn(*s, dtype=torch.bfloat16) * 0.05)(H * Dh, D) * ((H * Dh) ** -0.5)),
        }
    _A_in = _A_setup()

    def _B_setup():
        B, Sq, D = 4, 1, 512
        Hq, Hkv, Sk, Dh = 32, 8, 8192, 128
        assert Hq % Hkv == 0, (Hq, Hkv)
        return {
            "x": _w((lambda *s: torch.randn(*s, dtype=torch.bfloat16) * 0.05)(B, Sq, D)),
            "k_cache": _w((lambda *s: torch.randn(*s, dtype=torch.bfloat16) * 6.4)(B, Hkv, Sk, Dh)),
            "v_cache": _w((lambda *s: torch.randn(*s, dtype=torch.bfloat16) * 0.05)(B, Hkv, Sk, Dh)),
            "wq": _w((lambda *s: torch.randn(*s, dtype=torch.bfloat16) * 6.4)(D, Hq * Dh) * (D ** -0.5)),
            "wo": _w((lambda *s: torch.randn(*s, dtype=torch.bfloat16) * 0.05)(Hq * Dh, D) * ((Hq * Dh) ** -0.5)),
        }
    _B_in = _B_setup()

    return {
        "A_x": _A_in["x"],
        "A_k_cache": _A_in["k_cache"],
        "A_v_cache": _A_in["v_cache"],
        "A_wq": _A_in["wq"],
        "A_wo": _A_in["wo"],
        "B_x": _B_in["x"],
        "B_k_cache": _B_in["k_cache"],
        "B_v_cache": _B_in["v_cache"],
        "B_wq": _B_in["wq"],
        "B_wo": _B_in["wo"],
        "gate_w": _w((lambda *s: torch.randn(*s, dtype=torch.bfloat16) * 0.05)(512) * 16.0),
    }

def reference(A_x, A_k_cache, A_v_cache, A_wq, A_wo, B_x, B_k_cache, B_v_cache, B_wq, B_wo, gate_w):
    def _A_ref(x, k_cache, v_cache, wq, wo):
        B, Sq, D = x.shape
        H, Dh = 32, 128
        q = (lambda x, *d: x.permute(*d))((x @ wq).reshape(B, Sq, H, Dh), 0, 2, 1, 3)
        scores = torch.einsum("bhqd,bhkd->bhqk", q, k_cache) * (Dh ** -0.5)
        p = _softmax(scores, axis=-1)
        o = torch.einsum("bhqk,bhkd->bhqd", p, v_cache)
        return (lambda x, *d: x.permute(*d))(o, 0, 2, 1, 3).reshape(B, Sq, H * Dh) @ wo

    def _B_ref(x, k_cache, v_cache, wq, wo):
        B, Sq, D = x.shape
        Hq, Dh = 32, 128
        Hkv, Sk = k_cache.shape[1], k_cache.shape[2]
        W = min(4096, max(256, Sk // 4))
        rep = Hq // Hkv
        q = (lambda x, *d: x.permute(*d))((x @ wq).reshape(B, Sq, Hq, Dh), 0, 2, 1, 3)
        k = (lambda x, n, axis: torch.repeat_interleave(x, n, dim=axis))(k_cache[:, :, Sk - W:, :], rep, axis=1)
        v = (lambda x, n, axis: torch.repeat_interleave(x, n, dim=axis))(v_cache[:, :, Sk - W:, :], rep, axis=1)
        scores = torch.einsum("bhqd,bhkd->bhqk", q, k) * (Dh ** -0.5)
        p = _softmax(scores, axis=-1)
        o = torch.einsum("bhqk,bhkd->bhqd", p, v)
        return (lambda x, *d: x.permute(*d))(o, 0, 2, 1, 3).reshape(B, Sq, Hq * Dh) @ wo

    x = A_x
    g = _sigmoid((x * gate_w).sum(-1, keepdims=True))
    a = _A_ref(x, A_k_cache, A_v_cache, A_wq, A_wo)
    b = _B_ref(x, B_k_cache, B_v_cache, B_wq, B_wo)
    return g * a + (1 - g) * b
```

```cpp
#include <hip/hip_runtime.h>
#include <hip/hip_bf16.h>
#include <cstdint>

typedef unsigned u32x4 __attribute__((ext_vector_type(4)));
typedef float f32x4 __attribute__((ext_vector_type(4)));
typedef __attribute__((ext_vector_type(2))) __bf16 bf16x2;

constexpr int NB = 4, DM = 512, NH = 32, SK = 8192, DH = 128, HKV = 8, WIN = 2048;
constexpr float C2 = 0.088388347648318440f * 1.4426950408889634f;

__device__ __forceinline__ float dot2(unsigned a, unsigned b, float c) {
  return __builtin_amdgcn_fdot2_f32_bf16(__builtin_bit_cast(bf16x2, a), __builtin_bit_cast(bf16x2, b), c, false);
}
__device__ __forceinline__ unsigned cvtpk(float lo, float hi) {
  unsigned r; asm volatile("v_cvt_pk_bf16_f32 %0, %1, %2" : "=v"(r) : "v"(lo), "v"(hi)); return r;
}
template <int CTRL> __device__ __forceinline__ float dpp(float x) {
  return __builtin_bit_cast(float, __builtin_amdgcn_mov_dpp(__builtin_bit_cast(int, x), CTRL, 0xf, 0xf, true));
}
constexpr int XOR1 = 0xB1, XOR2 = 0x4E, XOR7 = 0x141, XOR8 = 0x128;
__device__ __forceinline__ float row16_sum(float x) {
  x += dpp<XOR1>(x); x += dpp<XOR2>(x); x += dpp<XOR7>(x); x += dpp<XOR8>(x); return x;
}
__device__ __forceinline__ float xrow_sum(float x) {
  auto s = __builtin_amdgcn_permlane16_swap(__float_as_uint(x), __float_as_uint(x), false, false);
  x = __uint_as_float(s[0]) + __uint_as_float(s[1]);
  auto t = __builtin_amdgcn_permlane32_swap(__float_as_uint(x), __float_as_uint(x), false, false);
  return __uint_as_float(t[0]) + __uint_as_float(t[1]);
}
__device__ __forceinline__ float xrow_max(float x) {
  auto s = __builtin_amdgcn_permlane16_swap(__float_as_uint(x), __float_as_uint(x), false, false);
  x = fmaxf(__uint_as_float(s[0]), __uint_as_float(s[1]));
  auto t = __builtin_amdgcn_permlane32_swap(__float_as_uint(x), __float_as_uint(x), false, false);
  return fmaxf(__uint_as_float(t[0]), __uint_as_float(t[1]));
}
__device__ __forceinline__ float bflo(unsigned u) { return __uint_as_float(u << 16); }
__device__ __forceinline__ float bfhi(unsigned u) { return __uint_as_float(u & 0xffff0000u); }
__device__ __forceinline__ u32x4 ldnt(const u32x4* p) { return __builtin_nontemporal_load(p); }

constexpr size_t WS_Q = 0;
constexpr size_t WS_PA = 65536;
constexpr size_t WS_PB = WS_PA + 128 * 2 * 132 * 4;

__global__ void __launch_bounds__(256) k_qproj(const unsigned short* __restrict__ x, const unsigned short* __restrict__ wqa, const unsigned short* __restrict__ wqb, unsigned short* __restrict__ q) {
  __shared__ float red[32][NB][64];
  __shared__ float xs[NB][DM];
  const int tid = threadIdx.x, br = blockIdx.x >> 6, c0 = (blockIdx.x & 63) * 64;
  const unsigned short* w = br ? wqb : wqa;
  const int cg = tid & 7, rg = tid >> 3;
  u32x4 wv[16];
#pragma unroll
  for (int i = 0; i < 16; ++i) wv[i] = ldnt(reinterpret_cast<const u32x4*>(w + (size_t)(rg * 16 + i) * 4096 + c0 + cg * 8));
  for (int i = tid; i < NB * DM; i += 256) xs[i >> 9][i & 511] = __uint_as_float((unsigned)x[i] << 16);
  __syncthreads();
  float acc[NB][8];
#pragma unroll
  for (int b = 0; b < NB; ++b)
#pragma unroll
    for (int j = 0; j < 8; ++j) acc[b][j] = 0.f;
#pragma unroll
  for (int i = 0; i < 16; ++i)
#pragma unroll
    for (int b = 0; b < NB; ++b) {
      const float xv = xs[b][rg * 16 + i];
#pragma unroll
      for (int j = 0; j < 4; ++j) { acc[b][2 * j] = fmaf(xv, bflo(wv[i][j]), acc[b][2 * j]); acc[b][2 * j + 1] = fmaf(xv, bfhi(wv[i][j]), acc[b][2 * j + 1]); }
    }
#pragma unroll
  for (int b = 0; b < NB; ++b)
#pragma unroll
    for (int j = 0; j < 8; ++j) red[rg][b][cg * 8 + j] = acc[b][j];
  __syncthreads();
  const int b = tid >> 6, c = tid & 63;
  float s = 0.f;
#pragma unroll
  for (int r = 0; r < 32; ++r) s += red[r][b][c];
  q[(size_t)(br * NB + b) * 4096 + c0 + c] = (unsigned short)(cvtpk(s, 0.f) & 0xffffu);
}

constexpr int NW = 8, T = 4;
template <int G>
__device__ __forceinline__ void attn_step(const u32x4 (&kc)[T], const u32x4 (&vc)[T], const u32x4 (&q)[G], float (&m)[G], float (&l)[G], float (&acc)[G][8]) {
#pragma unroll
  for (int g = 0; g < G; ++g) {
    float s[T];
#pragma unroll
    for (int t = 0; t < T; ++t) {
      float d = dot2(kc[t][0], q[g][0], 0.f); d = dot2(kc[t][1], q[g][1], d); d = dot2(kc[t][2], q[g][2], d); d = dot2(kc[t][3], q[g][3], d);
      s[t] = row16_sum(d) * C2;
    }
    float mn = m[g];
#pragma unroll
    for (int t = 0; t < T; ++t) mn = fmaxf(mn, s[t]);
    const float al = __builtin_amdgcn_exp2f(m[g] - mn);
    m[g] = mn;
    float ls = l[g] * al;
#pragma unroll
    for (int k = 0; k < 8; ++k) acc[g][k] *= al;
#pragma unroll
    for (int t = 0; t < T; ++t) {
      const float p = __builtin_amdgcn_exp2f(s[t] - mn);
      ls += p;
#pragma unroll
      for (int k = 0; k < 4; ++k) { acc[g][2 * k] = fmaf(p, bflo(vc[t][k]), acc[g][2 * k]); acc[g][2 * k + 1] = fmaf(p, bfhi(vc[t][k]), acc[g][2 * k + 1]); }
    }
    l[g] = ls;
  }
}

template <int G>
__device__ __forceinline__ void attn_run(const u32x4* k, const u32x4* v, int nsteps, const unsigned short* qg, float* lds, float* outp, int out_stride) {
  const int tid = threadIdx.x, lane = tid & 63, wave = __builtin_amdgcn_readfirstlane(tid >> 6), r = lane >> 4, c = lane & 15;
  k += wave * 64 * T + lane; v += wave * 64 * T + lane;
  u32x4 kn[T], vn[T];
#pragma unroll
  for (int t = 0; t < T; ++t) kn[t] = ldnt(k + t * 64);
#pragma unroll
  for (int t = 0; t < T; ++t) vn[t] = ldnt(v + t * 64);
  u32x4 q[G];
#pragma unroll
  for (int g = 0; g < G; ++g) q[g] = *reinterpret_cast<const u32x4*>(qg + g * DH + c * 8);
  float m[G], l[G], acc[G][8];
#pragma unroll
  for (int g = 0; g < G; ++g) { m[g] = -1e30f; l[g] = 0.f;
#pragma unroll
    for (int j = 0; j < 8; ++j) acc[g][j] = 0.f; }
  for (int i = 1; i < nsteps; ++i) {
    u32x4 kc[T], vc[T];
    k += NW * 64 * T; v += NW * 64 * T;
#pragma unroll
    for (int t = 0; t < T; ++t) { kc[t] = kn[t]; kn[t] = ldnt(k + t * 64); }
#pragma unroll
    for (int t = 0; t < T; ++t) { vc[t] = vn[t]; vn[t] = ldnt(v + t * 64); }
    attn_step<G>(kc, vc, q, m, l, acc);
  }
  attn_step<G>(kn, vn, q, m, l, acc);
#pragma unroll
  for (int g = 0; g < G; ++g) {
    const float M = xrow_max(m[g]);
    const float w = __builtin_amdgcn_exp2f(m[g] - M);
    const float L = xrow_sum(l[g] * w);
    float* rec = lds + (wave * G + g) * 132;
#pragma unroll
    for (int j = 0; j < 8; ++j) { const float a = xrow_sum(acc[g][j] * w); if (r == 0) rec[4 + c * 8 + j] = a; }
    if (lane == 0) { rec[0] = M; rec[1] = L; }
  }
  __syncthreads();
  if (tid < G * DH) {
    const int g = tid >> 7, d = tid & 127;
    float M = -1e30f;
#pragma unroll
    for (int w = 0; w < NW; ++w) M = fmaxf(M, lds[(w * G + g) * 132]);
    float L = 0.f, o = 0.f;
#pragma unroll
    for (int w = 0; w < NW; ++w) { const float* rec = lds + (w * G + g) * 132; const float wt = __builtin_amdgcn_exp2f(rec[0] - M); L = fmaf(wt, rec[1], L); o = fmaf(wt, rec[4 + d], o); }
    float* dst = outp + g * out_stride;
    dst[4 + d] = o;
    if (d == 0) { dst[0] = M; dst[1] = L; }
  }
  __syncthreads();
}

__global__ void __launch_bounds__(NW * 64) k_attn(const u32x4* __restrict__ ka, const u32x4* __restrict__ va, const u32x4* __restrict__ kb, const u32x4* __restrict__ vb, unsigned char* __restrict__ ws) {
  __shared__ __attribute__((aligned(16))) float lds[NW * 4 * 132];
  const int bid = blockIdx.x;
  const unsigned short* q = reinterpret_cast<const unsigned short*>(ws + WS_Q);
  {
    const int grp = bid >> 3, sp = bid & 7, b = grp >> 3, kvh = grp & 7;
    const size_t off = ((size_t)grp * SK + (SK - WIN) + sp * 256) * (DH / 8);
    attn_run<4>(kb + off, vb + off, 256 / (NW * T * 4), q + (size_t)(NB + b) * 4096 + kvh * 4 * DH, lds,
                reinterpret_cast<float*>(ws + WS_PB) + ((size_t)(b * NH + kvh * 4) * 8 + sp) * 132, 8 * 132);
  }
  {
    const int pair = bid >> 1, b = pair >> 5, h = pair & 31;
    const size_t off = (size_t)bid * 4096 * (DH / 8);
    attn_run<1>(ka + off, va + off, 4096 / (NW * T * 4), q + (size_t)b * 4096 + h * DH, lds, reinterpret_cast<float*>(ws + WS_PA) + (size_t)bid * 132, 0);
  }
}

__global__ void __launch_bounds__(1024) k_out(const unsigned short* __restrict__ x, const unsigned short* __restrict__ gw, const unsigned short* __restrict__ woa, const unsigned short* __restrict__ wob,
                                              const unsigned char* __restrict__ ws, unsigned short* __restrict__ out) {
  __shared__ float red[16][NB][8];
  __shared__ float gs[NB];
  const int tid = threadIdx.x, lane = tid & 63, wave = tid >> 6;
  const int cgrp = (blockIdx.x & 7) * 8 + (blockIdx.x >> 3);
  const float* pa = reinterpret_cast<const float*>(ws + WS_PA);
  const float* pb = reinterpret_cast<const float*>(ws + WS_PB);
  u32x4 wa[4], wb[4];
#pragma unroll
  for (int i = 0; i < 4; ++i) { wa[i] = *reinterpret_cast<const u32x4*>(woa + (size_t)(tid + 1024 * i) * DM + cgrp * 8); wb[i] = *reinterpret_cast<const u32x4*>(wob + (size_t)(tid + 1024 * i) * DM + cgrp * 8); }
  if (wave < NB) {
    float s = 0.f;
#pragma unroll
    for (int i = 0; i < 8; ++i) s += __uint_as_float((unsigned)x[wave * DM + lane + 64 * i] << 16) * __uint_as_float((unsigned)gw[lane + 64 * i] << 16);
    s = xrow_sum(row16_sum(s));
    if (lane == 0) gs[wave] = 1.f / (1.f + __expf(-s));
  }
  __syncthreads();
  float acc[NB][8];
#pragma unroll
  for (int b = 0; b < NB; ++b) {
#pragma unroll
    for (int j = 0; j < 8; ++j) acc[b][j] = 0.f;
    const float g = gs[b];
#pragma unroll
    for (int i = 0; i < 4; ++i) {
      const int row = tid + 1024 * i, h = row >> 7, d = row & 127;
      const float* r0 = pa + (size_t)((b * NH + h) * 2) * 132; const float* r1 = r0 + 132;
      const float M = fmaxf(r0[0], r1[0]), w0 = __builtin_amdgcn_exp2f(r0[0] - M), w1 = __builtin_amdgcn_exp2f(r1[0] - M);
      const float oa = g * (w0 * r0[4 + d] + w1 * r1[4 + d]) / (w0 * r0[1] + w1 * r1[1]);
      const float* rb = pb + (size_t)((b * NH + h) * 8) * 132;
      float MB = -1e30f;
#pragma unroll
      for (int s = 0; s < 8; ++s) MB = fmaxf(MB, rb[s * 132]);
      float L = 0.f, o = 0.f;
#pragma unroll
      for (int s = 0; s < 8; ++s) { const float wt = __builtin_amdgcn_exp2f(rb[s * 132] - MB); L = fmaf(wt, rb[s * 132 + 1], L); o = fmaf(wt, rb[s * 132 + 4 + d], o); }
      const float ob = (1.f - g) * o / L;
#pragma unroll
      for (int j = 0; j < 4; ++j) {
        acc[b][2 * j] += oa * bflo(wa[i][j]) + ob * bflo(wb[i][j]);
        acc[b][2 * j + 1] += oa * bfhi(wa[i][j]) + ob * bfhi(wb[i][j]);
      }
    }
#pragma unroll
    for (int j = 0; j < 8; ++j) { const float s = xrow_sum(row16_sum(acc[b][j])); if (lane == 0) red[wave][b][j] = s; }
  }
  __syncthreads();
  if (tid < NB * 8) {
    const int b = tid >> 3, j = tid & 7;
    float s = 0.f;
#pragma unroll
    for (int w = 0; w < 16; ++w) s += red[w][b][j];
    out[b * DM + cgrp * 8 + j] = (unsigned short)(cvtpk(s, 0.f) & 0xffffu);
  }
}

extern "C" void kernel_launch(void* const* d_in, const int* in_sizes, int n_in, void* d_out, int out_size, void* d_ws, size_t ws_size, hipStream_t stream) {
  const unsigned short* x = (const unsigned short*)d_in[0];
  unsigned char* ws = (unsigned char*)d_ws;
  k_qproj<<<128, 256, 0, stream>>>(x, (const unsigned short*)d_in[3], (const unsigned short*)d_in[8], (unsigned short*)(ws + WS_Q));
  k_attn<<<256, NW * 64, 0, stream>>>((const u32x4*)d_in[1], (const u32x4*)d_in[2], (const u32x4*)d_in[6], (const u32x4*)d_in[7], ws);
  k_out<<<64, 1024, 0, stream>>>(x, (const unsigned short*)d_in[10], (const unsigned short*)d_in[4], (const unsigned short*)d_in[9], ws, (unsigned short*)d_out);
}
```

```cpp
#include <hip/hip_runtime.h>
#include <hip/hip_bf16.h>
#include <cstdint>

typedef unsigned u32x4 __attribute__((ext_vector_type(4)));
typedef unsigned u32x2 __attribute__((ext_vector_type(2)));
typedef float f32x4 __attribute__((ext_vector_type(4)));
typedef float f32x2 __attribute__((ext_vector_type(2)));
typedef __attribute__((ext_vector_type(2))) __bf16 bf16x2;
typedef unsigned long long u64;
typedef unsigned short u16;
#define GAS __attribute__((address_space(1)))
#define LAS __attribute__((address_space(3)))
#define RLX_AGENT __ATOMIC_RELAXED, __HIP_MEMORY_SCOPE_AGENT
#define RLX_WG __ATOMIC_RELAXED, __HIP_MEMORY_SCOPE_WORKGROUP
#define DRAIN() asm volatile("s_waitcnt vmcnt(0)" ::: "memory")
#define LDS_BARRIER() do { asm volatile("s_waitcnt lgkmcnt(0)" ::: "memory"); __builtin_amdgcn_s_barrier(); asm volatile("" ::: "memory"); } while (0)
typedef __amdgpu_buffer_rsrc_t rsrc_t;

#define CAPV 5
#define CAPB 5
#define UPB_LOG 2
#define WO_CAP 1
#define QPOLL_DELAY 16
#define T_QB 0
#define T_QBG 1200
#define T_WO 2400
#define T_ST 500

constexpr int NB = 4, DM = 512, NH = 32, SK = 8192, DH = 128, WIN = 2048;
constexpr float SCALE = 0.088388347648318440f, L2E = 1.4426950408889634f;
constexpr int AUX_NT = 2, AUX_SC1 = 16, RSRC_W3 = 0x00020000;
constexpr int NU = 128;
constexpr unsigned SPIN_CAP = 1u << 15;

constexpr int WS_EPOCH = 0;
constexpr int WS_PH = 4096, NCP = 14;
constexpr int WS_QF = 32768;
constexpr int WS_QP = WS_QF + 32768;
constexpr int WS_PBM = WS_QP + 524288;
constexpr int WS_ST = WS_PBM + 16384;
constexpr int WS_PBP = WS_ST + 16384;
constexpr int WS_YG = WS_PBP + 262144;
constexpr int WS_STAMP = WS_YG + 256 * 4096;
constexpr int WS_BYTES = WS_STAMP + 2 * 256 * 256;

constexpr int L_MISC = 0, L_QA = 256, L_QB = 512, L_REC = 1536, L_PROG = 3584, L_SC = 4096, L_TMP = 12288, L_PUB = L_TMP + 8704, L_BIG = 22528, L_BYTES = L_BIG + 131072;
constexpr int REC_F = 132;
enum { M_TAGA = 0, M_TAGB = 1, M_KCLAIM = 2, M_VCLAIM = 3, M_QRDY = 4, M_HSYNC = 5, M_KDONE = 6, M_BDMA = 7, M_WMAX = 8, M_LSUM = 12, M_CNORM = 16, M_ANCH = 17 };

__device__ __forceinline__ float dot2(unsigned a, unsigned b, float c) {
  return __builtin_amdgcn_fdot2_f32_bf16(__builtin_bit_cast(bf16x2, a), __builtin_bit_cast(bf16x2, b), c, false);
}
__device__ __forceinline__ unsigned cvtpk(float lo, float hi) { return __builtin_bit_cast(unsigned, __builtin_convertvector(f32x2{lo, hi}, bf16x2)); }
__device__ __forceinline__ float bflo(unsigned u) { return __uint_as_float(u << 16); }
__device__ __forceinline__ float bfhi(unsigned u) { return __uint_as_float(u & 0xffff0000u); }
__device__ __forceinline__ float rbf(float x) { return bflo(cvtpk(x, 0.f)); }
__device__ __forceinline__ float ex2(float x) { return __builtin_amdgcn_exp2f(x); }
template <int CTRL> __device__ __forceinline__ float dpp(float x) {
  return __builtin_bit_cast(float, __builtin_amdgcn_mov_dpp(__builtin_bit_cast(int, x), CTRL, 0xf, 0xf, true));
}
constexpr int XOR1 = 0xB1, XOR2 = 0x4E, XOR7 = 0x141, XOR8 = 0x128;
__device__ __forceinline__ float row16_sum(float x) { x += dpp<XOR1>(x); x += dpp<XOR2>(x); x += dpp<XOR7>(x); x += dpp<XOR8>(x); return x; }
__device__ __forceinline__ float row16_max(float x) { x = fmaxf(x, dpp<XOR1>(x)); x = fmaxf(x, dpp<XOR2>(x)); x = fmaxf(x, dpp<XOR7>(x)); return fmaxf(x, dpp<XOR8>(x)); }
template <int CTRL> __device__ __forceinline__ float foldd(float a, float b, bool hi) { return (hi ? b : a) + dpp<CTRL>(hi ? a : b); }
__device__ __forceinline__ float fold32(float x, float y) {
  auto t = __builtin_amdgcn_permlane32_swap(__float_as_uint(x), __float_as_uint(y), false, false);
  return __uint_as_float(t[0]) + __uint_as_float(t[1]);
}
__device__ __forceinline__ float fold16(float x, float y) {
  auto t = __builtin_amdgcn_permlane16_swap(__float_as_uint(x), __float_as_uint(y), false, false);
  return __uint_as_float(t[0]) + __uint_as_float(t[1]);
}
__device__ __forceinline__ float xrow_sum(float x) { x = fold16(x, x); return fold32(x, x); }
__device__ __forceinline__ float xrow_max(float x) {
  auto s = __builtin_amdgcn_permlane16_swap(__float_as_uint(x), __float_as_uint(x), false, false);
  x = fmaxf(__uint_as_float(s[0]), __uint_as_float(s[1]));
  auto t = __builtin_amdgcn_permlane32_swap(__float_as_uint(x), __float_as_uint(x), false, false);
  return fmaxf(__uint_as_float(t[0]), __uint_as_float(t[1]));
}
__device__ __forceinline__ float wave_sum(float x) { return xrow_sum(row16_sum(x)); }
__device__ __forceinline__ rsrc_t mk_rsrc(const void* p, int bytes) { return __builtin_amdgcn_make_buffer_rsrc(const_cast<void*>(p), 0, bytes, RSRC_W3); }
template <int AUX> __device__ __forceinline__ u32x4 bld(rsrc_t r, int voff, int soff) { return __builtin_amdgcn_raw_buffer_load_b128(r, voff, soff, AUX); }
template <int AUX> __device__ __forceinline__ u32x2 bld2(rsrc_t r, int voff, int soff) { return __builtin_amdgcn_raw_buffer_load_b64(r, voff, soff, AUX); }
__device__ __forceinline__ void bst(u32x4 v, rsrc_t r, int voff, int soff) { __builtin_amdgcn_raw_buffer_store_b128(v, r, voff, soff, AUX_SC1); }
__device__ __forceinline__ unsigned plo(unsigned hi, unsigned lo) { return __builtin_amdgcn_perm(hi, lo, 0x05040100u); }
__device__ __forceinline__ unsigned phi(unsigned hi, unsigned lo) { return __builtin_amdgcn_perm(hi, lo, 0x07060302u); }
#define CAPWAIT(n) do { __builtin_amdgcn_sched_barrier(0); asm volatile("s_waitcnt vmcnt(%0)" :: "i"((n) - 1) : "memory"); } while (0)

#define ADAPT 1
#define TAU 28.0f
#define TK0 700
#define LSTEP 40
#define CENTER 1
#define FLOORW 2
#define T_GOSSIP 600
constexpr int TAU64 = (int)(TAU * 64);
#define WINWAIT(lvl) do { __builtin_amdgcn_sched_barrier(0); asm volatile("s_cmp_gt_i32 %0, 1\n\ts_cbranch_scc1 2f\n\ts_cmp_gt_i32 %0, 0\n\ts_cbranch_scc1 1f\n\ts_waitcnt vmcnt(3)\n1:\n\ts_waitcnt vmcnt(4)\n" \
    "2:\n\ts_cmp_gt_i32 %0, 2\n\ts_cbranch_scc1 3f\n\ts_waitcnt vmcnt(5)\n3:\n\ts_waitcnt vmcnt(6)" :: "s"(lvl) : "memory", "scc"); } while (0)
__device__ __forceinline__ int win_level(unsigned now, unsigned anchor, int n, int sw) {
  const int late = (int)(now - anchor) - ((n * TAU64) >> 6);
  const int lv = (late + (7 + CENTER - sw) * LSTEP + LSTEP / 2) / (4 * LSTEP);
  return sw < FLOORW ? max(lv, 1) : lv;
}
__device__ __forceinline__ void glds16(const GAS unsigned char* src, unsigned dst) {
  unsigned keep;
  asm volatile("s_mov_b32 %0, m0\n\ts_mov_b32 m0, %2\n\ts_nop 0\n\tglobal_load_lds_dwordx4 %1, off nt\n\ts_mov_b32 m0, %0" : "=&s"(keep) : "v"(src), "s"(dst) : "memory");
}

#define STAMP_AT(i) do {} while (0)
#define STAMP_LDS(i) do {} while (0)
#define PROG(u, base) do {} while (0)

__device__ __forceinline__ void wait_time(u64 t0, unsigned ticks) {
#pragma nounroll
  while (__builtin_amdgcn_s_memrealtime() - t0 < ticks) __builtin_amdgcn_s_sleep(4);
}
__device__ __forceinline__ void lds_wait_ge(LAS unsigned* p, unsigned target) {
#pragma nounroll
  for (unsigned spins = 0; spins < 32 * SPIN_CAP && (unsigned)__builtin_amdgcn_readfirstlane(__hip_atomic_load(p, RLX_WG)) < target; ++spins) __builtin_amdgcn_s_sleep(2);
  asm volatile("" ::: "memory");
}
__device__ __forceinline__ int claim(LAS unsigned* p, int lane) {
  unsigned r = 0;
  if (lane == 0) r = __builtin_amdgcn_atomic_inc32(p, 0xffffffffu, __ATOMIC_RELAXED, "workgroup");
  return (int)r;
}
__device__ __forceinline__ void lds_signal(LAS unsigned* p, int lane) {
  asm volatile("s_waitcnt lgkmcnt(0)" ::: "memory");
  if (lane == 0) __builtin_amdgcn_atomic_inc32(p, 0xffffffffu, __ATOMIC_RELAXED, "workgroup");
}
__device__ __forceinline__ void hsync(LAS unsigned* misc, unsigned n, int lane) { lds_signal(misc + M_HSYNC, lane); lds_wait_ge(misc + M_HSYNC, 4 * n); }
template <int N> __device__ __forceinline__ void poll(u32x4 (&g)[N], rsrc_t rws, int voff, int stride, int soff, unsigned tagA, unsigned tagB) {
#pragma nounroll
  for (unsigned spins = 0; spins < SPIN_CAP; ++spins) {
    asm volatile("" ::: "memory");
    bool ok = true;
#pragma unroll
    for (int i = 0; i < N; ++i) g[i] = bld<AUX_SC1>(rws, voff + i * stride, soff);
#pragma unroll
    for (int i = 0; i < N; ++i) ok &= (g[i][1] == tagA) & (g[i][3] == tagB);
    if (__all(ok)) break;
    __builtin_amdgcn_s_sleep(2);
  }
  __builtin_amdgcn_fence(__ATOMIC_ACQUIRE, "wavefront");
  asm volatile("" ::: "memory");
}

__device__ __forceinline__ void gossip(int k, LAS unsigned* misc, rsrc_t rws, int bid, unsigned tagA, int lane) {
  const unsigned kc = __hip_atomic_load(misc + M_KCLAIM, RLX_WG), vc = __hip_atomic_load(misc + M_VCLAIM, RLX_WG);
  const unsigned ph = (unsigned)__builtin_amdgcn_s_memrealtime() - (((kc < NU ? kc : NU + vc) * TAU64) >> 6);
  if (lane == 0) __builtin_amdgcn_raw_buffer_store_b64(u32x2{ph, tagA}, rws, 0, WS_PH + k * 2048 + bid * 8, AUX_SC1);
  if (k > 0) {
    asm volatile("" ::: "memory");
    const u32x4 g0 = bld<AUX_SC1>(rws, lane * 16, WS_PH + (k - 1) * 2048), g1 = bld<AUX_SC1>(rws, lane * 16, WS_PH + (k - 1) * 2048 + 1024);
    float d = 0.f, n = 0.f;
    if (g0[1] == tagA) { d += (float)(int)(g0[0] - ph); n += 1.f; }
    if (g0[3] == tagA) { d += (float)(int)(g0[2] - ph); n += 1.f; }
    if (g1[1] == tagA) { d += (float)(int)(g1[0] - ph); n += 1.f; }
    if (g1[3] == tagA) { d += (float)(int)(g1[2] - ph); n += 1.f; }
    d = wave_sum(d); n = wave_sum(n);
    if (n >= 128.f && lane == 0) __hip_atomic_store(misc + M_ANCH, ph + (int)(d / n), RLX_WG);
  }
}

__device__ __forceinline__ void gqa_step(const u32x4 (&k)[4], const u32x4 (&v)[4], const u32x4 (&q)[4], float (&m)[4], float (&l)[4], float (&acc)[4][8]) {
#pragma unroll
  for (int g = 0; g < 4; ++g) {
    float s[4];
#pragma unroll
    for (int t = 0; t < 4; ++t) { float d = dot2(k[t][0], q[g][0], 0.f); d = dot2(k[t][1], q[g][1], d); d = dot2(k[t][2], q[g][2], d); s[t] = dot2(k[t][3], q[g][3], d); }
#pragma unroll
    for (int t = 0; t < 4; t += 2) {
      const unsigned r1 = cvtpk(row16_sum(s[t]), row16_sum(s[t + 1]));
      const unsigned r2 = cvtpk(bflo(r1) * SCALE, bfhi(r1) * SCALE);
      s[t] = bflo(r2); s[t + 1] = bfhi(r2);
    }
    const float mn = fmaxf(fmaxf(m[g], fmaxf(s[0], s[1])), fmaxf(s[2], s[3]));
    const float al = ex2((m[g] - mn) * L2E), mL = -mn * L2E;
    m[g] = mn;
    float ls = l[g] * al;
#pragma unroll
    for (int j = 0; j < 8; ++j) acc[g][j] *= al;
#pragma unroll
    for (int t = 0; t < 4; t += 2) {
      const float p0 = ex2(fmaf(s[t], L2E, mL)), p1 = ex2(fmaf(s[t + 1], L2E, mL));
      ls += p0 + p1;
      const unsigned w = cvtpk(p0, p1);
#pragma unroll
      for (int j = 0; j < 4; ++j) {
        acc[g][2 * j] = dot2(v[t + 1][j], w >> 16, dot2(v[t][j], w & 0xffffu, acc[g][2 * j]));
        acc[g][2 * j + 1] = dot2(v[t + 1][j], w & 0xffff0000u, dot2(v[t][j], w << 16, acc[g][2 * j + 1]));
      }
    }
    l[g] = ls;
  }
}
__device__ __forceinline__ f32x2 fold_rows(const float (&a)[8]) {
  const float u0 = fold32(a[0], a[4]), u1 = fold32(a[1], a[5]), u2 = fold32(a[2], a[6]), u3 = fold32(a[3], a[7]);
  return f32x2{fold16(u0, u2), fold16(u1, u3)};
}
struct QLoad { u32x4 w[4]; u32x2 x[NB]; };
__device__ __forceinline__ void q_issue(QLoad& Q, const u16* x, const u16* wq, int row0, int col0, int lane) {
  const rsrc_t rx = mk_rsrc(x, NB * DM * 2), rw = mk_rsrc(wq, DM * 4096 * 2);
#pragma unroll
  for (int i = 0; i < 4; ++i) Q.w[i] = bld<AUX_NT>(rw, (lane >> 3) * 4 * 8192 + (lane & 7) * 16, (row0 + i) * 8192 + col0 * 2);
#pragma unroll
  for (int bb = 0; bb < NB; ++bb) Q.x[bb] = bld2<AUX_NT>(rx, (lane >> 3) * 8, (bb * DM + row0) * 2);
  __builtin_amdgcn_sched_barrier(0);
}
__device__ __forceinline__ void q_partial(const QLoad& Q, LAS float* part, int hw, int lane) {
  float a[NB][8];
#pragma unroll
  for (int j = 0; j < 4; ++j) {
    const unsigned p0 = plo(Q.w[1][j], Q.w[0][j]), p1 = phi(Q.w[1][j], Q.w[0][j]), p2 = plo(Q.w[3][j], Q.w[2][j]), p3 = phi(Q.w[3][j], Q.w[2][j]);
#pragma unroll
    for (int bb = 0; bb < NB; ++bb) { a[bb][2 * j] = dot2(p2, Q.x[bb][1], dot2(p0, Q.x[bb][0], 0.f)); a[bb][2 * j + 1] = dot2(p3, Q.x[bb][1], dot2(p1, Q.x[bb][0], 0.f)); }
  }
  float f[8];
#pragma unroll
  for (int j = 0; j < 8; ++j) f[j] = fold16(fold32(a[0][j], a[2][j]), fold32(a[1][j], a[3][j]));
#pragma unroll
  for (int j = 0; j < 4; ++j) f[j] = foldd<XOR8>(f[j], f[j + 4], lane & 8);
  *(LAS f32x4*)(part + hw * 256 + (lane >> 4) * 64 + (lane & 7) * 8 + ((lane >> 3) & 1) * 4) = f32x4{f[0], f[1], f[2], f[3]};
}
__device__ __forceinline__ void q_publish(LAS float* part, rsrc_t rws, int br, int band, int cg, unsigned tagA, unsigned tagB, int lane) {
  f32x4 s = *(LAS f32x4*)(part + lane * 4);
#pragma unroll
  for (int w = 1; w < 4; ++w) s += *(LAS f32x4*)(part + w * 256 + lane * 4);
  bst(__builtin_bit_cast(u32x4, s), rws, (lane >> 4) * 65536 + (lane & 15) * 16, WS_QP + br * 262144 + band * 16384 + cg * 256);
  DRAIN();
  if (lane < 4) bst(u32x4{0u, tagA, 0u, tagB}, rws, lane * 16, WS_QF + br * 16384 + (cg * 4 + band) * 64);
}
__device__ __forceinline__ unsigned q_gather(rsrc_t rws, int br, int b, int hq, unsigned tagA, unsigned tagB, int lane) {
  u32x4 f[1];
  poll<1>(f, rws, (lane & 7) * 64, 0, WS_QF + br * 16384 + hq * 512, tagA, tagB);
  f32x2 s = {0.f, 0.f};
  u32x2 p[4];
#pragma unroll
  for (int i = 0; i < 4; ++i) p[i] = bld2<AUX_SC1>(rws, lane * 8 + i * 16384, WS_QP + br * 262144 + b * 65536 + hq * 512);
#pragma unroll
  for (int i = 0; i < 4; ++i) s += __builtin_bit_cast(f32x2, p[i]);
  return cvtpk(s[0], s[1]);
}

__global__ void __launch_bounds__(512, 2) k_fused(const u16* __restrict__ x, const u16* __restrict__ ka, const u16* __restrict__ va, const u16* __restrict__ wqa, const u16* __restrict__ woa,
                                                  const u16* __restrict__ kb, const u16* __restrict__ vb, const u16* __restrict__ wqb, const u16* __restrict__ wob,
                                                  const u16* __restrict__ gw, u16* __restrict__ out, unsigned char* __restrict__ ws) {
  __shared__ __attribute__((aligned(1024))) unsigned char smem[L_BYTES];
  LAS unsigned char* L = (LAS unsigned char*)smem;
  LAS unsigned* misc = (LAS unsigned*)(L + L_MISC);
  LAS float* miscf = (LAS float*)(L + L_MISC);
  const unsigned lds0 = (unsigned)(size_t)L;
  const int tid = threadIdx.x, lane = tid & 63, wave = __builtin_amdgcn_readfirstlane(tid >> 6), r = lane >> 4, c = lane & 15;
  const int bid = blockIdx.x, xg = bid & 7, idx = bid >> 3, b = idx & 3, sp = idx >> 2;
  const int side = sp & 1, h = xg * 4 + (idx >> 3), jp = h * 2 + side;
  asm volatile("" :: "s"(x), "s"(ka), "s"(wqa), "s"(kb), "s"(vb), "s"(ws));
  if (wave == 0) STAMP_AT(0);

  if (wave >= 4) {
    __builtin_amdgcn_s_setprio(1);
    const int sw = wave - 4, voff = lane * 16;
    if (tid == 256) { *(LAS u32x4*)(misc + 4) = u32x4{0u, 0u, 0u, 0u}; *(LAS u32x2*)(misc + 2) = u32x2{4u, 4u}; misc[M_ANCH] = (unsigned)__builtin_amdgcn_s_memrealtime() + TK0; }
    LDS_BARRIER();
    {
      const size_t o = ((size_t)(b * 8 + xg) * SK + (SK - WIN) + sp * 256 + sw * 16) * DH;
      const GAS unsigned char* pk = (const GAS unsigned char*)(kb + o) + voff;
      const GAS unsigned char* pv = (const GAS unsigned char*)(vb + o) + voff;
#pragma unroll 4
      for (int i = 0; i < 16; ++i) { const int o2 = (i >> 2) * 16384 + (i & 3) * 1024; CAPWAIT(CAPB); glds16(pk + o2, lds0 + L_BIG + sw * 4096 + o2); }
#pragma unroll 4
      for (int i = 0; i < 16; ++i) { const int o2 = (i >> 2) * 16384 + (i & 3) * 1024; CAPWAIT(CAPB); glds16(pv + o2, lds0 + L_BIG + 65536 + sw * 4096 + o2); }
    }
    const size_t kvoff = (size_t)(b * NH + h) * SK * DH;
    const u16* kp = ka + kvoff; const u16* vp = va + kvoff;
#define UOFF(u) (((((u) >> UPB_LOG) * 2 + side) << (UPB_LOG + 13)) + (((u) & ((1 << UPB_LOG) - 1)) << 13))
    u32x4 d[8];
    {
      const rsrc_t rk = mk_rsrc(kp, SK * 256);
#pragma unroll
      for (int t = 0; t < 8; ++t) { CAPWAIT(CAPB); d[t] = bld<AUX_NT>(rk, voff + (t & 3) * 1024, UOFF(sw) + (t >> 2) * 4096); }
      __builtin_amdgcn_sched_barrier(0);
    }
    static_assert(CAPB <= 8, "the 8 loads above must push the LDS-DMA out of the window");
    lds_signal(misc + M_BDMA, lane);
    if (sw == 0) STAMP_AT(15);
    lds_wait_ge(misc + M_QRDY, 1);
    u32x4 q = *(LAS u32x4*)(L + L_QA + c * 16);
    asm volatile("" : "+v"(q));
    if (sw == 0) STAMP_AT(1);
    float mx = -INFINITY;
    int cur = sw, nxt = claim(misc + M_KCLAIM, lane);
    u64 now = __builtin_amdgcn_s_memrealtime();
    unsigned anch = __hip_atomic_load(misc + M_ANCH, RLX_WG);
    bool more;
    do {
      const int u = __builtin_amdgcn_readfirstlane(nxt);
      nxt = claim(misc + M_KCLAIM, lane);
      more = (unsigned)u < (unsigned)NU;
      PROG(u, 0);
      asm volatile("" : "+s"(now));
      const int lvl = win_level((unsigned)now, __builtin_amdgcn_readfirstlane(anch), u, sw);
      now = __builtin_amdgcn_s_memrealtime();
      anch = __hip_atomic_load(misc + M_ANCH, RLX_WG);
      const rsrc_t rn = mk_rsrc(more ? kp : vp, SK * 256);
      const int on = UOFF(more ? u : sw);
      float s[8];
#pragma unroll
      for (int t = 0; t < 8; ++t) {
        s[t] = dot2(d[t][3], q[3], dot2(d[t][2], q[2], dot2(d[t][1], q[1], dot2(d[t][0], q[0], 0.f))));
        WINWAIT(lvl); d[t] = bld<AUX_NT>(rn, voff + (t & 3) * 1024, on + (t >> 2) * 4096); __builtin_amdgcn_sched_barrier(0);
      }
#pragma unroll
      for (int i = 0; i < 4; ++i) s[i] = foldd<XOR7>(s[i], s[i + 4], lane & 4);
#pragma unroll
      for (int i = 0; i < 2; ++i) s[i] = foldd<XOR2>(s[i], s[i + 2], lane & 2);
      s[0] = foldd<XOR1>(s[0], s[1], lane & 1);
      s[0] += dpp<XOR8>(s[0]);
      const unsigned sb = cvtpk(rbf(s[0]) * SCALE, 0.f);
      mx = fmaxf(mx, bflo(sb));
      *(LAS u16*)(L + L_SC + cur * 64 + r * 16 + (c & 7) * 2) = (u16)sb;
      cur = u;
    } while (more);
    mx = xrow_max(row16_max(mx));
    if (lane == 0) miscf[M_WMAX + sw] = mx;
    lds_signal(misc + M_KDONE, lane);
    if (sw == 0) STAMP_AT(2);
    float acc[8];
#pragma unroll
    for (int j = 0; j < 8; ++j) acc[j] = 0.f;
    asm volatile("" :: "v"(nxt));
    nxt = claim(misc + M_VCLAIM, lane);
    u32x4 sc = *(LAS u32x4*)(L + L_SC + sw * 64 + r * 16);
    float mL = -mx * L2E;
#define V_STEP() \
      const int u = __builtin_amdgcn_readfirstlane(nxt); \
      nxt = claim(misc + M_VCLAIM, lane); \
      more = (unsigned)u < (unsigned)NU; \
      PROG(u, 9); \
      asm volatile("" : "+s"(now)); \
      const int lvl = win_level((unsigned)now, __builtin_amdgcn_readfirstlane(anch), NU + u, sw); \
      now = __builtin_amdgcn_s_memrealtime(); \
      anch = __hip_atomic_load(misc + M_ANCH, RLX_WG); \
      const rsrc_t rn = mk_rsrc(vp, more ? SK * 256 : 0);                   \
      const int on = more ? UOFF(u) : 0; \
      _Pragma("unroll") for (int t = 0; t < 8; t += 2) { \
        const unsigned w = cvtpk(ex2(fmaf(bflo(sc[t >> 1]), L2E, mL)), ex2(fmaf(bfhi(sc[t >> 1]), L2E, mL))); \
        _Pragma("unroll") for (int j = 0; j < 4; ++j) { acc[2 * j] = dot2(d[t][j], w & 0xffffu, acc[2 * j]); acc[2 * j + 1] = dot2(d[t][j], w << 16, acc[2 * j + 1]); } \
        WINWAIT(lvl); d[t] = bld<AUX_NT>(rn, voff + (t & 3) * 1024, on + (t >> 2) * 4096); __builtin_amdgcn_sched_barrier(0); \
        _Pragma("unroll") for (int j = 0; j < 4; ++j) { acc[2 * j] = dot2(d[t + 1][j], w >> 16, acc[2 * j]); acc[2 * j + 1] = dot2(d[t + 1][j], w & 0xffff0000u, acc[2 * j + 1]); } \
        WINWAIT(lvl); d[t + 1] = bld<AUX_NT>(rn, voff + ((t + 1) & 3) * 1024, on + ((t + 1) >> 2) * 4096); __builtin_amdgcn_sched_barrier(0); \
      }
    {
      V_STEP()
      lds_wait_ge(misc + M_KDONE, 4);
      const f32x4 wm = *(LAS f32x4*)(miscf + M_WMAX);
      const float M = fmaxf(fmaxf(wm[0], wm[1]), fmaxf(wm[2], wm[3])), al = ex2((mx - M) * L2E);
#pragma unroll
      for (int j = 0; j < 8; ++j) acc[j] *= al;
      mL = -M * L2E;
      cur = u;
    }
    u32x4 scn = *(LAS u32x4*)(L + L_SC + (cur & (NU - 1)) * 64 + r * 16);
    while (more) {
      sc = scn;
      asm volatile("" : "+v"(sc));
      scn = *(LAS u32x4*)(L + L_SC + (__builtin_amdgcn_readfirstlane(nxt) & (NU - 1)) * 64 + r * 16);
      V_STEP()
    }
    asm volatile("" :: "v"(nxt), "s"(now), "v"(scn), "v"(anch));
    STAMP_AT(8 + sw);
    *(LAS f32x2*)(L + L_REC + sw * 512 + c * 32 + r * 8) = fold_rows(acc);
    LDS_BARRIER();
    return;
  }

  __builtin_amdgcn_s_setprio(2);
  const int hw = wave;
  const rsrc_t rws = mk_rsrc(ws, WS_BYTES);
  const u64 t0 = __builtin_amdgcn_s_memrealtime();
  LAS float* tmp = (LAS float*)(L + L_TMP);
  float gate = 0.f;
  u64 epoch = 0;
  const int band = xg >> 1, cg = (xg & 1) * 32 + idx;
  {
    QLoad Q;
    q_issue(Q, x, wqa, band * 128 + hw * 32, cg * 64, lane);
    if (hw == 0) epoch = __hip_atomic_load((GAS u64*)(ws + WS_EPOCH), RLX_AGENT);
    LDS_BARRIER();
    asm volatile("" : "+v"(Q.w[0]));
    __builtin_amdgcn_sched_barrier(0);
    q_partial(Q, tmp, hw, lane);
    asm volatile("" : "+v"(epoch));
    epoch += 1;
    if (tid == 0) { misc[M_TAGA] = (unsigned)epoch; misc[M_TAGB] = (unsigned)(epoch >> 32) ^ ((unsigned)epoch * 0x9E3779B1u); }
  }
  hsync(misc, 1, lane);
  const unsigned tagA = misc[M_TAGA], tagB = misc[M_TAGB];
  if (hw == 0) {
    q_publish(tmp, rws, 0, band, cg, tagA, tagB, lane);
    STAMP_AT(16);
    __builtin_amdgcn_s_sleep(QPOLL_DELAY);
    *(LAS unsigned*)(L + L_QA + lane * 4) = q_gather(rws, 0, b, h, tagA, tagB, lane);
    lds_signal(misc + M_QRDY, lane);
    STAMP_AT(17);
    const u32x4 xv = __builtin_nontemporal_load((const u32x4*)(x + b * DM + lane * 8)), gv = __builtin_nontemporal_load((const u32x4*)(gw + lane * 8));
    float s = 0.f;
#pragma unroll
    for (int j = 0; j < 4; ++j) { const unsigned p = cvtpk(bflo(xv[j]) * bflo(gv[j]), bfhi(xv[j]) * bfhi(gv[j])); s += bflo(p); s += bfhi(p); }
    s = rbf(wave_sum(s));
    gate = rbf(1.f / (1.f + __expf(-s)));
  }
  unsigned zpk = 0;
  {
    wait_time(t0, T_QB);
    QLoad Q;
    q_issue(Q, x, wqb, band * 128 + hw * 32, cg * 64, lane);
    q_partial(Q, tmp + 1024, hw, lane);
    hsync(misc, 2, lane);
    if (hw == 1) q_publish(tmp + 1024, rws, 1, band, cg, tagA, tagB, lane);
    wait_time(t0, T_QBG);
    if (hw == 3) gossip(0, misc, rws, bid, tagA, lane);
    *(LAS unsigned*)(L + L_QB + hw * 256 + lane * 4) = q_gather(rws, 1, b, xg * 4 + hw, tagA, tagB, lane);
    lds_wait_ge(misc + M_BDMA, 4);
    if (hw == 0) STAMP_AT(3);
  }
  __builtin_amdgcn_s_setprio(0);
  hsync(misc, 3, lane);
  {
    u32x4 q[4];
#pragma unroll
    for (int g = 0; g < 4; ++g) q[g] = *(LAS u32x4*)(L + L_QB + g * 256 + c * 16);
    float m[4], l[4], acc[4][8];
#pragma unroll
    for (int g = 0; g < 4; ++g) { m[g] = -INFINITY; l[g] = 0.f;
#pragma unroll
      for (int j = 0; j < 8; ++j) acc[g][j] = 0.f; }
#pragma nounroll
    for (int s = 0; s < 4; ++s) {
      u32x4 k[4], v[4];
#pragma unroll
      for (int t = 0; t < 4; ++t) { k[t] = *(LAS u32x4*)(L + L_BIG + (hw * 16 + s * 4 + t) * 1024 + lane * 16); v[t] = *(LAS u32x4*)(L + L_BIG + 65536 + (hw * 16 + s * 4 + t) * 1024 + lane * 16); }
      gqa_step(k, v, q, m, l, acc);
    }
#pragma unroll
    for (int g = 0; g < 4; ++g) {
      const float M = xrow_max(m[g]), w = ex2((m[g] - M) * L2E), Ls = xrow_sum(l[g] * w);
#pragma unroll
      for (int j = 0; j < 8; ++j) acc[g][j] *= w;
      LAS float* rec = tmp + (g * 4 + hw) * REC_F;
      *(LAS f32x2*)(rec + 4 + c * 8 + r * 2) = fold_rows(acc[g]);
      if (lane == 0) *(LAS f32x2*)rec = f32x2{M, Ls};
    }
  }
  hsync(misc, 4, lane);
  {
    LAS float* rec = tmp + hw * 4 * REC_F;
    f32x2 ml[4], av[4];
#pragma unroll
    for (int i = 0; i < 4; ++i) { ml[i] = *(LAS f32x2*)(rec + i * REC_F); av[i] = *(LAS f32x2*)(rec + i * REC_F + 4 + lane * 2); }
    const float M = fmaxf(fmaxf(ml[0][0], ml[1][0]), fmaxf(ml[2][0], ml[3][0]));
    float Ls = 0.f, o0 = 0.f, o1 = 0.f;
#pragma unroll
    for (int i = 0; i < 4; ++i) { const float wt = ex2((ml[i][0] - M) * L2E); Ls = fmaf(wt, ml[i][1], Ls); o0 = fmaf(wt, av[i][0], o0); o1 = fmaf(wt, av[i][1], o1); }
    *(LAS unsigned*)(L + L_PUB + hw * 256 + lane * 4) = cvtpk(o0 / Ls, o1 / Ls);
    if (lane == 0) *(LAS f32x2*)(L + L_PUB + 1024 + hw * 8) = f32x2{M, Ls};
  }
  __builtin_amdgcn_s_setprio(2);
  hsync(misc, 5, lane);
  if (hw == 0) {
    const int pi = (b * 8 + xg) * 8 + sp;
    bst(*(LAS u32x4*)(L + L_PUB + lane * 16), rws, lane * 16, WS_PBP + pi * 1024);
    const u32x2 ml = *(LAS u32x2*)(L + L_PUB + 1024 + (lane & 3) * 8);
    DRAIN();
    if (lane < 4) bst(u32x4{ml[0], tagA, ml[1], tagB}, rws, lane * 16, WS_PBM + pi * 64);
    STAMP_AT(4);
  }
  if (hw == 3) gossip(1, misc, rws, bid, tagA, lane);
  wait_time(t0, T_WO);
  {
    const rsrc_t rw = mk_rsrc(wob, 4096 * DM * 2);
    u32x4 w[16];
#pragma unroll
    for (int i = 0; i < 16; ++i) { CAPWAIT(WO_CAP + 1); w[i] = bld<AUX_NT>(rw, lane * 16, (h * 128 + side * 64 + hw * 16 + i) * 1024); }
    __builtin_amdgcn_sched_barrier(0);
    u32x4 ml[1];
    poll<1>(ml, rws, (lane >> 3) * 64, 0, WS_PBM + (b * 8 + xg) * 512 + (h & 3) * 16, tagA, tagB);
    const unsigned av = __builtin_amdgcn_raw_buffer_load_b32(rws, (lane >> 3) * 1024 + (lane & 7) * 4, WS_PBP + (b * 8 + xg) * 8192 + (h & 3) * 256 + side * 128 + hw * 32, AUX_SC1);
    const float pm = __uint_as_float(ml[0][0]), M = xrow_max(fmaxf(pm, dpp<XOR8>(pm)));
    float Ls = ex2((pm - M) * L2E) * __uint_as_float(ml[0][2]), o0 = Ls * bflo(av), o1 = Ls * bfhi(av);
    Ls = xrow_sum(Ls + dpp<XOR8>(Ls)); o0 = xrow_sum(o0 + dpp<XOR8>(o0)); o1 = xrow_sum(o1 + dpp<XOR8>(o1));
    const unsigned ob = cvtpk(o0 / Ls, o1 / Ls);
    float z[8];
#pragma unroll
    for (int j = 0; j < 8; ++j) z[j] = 0.f;
#pragma unroll
    for (int i = 0; i < 16; i += 2) {
      const unsigned op = __builtin_amdgcn_readlane(ob, i >> 1);
#pragma unroll
      for (int j = 0; j < 4; ++j) { z[2 * j] = dot2(plo(w[i + 1][j], w[i][j]), op, z[2 * j]); z[2 * j + 1] = dot2(phi(w[i + 1][j], w[i][j]), op, z[2 * j + 1]); }
    }
    *(LAS f32x4*)(tmp + hw * 512 + lane * 8) = f32x4{z[0], z[1], z[2], z[3]};
    *(LAS f32x4*)(tmp + hw * 512 + lane * 8 + 4) = f32x4{z[4], z[5], z[6], z[7]};
    hsync(misc, 6, lane);
    f32x2 zs = {0.f, 0.f};
#pragma unroll
    for (int i = 0; i < 4; ++i) zs += *(LAS f32x2*)(tmp + i * 512 + tid * 2);
    zpk = cvtpk(zs[0], zs[1]);
  }
  if (hw == 0) STAMP_AT(5);
  if (hw == 3) gossip(2, misc, rws, bid, tagA, lane);
  constexpr int HS = 6;
  unsigned wa[16][8];
  {
    const rsrc_t rw = mk_rsrc(woa, 4096 * DM * 2);
    u32x4 w[32];
#pragma unroll
    for (int i = 0; i < 32; ++i) { CAPWAIT(WO_CAP + 1); w[i] = bld<AUX_NT>(rw, lane * 16, (h * 128 + hw * 32 + i) * 1024); }
    __builtin_amdgcn_sched_barrier(0);
#pragma unroll
    for (int i = 0; i < 16; ++i)
#pragma unroll
      for (int j = 0; j < 4; ++j) { wa[i][2 * j] = plo(w[2 * i + 1][j], w[2 * i][j]); wa[i][2 * j + 1] = phi(w[2 * i + 1][j], w[2 * i][j]); asm volatile("" : "+v"(wa[i][2 * j]), "+v"(wa[i][2 * j + 1])); }
  }
  if (hw == 0) STAMP_AT(7);
  if (hw == 3) gossip(3, misc, rws, bid, tagA, lane);
  const u64 t3 = __builtin_amdgcn_s_memrealtime();
  {
    lds_wait_ge(misc + M_KDONE, 4);
    const f32x4 wm = *(LAS f32x4*)(miscf + M_WMAX);
    const float M = fmaxf(fmaxf(wm[0], wm[1]), fmaxf(wm[2], wm[3])), mL = -M * L2E;
    float ls = 0.f;
#pragma unroll
    for (int i = 0; i < 2; ++i) {
      const u32x4 sc = *(LAS u32x4*)(L + L_SC + hw * 2048 + i * 1024 + lane * 16);
#pragma unroll
      for (int j = 0; j < 4; ++j) ls += ex2(fmaf(bflo(sc[j]), L2E, mL)) + ex2(fmaf(bfhi(sc[j]), L2E, mL));
    }
    ls = wave_sum(ls);
    if (lane == 0) miscf[M_LSUM + hw] = ls;
    hsync(misc, HS + 1, lane);
    if (hw == 0) {
      const f32x4 lw = *(LAS f32x4*)(miscf + M_LSUM);
      const float Ls = (lw[0] + lw[1]) + (lw[2] + lw[3]);
      if (lane < 4) bst(u32x4{__float_as_uint(M), tagA, __float_as_uint(Ls), tagB}, rws, lane * 16, WS_ST + (b * 64 + jp) * 64);
      wait_time(__builtin_amdgcn_s_memrealtime(), T_ST);
      u32x4 o[1];
      poll<1>(o, rws, 0, 0, WS_ST + (b * 64 + (jp ^ 1)) * 64, tagA, tagB);
      const float Mo = __uint_as_float(o[0][0]), MM = fmaxf(M, Mo), wt = ex2((M - MM) * L2E);
      if (lane == 0) miscf[M_CNORM] = wt / (wt * Ls + ex2((Mo - MM) * L2E) * __uint_as_float(o[0][2]));
      if (bid == 0) {
        u32x4 all[4];
        poll<4>(all, rws, lane * 64, 4096, WS_ST, tagA, tagB);
        if (lane == 0) __hip_atomic_store((GAS u64*)(ws + WS_EPOCH), epoch, RLX_AGENT);
      }
      STAMP_AT(6);
    } else if (hw == 3) {
#pragma nounroll
      for (int k = 4; k < NCP && (unsigned)__builtin_amdgcn_readfirstlane(__hip_atomic_load(misc + M_VCLAIM, RLX_WG)) < NU - 16 - T_GOSSIP / 28; ++k) {
        wait_time(t3, (k - 3) * T_GOSSIP);
        gossip(k, misc, rws, bid, tagA, lane);
      }
    }
  }
  LDS_BARRIER();
  if (hw == 1) STAMP_AT(12);
  {
    const f32x2 av = *(LAS f32x2*)(L + L_REC + r * 512 + hw * 128 + c * 8);
    const float cn = miscf[M_CNORM];
    const unsigned ob = cvtpk(xrow_sum(av[0]) * cn, xrow_sum(av[1]) * cn);
    float y[8];
#pragma unroll
    for (int j = 0; j < 8; ++j) y[j] = 0.f;
#pragma unroll
    for (int i = 0; i < 16; ++i) {
      const unsigned op = __builtin_amdgcn_readlane(ob, i);
#pragma unroll
      for (int j = 0; j < 8; ++j) y[j] = dot2(wa[i][j], op, y[j]);
    }
    *(LAS f32x4*)(tmp + hw * 512 + lane * 8) = f32x4{y[0], y[1], y[2], y[3]};
    *(LAS f32x4*)(tmp + hw * 512 + lane * 8 + 4) = f32x4{y[4], y[5], y[6], y[7]};
  }
  LDS_BARRIER();
  {
    f32x2 ys = {0.f, 0.f};
#pragma unroll
    for (int i = 0; i < 4; ++i) ys += *(LAS f32x2*)(tmp + i * 512 + tid * 2);
    const unsigned ypk = cvtpk(ys[0], ys[1]);
    bst(u32x4{plo(zpk, ypk), tagA, phi(zpk, ypk), tagB}, rws, (tid >> 2) * 4096 + (tid & 3) * 16, WS_YG + b * 64 * 4096 + jp * 64);
  }
  if (hw == 1) STAMP_AT(13);
  if (hw == 0) {
    u32x4 g[4];
    poll<4>(g, rws, lane * 64, 16, WS_YG + (b * 64 + jp) * 4096, tagA, tagB);
    float a[8], z[8];
#pragma unroll
    for (int j = 0; j < 8; ++j) { a[j] = bflo(g[j >> 1][(j & 1) * 2]); z[j] = bfhi(g[j >> 1][(j & 1) * 2]); }
    float u[8];
#pragma unroll
    for (int j = 0; j < 8; ++j) u[j] = fold32(a[j], z[j]);
#pragma unroll
    for (int j = 0; j < 4; ++j) u[j] = fold16(u[j], u[j + 4]);
#pragma unroll
    for (int j = 0; j < 2; ++j) u[j] = foldd<XOR8>(u[j], u[j + 2], lane & 8);
    u[0] = foldd<XOR7>(u[0], u[1], lane & 4);
    u[0] += dpp<XOR2>(u[0]); u[0] += dpp<XOR1>(u[0]);
    const float t = rbf(rbf(u[0]) * (lane < 32 ? gate : rbf(1.f - gate)));
    const unsigned res = cvtpk(fold32(t, t), 0.f);
    if (lane < 32 && (lane & 3) == 0) out[b * DM + jp * 8 + (lane >> 2)] = (u16)res;
    STAMP_AT(14);
  }
}

extern "C" void kernel_launch(void* const* d_in, const int* in_sizes, int n_in, void* d_out, int out_size, void* d_ws, size_t ws_size, hipStream_t stream) {
  k_fused<<<256, 512, 0, stream>>>((const u16*)d_in[0], (const u16*)d_in[1], (const u16*)d_in[2], (const u16*)d_in[3], (const u16*)d_in[4], (const u16*)d_in[6], (const u16*)d_in[7],
                                   (const u16*)d_in[8], (const u16*)d_in[9], (const u16*)d_in[10], (u16*)d_out, (unsigned char*)d_ws);
}
```

```cpp
#include <hip/hip_runtime.h>
#include <hip/hip_bf16.h>
#include <cstdint>

typedef unsigned u32x4 __attribute__((ext_vector_type(4)));
typedef unsigned u32x2 __attribute__((ext_vector_type(2)));
typedef float f32x4 __attribute__((ext_vector_type(4)));
typedef float f32x2 __attribute__((ext_vector_type(2)));
typedef __attribute__((ext_vector_type(2))) __bf16 bf16x2;
typedef unsigned long long u64;
typedef unsigned short u16;
#define GAS __attribute__((address_space(1)))
#define LAS __attribute__((address_space(3)))
#define RLX_AGENT __ATOMIC_RELAXED, __HIP_MEMORY_SCOPE_AGENT
#define RLX_WG __ATOMIC_RELAXED, __HIP_MEMORY_SCOPE_WORKGROUP
#define DRAIN() asm volatile("s_waitcnt vmcnt(0)" ::: "memory")
#define LDS_BARRIER() do { asm volatile("s_waitcnt lgkmcnt(0)" ::: "memory"); __builtin_amdgcn_s_barrier(); asm volatile("" ::: "memory"); } while (0)
typedef __amdgpu_buffer_rsrc_t rsrc_t;

#define CAPV 5
#define CAPB 5
#define UPB_LOG 2
#define WO_CAP 1
#define QPOLL_DELAY 16
#define T_QB 0
#define T_QBG 1200
#define T_WO 2400
#define T_ST 500

constexpr int NB = 4, DM = 512, NH = 32, SK = 8192, DH = 128, WIN = 2048;
constexpr float SCALE = 0.088388347648318440f, L2E = 1.4426950408889634f;
constexpr int AUX_NT = 2, AUX_SC1 = 16, RSRC_W3 = 0x00020000;
constexpr int NU = 128;
constexpr unsigned SPIN_CAP = 1u << 15;

constexpr int WS_EPOCH = 0;
constexpr int WS_PH = 4096, NCP = 14;
constexpr int WS_QF = 32768;
constexpr int WS_QP = WS_QF + 32768;
constexpr int WS_PBM = WS_QP + 524288;
constexpr int WS_ST = WS_PBM + 16384;
constexpr int WS_PBP = WS_ST + 16384;
constexpr int WS_YG = WS_PBP + 262144;
constexpr int WS_STAMP = WS_YG + 256 * 4096;
constexpr int WS_BYTES = WS_STAMP + 2 * 256 * 256;

constexpr int L_MISC = 0, L_QA = 256, L_QB = 512, L_REC = 1536, L_PROG = 3584, L_SC = 4096, L_TMP = 12288, L_PUB = L_TMP + 8704, L_BIG = 22528, L_BYTES = L_BIG + 131072;
constexpr int REC_F = 132;
enum { M_TAGA = 0, M_TAGB = 1, M_KCLAIM = 2, M_VCLAIM = 3, M_QRDY = 4, M_HSYNC = 5, M_KDONE = 6, M_BDMA = 7, M_WMAX = 8, M_LSUM = 12, M_CNORM = 16, M_ANCH = 17 };

__device__ __forceinline__ float dot2(unsigned a, unsigned b, float c) {
  return __builtin_amdgcn_fdot2_f32_bf16(__builtin_bit_cast(bf16x2, a), __builtin_bit_cast(bf16x2, b), c, false);
}
__device__ __forceinline__ unsigned cvtpk(float lo, float hi) { return __builtin_bit_cast(unsigned, __builtin_convertvector(f32x2{lo, hi}, bf16x2)); }
__device__ __forceinline__ float bflo(unsigned u) { return __uint_as_float(u << 16); }
__device__ __forceinline__ float bfhi(unsigned u) { return __uint_as_float(u & 0xffff0000u); }
__device__ __forceinline__ float rbf(float x) { return bflo(cvtpk(x, 0.f)); }
__device__ __forceinline__ float ex2(float x) { return __builtin_amdgcn_exp2f(x); }
template <int CTRL> __device__ __forceinline__ float dpp(float x) {
  return __builtin_bit_cast(float, __builtin_amdgcn_mov_dpp(__builtin_bit_cast(int, x), CTRL, 0xf, 0xf, true));
}
constexpr int XOR1 = 0xB1, XOR2 = 0x4E, XOR7 = 0x141, XOR8 = 0x128;
__device__ __forceinline__ float row16_sum(float x) { x += dpp<XOR1>(x); x += dpp<XOR2>(x); x += dpp<XOR7>(x); x += dpp<XOR8>(x); return x; }
__device__ __forceinline__ float row16_max(float x) { x = fmaxf(x, dpp<XOR1>(x)); x = fmaxf(x, dpp<XOR2>(x)); x = fmaxf(x, dpp<XOR7>(x)); return fmaxf(x, dpp<XOR8>(x)); }
template <int CTRL> __device__ __forceinline__ float foldd(float a, float b, bool hi) { return (hi ? b : a) + dpp<CTRL>(hi ? a : b); }
__device__ __forceinline__ float fold32(float x, float y) {
  auto t = __builtin_amdgcn_permlane32_swap(__float_as_uint(x), __float_as_uint(y), false, false);
  return __uint_as_float(t[0]) + __uint_as_float(t[1]);
}
__device__ __forceinline__ float fold16(float x, float y) {
  auto t = __builtin_amdgcn_permlane16_swap(__float_as_uint(x), __float_as_uint(y), false, false);
  return __uint_as_float(t[0]) + __uint_as_float(t[1]);
}
__device__ __forceinline__ float xrow_sum(float x) { x = fold16(x, x); return fold32(x, x); }
__device__ __forceinline__ float xrow_max(float x) {
  auto s = __builtin_amdgcn_permlane16_swap(__float_as_uint(x), __float_as_uint(x), false, false);
  x = fmaxf(__uint_as_float(s[0]), __uint_as_float(s[1]));
  auto t = __builtin_amdgcn_permlane32_swap(__float_as_uint(x), __float_as_uint(x), false, false);
  return fmaxf(__uint_as_float(t[0]), __uint_as_float(t[1]));
}
__device__ __forceinline__ float wave_sum(float x) { return xrow_sum(row16_sum(x)); }
__device__ __forceinline__ rsrc_t mk_rsrc(const void* p, int bytes) { return __builtin_amdgcn_make_buffer_rsrc(const_cast<void*>(p), 0, bytes, RSRC_W3); }
template <int AUX> __device__ __forceinline__ u32x4 bld(rsrc_t r, int voff, int soff) { return __builtin_amdgcn_raw_buffer_load_b128(r, voff, soff, AUX); }
template <int AUX> __device__ __forceinline__ u32x2 bld2(rsrc_t r, int voff, int soff) { return __builtin_amdgcn_raw_buffer_load_b64(r, voff, soff, AUX); }
__device__ __forceinline__ void bst(u32x4 v, rsrc_t r, int voff, int soff) { __builtin_amdgcn_raw_buffer_store_b128(v, r, voff, soff, AUX_SC1); }
__device__ __forceinline__ unsigned plo(unsigned hi, unsigned lo) { return __builtin_amdgcn_perm(hi, lo, 0x05040100u); }
__device__ __forceinline__ unsigned phi(unsigned hi, unsigned lo) { return __builtin_amdgcn_perm(hi, lo, 0x07060302u); }
#define CAPWAIT(n) do { __builtin_amdgcn_sched_barrier(0); asm volatile("s_waitcnt vmcnt(%0)" :: "i"((n) - 1) : "memory"); } while (0)

#define ADAPT 1
#define TAU 28.0f
#define TK0 700
#define LSTEP 40
#define CENTER 1
#define FLOORW 2
#define T_GOSSIP 600
constexpr int TAU64 = (int)(TAU * 64);
#define WINWAIT(lvl) do { __builtin_amdgcn_sched_barrier(0); asm volatile("s_cmp_gt_i32 %0, 1\n\ts_cbranch_scc1 2f\n\ts_cmp_gt_i32 %0, 0\n\ts_cbranch_scc1 1f\n\ts_waitcnt vmcnt(3)\n1:\n\ts_waitcnt vmcnt(4)\n" \
    "2:\n\ts_cmp_gt_i32 %0, 2\n\ts_cbranch_scc1 3f\n\ts_waitcnt vmcnt(5)\n3:\n\ts_waitcnt vmcnt(6)" :: "s"(lvl) : "memory", "scc"); } while (0)
__device__ __forceinline__ int win_level(unsigned now, unsigned anchor, int n, int sw) {
  const int late = (int)(now - anchor) - ((n * TAU64) >> 6);
  const int lv = (late + (7 + CENTER - sw) * LSTEP + LSTEP / 2) / (4 * LSTEP);
  return sw < FLOORW ? max(lv, 1) : lv;
}
__device__ __forceinline__ void glds16(const GAS unsigned char* src, unsigned dst) {
  unsigned keep;
  asm volatile("s_mov_b32 %0, m0\n\ts_mov_b32 m0, %2\n\ts_nop 0\n\tglobal_load_lds_dwordx4 %1, off nt\n\ts_mov_b32 m0, %0" : "=&s"(keep) : "v"(src), "s"(dst) : "memory");
}

#define STAMP_AT(i) do {} while (0)
#define STAMP_LDS(i) do {} while (0)
#define PROG(u, base) do {} while (0)

__device__ __forceinline__ void wait_time(u64 t0, unsigned ticks) {
#pragma nounroll
  while (__builtin_amdgcn_s_memrealtime() - t0 < ticks) __builtin_amdgcn_s_sleep(4);
}
__device__ __forceinline__ void lds_wait_ge(LAS unsigned* p, unsigned target) {
#pragma nounroll
  for (unsigned spins = 0; spins < 32 * SPIN_CAP && (unsigned)__builtin_amdgcn_readfirstlane(__hip_atomic_load(p, RLX_WG)) < target; ++spins) __builtin_amdgcn_s_sleep(2);
  asm volatile("" ::: "memory");
}
__device__ __forceinline__ int claim(LAS unsigned* p, int lane) {
  unsigned r = 0;
  if (lane == 0) r = __builtin_amdgcn_atomic_inc32(p, 0xffffffffu, __ATOMIC_RELAXED, "workgroup");
  return (int)r;
}
__device__ __forceinline__ void lds_signal(LAS unsigned* p, int lane) {
  asm volatile("s_waitcnt lgkmcnt(0)" ::: "memory");
  if (lane == 0) __builtin_amdgcn_atomic_inc32(p, 0xffffffffu, __ATOMIC_RELAXED, "workgroup");
}
__device__ __forceinline__ void hsync(LAS unsigned* misc, unsigned n, int lane) { lds_signal(misc + M_HSYNC, lane); lds_wait_ge(misc + M_HSYNC, 4 * n); }
template <int N> __device__ __forceinline__ void poll(u32x4 (&g)[N], rsrc_t rws, int voff, int stride, int soff, unsigned tagA, unsigned tagB) {
#pragma nounroll
  for (unsigned spins = 0; spins < SPIN_CAP; ++spins) {
    asm volatile("" ::: "memory");
    bool ok = true;
#pragma unroll
    for (int i = 0; i < N; ++i) g[i] = bld<AUX_SC1>(rws, voff + i * stride, soff);
#pragma unroll
    for (int i = 0; i < N; ++i) ok &= (g[i][1] == tagA) & (g[i][3] == tagB);
    if (__all(ok)) break;
    __builtin_amdgcn_s_sleep(2);
  }
  __builtin_amdgcn_fence(__ATOMIC_ACQUIRE, "wavefront");
  asm volatile("" ::: "memory");
}

__device__ __forceinline__ void gossip(int k, LAS unsigned* misc, rsrc_t rws, int bid, unsigned tagA, int lane) {
  const unsigned kc = __hip_atomic_load(misc + M_KCLAIM, RLX_WG), vc = __hip_atomic_load(misc + M_VCLAIM, RLX_WG);
  const unsigned ph = (unsigned)__builtin_amdgcn_s_memrealtime() - (((kc < NU ? kc : NU + vc) * TAU64) >> 6);
  if (lane == 0) __builtin_amdgcn_raw_buffer_store_b64(u32x2{ph, tagA}, rws, 0, WS_PH + k * 2048 + bid * 8, AUX_SC1);
  if (k > 0) {
    asm volatile("" ::: "memory");
    const u32x4 g0 = bld<AUX_SC1>(rws, lane * 16, WS_PH + (k - 1) * 2048), g1 = bld<AUX_SC1>(rws, lane * 16, WS_PH + (k - 1) * 2048 + 1024);
    float d = 0.f, n = 0.f;
    if (g0[1] == tagA) { d += (float)(int)(g0[0] - ph); n += 1.f; }
    if (g0[3] == tagA) { d += (float)(int)(g0[2] - ph); n += 1.f; }
    if (g1[1] == tagA) { d += (float)(int)(g1[0] - ph); n += 1.f; }
    if (g1[3] == tagA) { d += (float)(int)(g1[2] - ph); n += 1.f; }
    d = wave_sum(d); n = wave_sum(n);
    if (n >= 128.f && lane == 0) __hip_atomic_store(misc + M_ANCH, ph + (int)(d / n), RLX_WG);
  }
}

__device__ __forceinline__ void gqa_step(const u32x4 (&k)[4], const u32x4 (&v)[4], const u32x4 (&q)[4], float (&m)[4], float (&l)[4], float (&acc)[4][8]) {
#pragma unroll
  for (int g = 0; g < 4; ++g) {
    float s[4];
#pragma unroll
    for (int t = 0; t < 4; ++t) { float d = dot2(k[t][0], q[g][0], 0.f); d = dot2(k[t][1], q[g][1], d); d = dot2(k[t][2], q[g][2], d); s[t] = dot2(k[t][3], q[g][3], d); }
#pragma unroll
    for (int t = 0; t < 4; t += 2) {
      const unsigned r1 = cvtpk(row16_sum(s[t]), row16_sum(s[t + 1]));
      const unsigned r2 = cvtpk(bflo(r1) * SCALE, bfhi(r1) * SCALE);
      s[t] = bflo(r2); s[t + 1] = bfhi(r2);
    }
    const float mn = fmaxf(fmaxf(m[g], fmaxf(s[0], s[1])), fmaxf(s[2], s[3]));
    const float al = ex2((m[g] - mn) * L2E), mL = -mn * L2E;
    m[g] = mn;
    float ls = l[g] * al;
#pragma unroll
    for (int j = 0; j < 8; ++j) acc[g][j] *= al;
#pragma unroll
    for (int t = 0; t < 4; t += 2) {
      const float p0 = ex2(fmaf(s[t], L2E, mL)), p1 = ex2(fmaf(s[t + 1], L2E, mL));
      ls += p0 + p1;
      const unsigned w = cvtpk(p0, p1);
#pragma unroll
      for (int j = 0; j < 4; ++j) {
        acc[g][2 * j] = dot2(v[t + 1][j], w >> 16, dot2(v[t][j], w & 0xffffu, acc[g][2 * j]));
        acc[g][2 * j + 1] = dot2(v[t + 1][j], w & 0xffff0000u, dot2(v[t][j], w << 16, acc[g][2 * j + 1]));
      }
    }
    l[g] = ls;
  }
}
__device__ __forceinline__ f32x2 fold_rows(const float (&a)[8]) {
  const float u0 = fold32(a[0], a[4]), u1 = fold32(a[1], a[5]), u2 = fold32(a[2], a[6]), u3 = fold32(a[3], a[7]);
  return f32x2{fold16(u0, u2), fold16(u1, u3)};
}
struct QLoad { u32x4 w[4]; u32x2 x[NB]; };
__device__ __forceinline__ void q_issue(QLoad& Q, const u16* x, const u16* wq, int row0, int col0, int lane) {
  const rsrc_t rx = mk_rsrc(x, NB * DM * 2), rw = mk_rsrc(wq, DM * 4096 * 2);
#pragma unroll
  for (int i = 0; i < 4; ++i) Q.w[i] = bld<AUX_NT>(rw, (lane >> 3) * 4 * 8192 + (lane & 7) * 16, (row0 + i) * 8192 + col0 * 2);
#pragma unroll
  for (int bb = 0; bb < NB; ++bb) Q.x[bb] = bld2<AUX_NT>(rx, (lane >> 3) * 8, (bb * DM + row0) * 2);
  __builtin_amdgcn_sched_barrier(0);
}
__device__ __forceinline__ void q_partial(const QLoad& Q, LAS float* part, int hw, int lane) {
  float a[NB][8];
#pragma unroll
  for (int j = 0; j < 4; ++j) {
    const unsigned p0 = plo(Q.w[1][j], Q.w[0][j]), p1 = phi(Q.w[1][j], Q.w[0][j]), p2 = plo(Q.w[3][j], Q.w[2][j]), p3 = phi(Q.w[3][j], Q.w[2][j]);
#pragma unroll
    for (int bb = 0; bb < NB; ++bb) { a[bb][2 * j] = dot2(p2, Q.x[bb][1], dot2(p0, Q.x[bb][0], 0.f)); a[bb][2 * j + 1] = dot2(p3, Q.x[bb][1], dot2(p1, Q.x[bb][0], 0.f)); }
  }
  float f[8];
#pragma unroll
  for (int j = 0; j < 8; ++j) f[j] = fold16(fold32(a[0][j], a[2][j]), fold32(a[1][j], a[3][j]));
#pragma unroll
  for (int j = 0; j < 4; ++j) f[j] = foldd<XOR8>(f[j], f[j + 4], lane & 8);
  *(LAS f32x4*)(part + hw * 256 + (lane >> 4) * 64 + (lane & 7) * 8 + ((lane >> 3) & 1) * 4) = f32x4{f[0], f[1], f[2], f[3]};
}
__device__ __forceinline__ void q_publish(LAS float* part, rsrc_t rws, int br, int band, int cg, unsigned tagA, unsigned tagB, int lane) {
  f32x4 s = *(LAS f32x4*)(part + lane * 4);
#pragma unroll
  for (int w = 1; w < 4; ++w) s += *(LAS f32x4*)(part + w * 256 + lane * 4);
  bst(__builtin_bit_cast(u32x4, s), rws, (lane >> 4) * 65536 + (lane & 15) * 16, WS_QP + br * 262144 + band * 16384 + cg * 256);
  DRAIN();
  if (lane < 4) bst(u32x4{0u, tagA, 0u, tagB}, rws, lane * 16, WS_QF + br * 16384 + (cg * 4 + band) * 64);
}
__device__ __forceinline__ unsigned q_gather(rsrc_t rws, int br, int b, int hq, unsigned tagA, unsigned tagB, int lane) {
  u32x4 f[1];
  poll<1>(f, rws, (lane & 7) * 64, 0, WS_QF + br * 16384 + hq * 512, tagA, tagB);
  f32x2 s = {0.f, 0.f};
  u32x2 p[4];
#pragma unroll
  for (int i = 0; i < 4; ++i) p[i] = bld2<AUX_SC1>(rws, lane * 8 + i * 16384, WS_QP + br * 262144 + b * 65536 + hq * 512);
#pragma unroll
  for (int i = 0; i < 4; ++i) s += __builtin_bit_cast(f32x2, p[i]);
  return cvtpk(s[0], s[1]);
}

__global__ void __launch_bounds__(512, 2) k_fused(const u16* __restrict__ x, const u16* __restrict__ ka, const u16* __restrict__ va, const u16* __restrict__ wqa, const u16* __restrict__ woa,
                                                  const u16* __restrict__ kb, const u16* __restrict__ vb, const u16* __restrict__ wqb, const u16* __restrict__ wob,
                                                  const u16* __restrict__ gw, u16* __restrict__ out, unsigned char* __restrict__ ws) {
  __shared__ __attribute__((aligned(1024))) unsigned char smem[L_BYTES];
  LAS unsigned char* L = (LAS unsigned char*)smem;
  LAS unsigned* misc = (LAS unsigned*)(L + L_MISC);
  LAS float* miscf = (LAS float*)(L + L_MISC);
  const unsigned lds0 = (unsigned)(size_t)L;
  const int tid = threadIdx.x, lane = tid & 63, wave = __builtin_amdgcn_readfirstlane(tid >> 6), r = lane >> 4, c = lane & 15;
  const int bid = blockIdx.x, xg = bid & 7, idx = bid >> 3, b = idx & 3, sp = idx >> 2;
  const int side = sp & 1, h = xg * 4 + (idx >> 3), jp = h * 2 + side;
  asm volatile("" :: "s"(x), "s"(ka), "s"(wqa), "s"(kb), "s"(vb), "s"(ws), "s"(va), "s"(woa), "s"(wob), "s"(gw), "s"(out), "s"(wqb));
  if (wave == 0) STAMP_AT(0);

  if (wave >= 4) {
    __builtin_amdgcn_s_setprio(1);
    const int sw = wave - 4, voff = lane * 16;
    if (tid == 256) { *(LAS u32x4*)(misc + 4) = u32x4{0u, 0u, 0u, 0u}; *(LAS u32x2*)(misc + 2) = u32x2{4u, 4u}; misc[M_ANCH] = (unsigned)__builtin_amdgcn_s_memrealtime() + TK0; }
    LDS_BARRIER();
    {
      const size_t o = ((size_t)(b * 8 + xg) * SK + (SK - WIN) + sp * 256 + sw * 16) * DH;
      const GAS unsigned char* pk = (const GAS unsigned char*)(kb + o) + voff;
      const GAS unsigned char* pv = (const GAS unsigned char*)(vb + o) + voff;
#pragma unroll 4
      for (int i = 0; i < 16; ++i) { const int o2 = (i >> 2) * 16384 + (i & 3) * 1024; CAPWAIT(CAPB); glds16(pk + o2, lds0 + L_BIG + sw * 4096 + o2); }
#pragma unroll 4
      for (int i = 0; i < 16; ++i) { const int o2 = (i >> 2) * 16384 + (i & 3) * 1024; CAPWAIT(CAPB); glds16(pv + o2, lds0 + L_BIG + 65536 + sw * 4096 + o2); }
    }
    const size_t kvoff = (size_t)(b * NH + h) * SK * DH;
    const u16* kp = ka + kvoff; const u16* vp = va + kvoff;
#define UOFF(u) (((((u) >> UPB_LOG) * 2 + side) << (UPB_LOG + 13)) + (((u) & ((1 << UPB_LOG) - 1)) << 13))
    u32x4 d[8];
    {
      const rsrc_t rk = mk_rsrc(kp, SK * 256);
#pragma unroll
      for (int t = 0; t < 8; ++t) { CAPWAIT(CAPB); d[t] = bld<AUX_NT>(rk, voff + (t & 3) * 1024, UOFF(sw) + (t >> 2) * 4096); }
      __builtin_amdgcn_sched_barrier(0);
    }
    static_assert(CAPB <= 8, "the 8 loads above must push the LDS-DMA out of the window");
    lds_signal(misc + M_BDMA, lane);
    if (sw == 0) STAMP_AT(15);
    lds_wait_ge(misc + M_QRDY, 1);
    u32x4 q = *(LAS u32x4*)(L + L_QA + c * 16);
    asm volatile("" : "+v"(q));
    if (sw == 0) STAMP_AT(1);
    float mx = -INFINITY;
    int cur = sw, nxt = claim(misc + M_KCLAIM, lane);
    u64 now = __builtin_amdgcn_s_memrealtime();
    unsigned anch = __hip_atomic_load(misc + M_ANCH, RLX_WG);
    bool more;
    do {
      const int u = __builtin_amdgcn_readfirstlane(nxt);
      nxt = claim(misc + M_KCLAIM, lane);
      more = (unsigned)u < (unsigned)NU;
      PROG(u, 0);
      asm volatile("" : "+s"(now));
      const int lvl = win_level((unsigned)now, __builtin_amdgcn_readfirstlane(anch), u, sw);
      now = __builtin_amdgcn_s_memrealtime();
      anch = __hip_atomic_load(misc + M_ANCH, RLX_WG);
      const rsrc_t rn = mk_rsrc(more ? kp : vp, SK * 256);
      const int on = UOFF(more ? u : sw);
      float s[8];
#pragma unroll
      for (int t = 0; t < 8; ++t) {
        s[t] = dot2(d[t][3], q[3], dot2(d[t][2], q[2], dot2(d[t][1], q[1], dot2(d[t][0], q[0], 0.f))));
        WINWAIT(lvl); d[t] = bld<AUX_NT>(rn, voff + (t & 3) * 1024, on + (t >> 2) * 4096); __builtin_amdgcn_sched_barrier(0);
      }
#pragma unroll
      for (int i = 0; i < 4; ++i) s[i] = foldd<XOR7>(s[i], s[i + 4], lane & 4);
#pragma unroll
      for (int i = 0; i < 2; ++i) s[i] = foldd<XOR2>(s[i], s[i + 2], lane & 2);
      s[0] = foldd<XOR1>(s[0], s[1], lane & 1);
      s[0] += dpp<XOR8>(s[0]);
      const unsigned sb = cvtpk(rbf(s[0]) * SCALE, 0.f);
      mx = fmaxf(mx, bflo(sb));
      *(LAS u16*)(L + L_SC + cur * 64 + r * 16 + (c & 7) * 2) = (u16)sb;
      cur = u;
    } while (more);
    mx = xrow_max(row16_max(mx));
    if (lane == 0) miscf[M_WMAX + sw] = mx;
    lds_signal(misc + M_KDONE, lane);
    if (sw == 0) STAMP_AT(2);
    float acc[8];
#pragma unroll
    for (int j = 0; j < 8; ++j) acc[j] = 0.f;
    asm volatile("" :: "v"(nxt));
    nxt = claim(misc + M_VCLAIM, lane);
    u32x4 sc = *(LAS u32x4*)(L + L_SC + sw * 64 + r * 16);
    float mL = -mx * L2E;
#define V_STEP() \
      const int u = __builtin_amdgcn_readfirstlane(nxt); \
      nxt = claim(misc + M_VCLAIM, lane); \
      more = (unsigned)u < (unsigned)NU; \
      PROG(u, 9); \
      asm volatile("" : "+s"(now)); \
      const int lvl = win_level((unsigned)now, __builtin_amdgcn_readfirstlane(anch), NU + u, sw); \
      now = __builtin_amdgcn_s_memrealtime(); \
      anch = __hip_atomic_load(misc + M_ANCH, RLX_WG); \
      const rsrc_t rn = mk_rsrc(vp, more ? SK * 256 : 0);                   \
      const int on = more ? UOFF(u) : 0; \
      _Pragma("unroll") for (int t = 0; t < 8; t += 2) { \
        const unsigned w = cvtpk(ex2(fmaf(bflo(sc[t >> 1]), L2E, mL)), ex2(fmaf(bfhi(sc[t >> 1]), L2E, mL))); \
        _Pragma("unroll") for (int j = 0; j < 4; ++j) { acc[2 * j] = dot2(d[t][j], w & 0xffffu, acc[2 * j]); acc[2 * j + 1] = dot2(d[t][j], w << 16, acc[2 * j + 1]); } \
        WINWAIT(lvl); d[t] = bld<AUX_NT>(rn, voff + (t & 3) * 1024, on + (t >> 2) * 4096); __builtin_amdgcn_sched_barrier(0); \
        _Pragma("unroll") for (int j = 0; j < 4; ++j) { acc[2 * j] = dot2(d[t + 1][j], w >> 16, acc[2 * j]); acc[2 * j + 1] = dot2(d[t + 1][j], w & 0xffff0000u, acc[2 * j + 1]); } \
        WINWAIT(lvl); d[t + 1] = bld<AUX_NT>(rn, voff + ((t + 1) & 3) * 1024, on + ((t + 1) >> 2) * 4096); __builtin_amdgcn_sched_barrier(0); \
      }
    {
      V_STEP()
      lds_wait_ge(misc + M_KDONE, 4);
      const f32x4 wm = *(LAS f32x4*)(miscf + M_WMAX);
      const float M = fmaxf(fmaxf(wm[0], wm[1]), fmaxf(wm[2], wm[3])), al = ex2((mx - M) * L2E);
#pragma unroll
      for (int j = 0; j < 8; ++j) acc[j] *= al;
      mL = -M * L2E;
      cur = u;
    }
    u32x4 scn = *(LAS u32x4*)(L + L_SC + (cur & (NU - 1)) * 64 + r * 16);
    while (more) {
      sc = scn;
      asm volatile("" : "+v"(sc));
      scn = *(LAS u32x4*)(L + L_SC + (__builtin_amdgcn_readfirstlane(nxt) & (NU - 1)) * 64 + r * 16);
      V_STEP()
    }
    asm volatile("" :: "v"(nxt), "s"(now), "v"(scn), "v"(anch));
    STAMP_AT(8 + sw);
    *(LAS f32x2*)(L + L_REC + sw * 512 + c * 32 + r * 8) = fold_rows(acc);
    LDS_BARRIER();
    return;
  }

  __builtin_amdgcn_s_setprio(2);
  const int hw = wave;
  const rsrc_t rws = mk_rsrc(ws, WS_BYTES);
  const u64 t0 = __builtin_amdgcn_s_memrealtime();
  LAS float* tmp = (LAS float*)(L + L_TMP);
  float gate = 0.f;
  u64 epoch = 0;
  const int band = xg >> 1, cg = (xg & 1) * 32 + idx;
  {
    QLoad Q;
    q_issue(Q, x, wqa, band * 128 + hw * 32, cg * 64, lane);
    if (hw == 0) epoch = __hip_atomic_load((GAS u64*)(ws + WS_EPOCH), RLX_AGENT);
    unsigned warm = 0;
    if (hw == 3) {
      const size_t kvb = ((size_t)(b * 8 + xg) * SK + (SK - WIN) + sp * 256) * DH, kva = (size_t)(b * NH + h) * SK * DH;
      const unsigned char* p = ws + lane * 65536;
      if (lane >= 32) p = (const unsigned char*)((lane & 1 ? ka : va) + kva) + ((lane - 32) >> 1) * 262144;
      if (lane == 48) p = (const unsigned char*)(vb + kvb);
      if (lane == 49) p = (const unsigned char*)(woa + (size_t)h * 128 * DM);
      if (lane == 50) p = (const unsigned char*)(wob + (size_t)(h * 128 + side * 64) * DM);
      if (lane == 51) p = (const unsigned char*)gw;
      if (lane == 52) p = (const unsigned char*)out;
      if (lane == 53) p = (const unsigned char*)(wqb + (size_t)band * 128 * 4096);
      if (lane == 54) p = (const unsigned char*)(woa + (size_t)h * 128 * DM) + 65536;
      if (lane < 55) warm = __builtin_nontemporal_load((const unsigned*)p);
    }
    LDS_BARRIER();
    asm volatile("" : "+v"(Q.w[0]));
    __builtin_amdgcn_sched_barrier(0);
    q_partial(Q, tmp, hw, lane);
    asm volatile("" : "+v"(epoch) : "v"(warm));
    epoch += 1;
    if (tid == 0) { misc[M_TAGA] = (unsigned)epoch; misc[M_TAGB] = (unsigned)(epoch >> 32) ^ ((unsigned)epoch * 0x9E3779B1u); }
  }
  hsync(misc, 1, lane);
  const unsigned tagA = misc[M_TAGA], tagB = misc[M_TAGB];
  if (hw == 0) {
    q_publish(tmp, rws, 0, band, cg, tagA, tagB, lane);
    STAMP_AT(16);
    __builtin_amdgcn_s_sleep(QPOLL_DELAY);
    *(LAS unsigned*)(L + L_QA + lane * 4) = q_gather(rws, 0, b, h, tagA, tagB, lane);
    lds_signal(misc + M_QRDY, lane);
    STAMP_AT(17);
    const u32x4 xv = __builtin_nontemporal_load((const u32x4*)(x + b * DM + lane * 8)), gv = __builtin_nontemporal_load((const u32x4*)(gw + lane * 8));
    float s = 0.f;
#pragma unroll
    for (int j = 0; j < 4; ++j) { const unsigned p = cvtpk(bflo(xv[j]) * bflo(gv[j]), bfhi(xv[j]) * bfhi(gv[j])); s += bflo(p); s += bfhi(p); }
    s = rbf(wave_sum(s));
    gate = rbf(1.f / (1.f + __expf(-s)));
  }
  unsigned zpk = 0;
  {
    wait_time(t0, T_QB);
    QLoad Q;
    q_issue(Q, x, wqb, band * 128 + hw * 32, cg * 64, lane);
    q_partial(Q, tmp + 1024, hw, lane);
    hsync(misc, 2, lane);
    if (hw == 1) q_publish(tmp + 1024, rws, 1, band, cg, tagA, tagB, lane);
    wait_time(t0, T_QBG);
    if (hw == 3) gossip(0, misc, rws, bid, tagA, lane);
    *(LAS unsigned*)(L + L_QB + hw * 256 + lane * 4) = q_gather(rws, 1, b, xg * 4 + hw, tagA, tagB, lane);
    lds_wait_ge(misc + M_BDMA, 4);
    if (hw == 0) STAMP_AT(3);
  }
  __builtin_amdgcn_s_setprio(0);
  hsync(misc, 3, lane);
  {
    u32x4 q[4];
#pragma unroll
    for (int g = 0; g < 4; ++g) q[g] = *(LAS u32x4*)(L + L_QB + g * 256 + c * 16);
    float m[4], l[4], acc[4][8];
#pragma unroll
    for (int g = 0; g < 4; ++g) { m[g] = -INFINITY; l[g] = 0.f;
#pragma unroll
      for (int j = 0; j < 8; ++j) acc[g][j] = 0.f; }
#pragma nounroll
    for (int s = 0; s < 4; ++s) {
      u32x4 k[4], v[4];
#pragma unroll
      for (int t = 0; t < 4; ++t) { k[t] = *(LAS u32x4*)(L + L_BIG + (hw * 16 + s * 4 + t) * 1024 + lane * 16); v[t] = *(LAS u32x4*)(L + L_BIG + 65536 + (hw * 16 + s * 4 + t) * 1024 + lane * 16); }
      gqa_step(k, v, q, m, l, acc);
    }
#pragma unroll
    for (int g = 0; g < 4; ++g) {
      const float M = xrow_max(m[g]), w = ex2((m[g] - M) * L2E), Ls = xrow_sum(l[g] * w);
#pragma unroll
      for (int j = 0; j < 8; ++j) acc[g][j] *= w;
      LAS float* rec = tmp + (g * 4 + hw) * REC_F;
      *(LAS f32x2*)(rec + 4 + c * 8 + r * 2) = fold_rows(acc[g]);
      if (lane == 0) *(LAS f32x2*)rec = f32x2{M, Ls};
    }
  }
  hsync(misc, 4, lane);
  {
    LAS float* rec = tmp + hw * 4 * REC_F;
    f32x2 ml[4], av[4];
#pragma unroll
    for (int i = 0; i < 4; ++i) { ml[i] = *(LAS f32x2*)(rec + i * REC_F); av[i] = *(LAS f32x2*)(rec + i * REC_F + 4 + lane * 2); }
    const float M = fmaxf(fmaxf(ml[0][0], ml[1][0]), fmaxf(ml[2][0], ml[3][0]));
    float Ls = 0.f, o0 = 0.f, o1 = 0.f;
#pragma unroll
    for (int i = 0; i < 4; ++i) { const float wt = ex2((ml[i][0] - M) * L2E); Ls = fmaf(wt, ml[i][1], Ls); o0 = fmaf(wt, av[i][0], o0); o1 = fmaf(wt, av[i][1], o1); }
    *(LAS unsigned*)(L + L_PUB + hw * 256 + lane * 4) = cvtpk(o0 / Ls, o1 / Ls);
    if (lane == 0) *(LAS f32x2*)(L + L_PUB + 1024 + hw * 8) = f32x2{M, Ls};
  }
  __builtin_amdgcn_s_setprio(2);
  hsync(misc, 5, lane);
  if (hw == 0) {
    const int pi = (b * 8 + xg) * 8 + sp;
    bst(*(LAS u32x4*)(L + L_PUB + lane * 16), rws, lane * 16, WS_PBP + pi * 1024);
    const u32x2 ml = *(LAS u32x2*)(L + L_PUB + 1024 + (lane & 3) * 8);
    DRAIN();
    if (lane < 4) bst(u32x4{ml[0], tagA, ml[1], tagB}, rws, lane * 16, WS_PBM + pi * 64);
    STAMP_AT(4);
  }
  if (hw == 3) gossip(1, misc, rws, bid, tagA, lane);
  wait_time(t0, T_WO);
  {
    const rsrc_t rw = mk_rsrc(wob, 4096 * DM * 2);
    u32x4 w[16];
#pragma unroll
    for (int i = 0; i < 16; ++i) { CAPWAIT(WO_CAP + 1); w[i] = bld<AUX_NT>(rw, lane * 16, (h * 128 + side * 64 + hw * 16 + i) * 1024); }
    __builtin_amdgcn_sched_barrier(0);
    u32x4 ml[1];
    poll<1>(ml, rws, (lane >> 3) * 64, 0, WS_PBM + (b * 8 + xg) * 512 + (h & 3) * 16, tagA, tagB);
    const unsigned av = __builtin_amdgcn_raw_buffer_load_b32(rws, (lane >> 3) * 1024 + (lane & 7) * 4, WS_PBP + (b * 8 + xg) * 8192 + (h & 3) * 256 + side * 128 + hw * 32, AUX_SC1);
    const float pm = __uint_as_float(ml[0][0]), M = xrow_max(fmaxf(pm, dpp<XOR8>(pm)));
    float Ls = ex2((pm - M) * L2E) * __uint_as_float(ml[0][2]), o0 = Ls * bflo(av), o1 = Ls * bfhi(av);
    Ls = xrow_sum(Ls + dpp<XOR8>(Ls)); o0 = xrow_sum(o0 + dpp<XOR8>(o0)); o1 = xrow_sum(o1 + dpp<XOR8>(o1));
    const unsigned ob = cvtpk(o0 / Ls, o1 / Ls);
    float z[8];
#pragma unroll
    for (int j = 0; j < 8; ++j) z[j] = 0.f;
#pragma unroll
    for (int i = 0; i < 16; i += 2) {
      const unsigned op = __builtin_amdgcn_readlane(ob, i >> 1);
#pragma unroll
      for (int j = 0; j < 4; ++j) { z[2 * j] = dot2(plo(w[i + 1][j], w[i][j]), op, z[2 * j]); z[2 * j + 1] = dot2(phi(w[i + 1][j], w[i][j]), op, z[2 * j + 1]); }
    }
    *(LAS f32x4*)(tmp + hw * 512 + lane * 8) = f32x4{z[0], z[1], z[2], z[3]};
    *(LAS f32x4*)(tmp + hw * 512 + lane * 8 + 4) = f32x4{z[4], z[5], z[6], z[7]};
    hsync(misc, 6, lane);
    f32x2 zs = {0.f, 0.f};
#pragma unroll
    for (int i = 0; i < 4; ++i) zs += *(LAS f32x2*)(tmp + i * 512 + tid * 2);
    zpk = cvtpk(zs[0], zs[1]);
  }
  if (hw == 0) STAMP_AT(5);
  if (hw == 3) gossip(2, misc, rws, bid, tagA, lane);
  constexpr int HS = 6;
  unsigned wa[16][8];
  {
    const rsrc_t rw = mk_rsrc(woa, 4096 * DM * 2);
    u32x4 w[32];
#pragma unroll
    for (int i = 0; i < 32; ++i) { CAPWAIT(WO_CAP + 1); w[i] = bld<AUX_NT>(rw, lane * 16, (h * 128 + hw * 32 + i) * 1024); }
    __builtin_amdgcn_sched_barrier(0);
#pragma unroll
    for (int i = 0; i < 16; ++i)
#pragma unroll
      for (int j = 0; j < 4; ++j) { wa[i][2 * j] = plo(w[2 * i + 1][j], w[2 * i][j]); wa[i][2 * j + 1] = phi(w[2 * i + 1][j], w[2 * i][j]); asm volatile("" : "+v"(wa[i][2 * j]), "+v"(wa[i][2 * j + 1])); }
  }
  if (hw == 0) STAMP_AT(7);
  if (hw == 3) gossip(3, misc, rws, bid, tagA, lane);
  const u64 t3 = __builtin_amdgcn_s_memrealtime();
  {
    lds_wait_ge(misc + M_KDONE, 4);
    const f32x4 wm = *(LAS f32x4*)(miscf + M_WMAX);
    const float M = fmaxf(fmaxf(wm[0], wm[1]), fmaxf(wm[2], wm[3])), mL = -M * L2E;
    float ls = 0.f;
#pragma unroll
    for (int i = 0; i < 2; ++i) {
      const u32x4 sc = *(LAS u32x4*)(L + L_SC + hw * 2048 + i * 1024 + lane * 16);
#pragma unroll
      for (int j = 0; j < 4; ++j) ls += ex2(fmaf(bflo(sc[j]), L2E, mL)) + ex2(fmaf(bfhi(sc[j]), L2E, mL));
    }
    ls = wave_sum(ls);
    if (lane == 0) miscf[M_LSUM + hw] = ls;
    hsync(misc, HS + 1, lane);
    if (hw == 0) {
      const f32x4 lw = *(LAS f32x4*)(miscf + M_LSUM);
      const float Ls = (lw[0] + lw[1]) + (lw[2] + lw[3]);
      if (lane < 4) bst(u32x4{__float_as_uint(M), tagA, __float_as_uint(Ls), tagB}, rws, lane * 16, WS_ST + (b * 64 + jp) * 64);
      wait_time(__builtin_amdgcn_s_memrealtime(), T_ST);
      u32x4 o[1];
      poll<1>(o, rws, 0, 0, WS_ST + (b * 64 + (jp ^ 1)) * 64, tagA, tagB);
      const float Mo = __uint_as_float(o[0][0]), MM = fmaxf(M, Mo), wt = ex2((M - MM) * L2E);
      if (lane == 0) miscf[M_CNORM] = wt / (wt * Ls + ex2((Mo - MM) * L2E) * __uint_as_float(o[0][2]));
      if (bid == 0) {
        u32x4 all[4];
        poll<4>(all, rws, lane * 64, 4096, WS_ST, tagA, tagB);
        if (lane == 0) __hip_atomic_store((GAS u64*)(ws + WS_EPOCH), epoch, RLX_AGENT);
      }
      STAMP_AT(6);
    } else if (hw == 3) {
#pragma nounroll
      for (int k = 4; k < NCP && (unsigned)__builtin_amdgcn_readfirstlane(__hip_atomic_load(misc + M_VCLAIM, RLX_WG)) < NU - 16 - T_GOSSIP / 28; ++k) {
        wait_time(t3, (k - 3) * T_GOSSIP);
        gossip(k, misc, rws, bid, tagA, lane);
      }
    }
  }
  LDS_BARRIER();
  if (hw == 1) STAMP_AT(12);
  {
    const f32x2 av = *(LAS f32x2*)(L + L_REC + r * 512 + hw * 128 + c * 8);
    const float cn = miscf[M_CNORM];
    const unsigned ob = cvtpk(xrow_sum(av[0]) * cn, xrow_sum(av[1]) * cn);
    float y[8];
#pragma unroll
    for (int j = 0; j < 8; ++j) y[j] = 0.f;
#pragma unroll
    for (int i = 0; i < 16; ++i) {
      const unsigned op = __builtin_amdgcn_readlane(ob, i);
#pragma unroll
      for (int j = 0; j < 8; ++j) y[j] = dot2(wa[i][j], op, y[j]);
    }
    *(LAS f32x4*)(tmp + hw * 512 + lane * 8) = f32x4{y[0], y[1], y[2], y[3]};
    *(LAS f32x4*)(tmp + hw * 512 + lane * 8 + 4) = f32x4{y[4], y[5], y[6], y[7]};
  }
  LDS_BARRIER();
  {
    f32x2 ys = {0.f, 0.f};
#pragma unroll
    for (int i = 0; i < 4; ++i) ys += *(LAS f32x2*)(tmp + i * 512 + tid * 2);
    const unsigned ypk = cvtpk(ys[0], ys[1]);
    bst(u32x4{plo(zpk, ypk), tagA, phi(zpk, ypk), tagB}, rws, (tid >> 2) * 4096 + (tid & 3) * 16, WS_YG + b * 64 * 4096 + jp * 64);
  }
  if (hw == 1) STAMP_AT(13);
  if (hw == 0) {
    u32x4 g[4];
    poll<4>(g, rws, lane * 64, 16, WS_YG + (b * 64 + jp) * 4096, tagA, tagB);
    float a[8], z[8];
#pragma unroll
    for (int j = 0; j < 8; ++j) { a[j] = bflo(g[j >> 1][(j & 1) * 2]); z[j] = bfhi(g[j >> 1][(j & 1) * 2]); }
    float u[8];
#pragma unroll
    for (int j = 0; j < 8; ++j) u[j] = fold32(a[j], z[j]);
#pragma unroll
    for (int j = 0; j < 4; ++j) u[j] = fold16(u[j], u[j + 4]);
#pragma unroll
    for (int j = 0; j < 2; ++j) u[j] = foldd<XOR8>(u[j], u[j + 2], lane & 8);
    u[0] = foldd<XOR7>(u[0], u[1], lane & 4);
    u[0] += dpp<XOR2>(u[0]); u[0] += dpp<XOR1>(u[0]);
    const float t = rbf(rbf(u[0]) * (lane < 32 ? gate : rbf(1.f - gate)));
    const unsigned res = cvtpk(fold32(t, t), 0.f);
    if (lane < 32 && (lane & 3) == 0) out[b * DM + jp * 8 + (lane >> 2)] = (u16)res;
    STAMP_AT(14);
  }
}

extern "C" void kernel_launch(void* const* d_in, const int* in_sizes, int n_in, void* d_out, int out_size, void* d_ws, size_t ws_size, hipStream_t stream) {
  k_fused<<<256, 512, 0, stream>>>((const u16*)d_in[0], (const u16*)d_in[1], (const u16*)d_in[2], (const u16*)d_in[3], (const u16*)d_in[4], (const u16*)d_in[6], (const u16*)d_in[7],
                                   (const u16*)d_in[8], (const u16*)d_in[9], (const u16*)d_in[10], (u16*)d_out, (unsigned char*)d_ws);
}
```

```cpp
#include <hip/hip_runtime.h>
#include <hip/hip_bf16.h>
#include <cstdint>

typedef unsigned u32x4 __attribute__((ext_vector_type(4)));
typedef unsigned u32x2 __attribute__((ext_vector_type(2)));
typedef float f32x4 __attribute__((ext_vector_type(4)));
typedef float f32x2 __attribute__((ext_vector_type(2)));
typedef __attribute__((ext_vector_type(2))) __bf16 bf16x2;
typedef unsigned long long u64;
typedef unsigned short u16;
#define GAS __attribute__((address_space(1)))
#define LAS __attribute__((address_space(3)))
#define RLX_AGENT __ATOMIC_RELAXED, __HIP_MEMORY_SCOPE_AGENT
#define RLX_WG __ATOMIC_RELAXED, __HIP_MEMORY_SCOPE_WORKGROUP
#define DRAIN() asm volatile("s_waitcnt vmcnt(0)" ::: "memory")
#define LDS_BARRIER() do { asm volatile("s_waitcnt lgkmcnt(0)" ::: "memory"); __builtin_amdgcn_s_barrier(); asm volatile("" ::: "memory"); } while (0)
typedef __amdgpu_buffer_rsrc_t rsrc_t;

#define CAPV 5
#define CAPB 5
#define UPB_LOG 2
#define WO_CAP 1
#define QPOLL_DELAY 16
#define OPEN0 2
#define OPEN1 16
#define WO_LAG 0
#define WO_STAG 0
#define T_WOA 0
#define DRY 0
#define YSLEEP 2
#define EG_STEPS 8
#define EG_GAIN 30
#define WB_EARLY 4
#define TLBW 0
#define CAPB2 CAPB
#define GMODE 3
#define BSTEPS 4
#define ABL_WSZ (4096 * DM * 2)
#define T_QG 540
#define BPAT 2
#define WOI 1
#define YDELAY 24
#define T_QB 0
#define T_QBG 1200
#define T_WO 1800
#define T_ST 500

constexpr int NB = 4, DM = 512, NH = 32, SK = 8192, DH = 128, WIN = 2048;
constexpr float SCALE = 0.088388347648318440f, L2E = 1.4426950408889634f;
constexpr int AUX_NT = 2, AUX_SC1 = 16, RSRC_W3 = 0x00020000;
constexpr int NS = 32;
constexpr unsigned SPIN_CAP = 1u << 15;

constexpr int WS_EPOCH = 0;
constexpr int WS_PH = 4096, NCP = 14;
constexpr int WS_QF = 32768;
constexpr int WS_QP = WS_QF + 32768;
constexpr int WS_PBM = WS_QP + 524288;
constexpr int WS_ST = WS_PBM + 16384;
constexpr int WS_PBP = WS_ST + 16384;
constexpr int WS_YG = WS_PBP + 262144;
constexpr int WS_STAMP = WS_YG + 256 * 4096;
constexpr int WS_BYTES = WS_STAMP + 2 * 256 * 256;

constexpr int L_MISC = 0, L_QA = 256, L_QB = 512, L_REC = 1536, L_PROG = 3584, L_SC = 4096, L_TMP = 12288, L_PUB = L_TMP + 8704, L_BIG = 22528, L_BYTES = L_BIG + 131072;
constexpr int REC_F = 132;
enum { M_TAGA = 0, M_TAGB = 1, M_ANCH = 2, M_STEP = 3, M_QRDY = 4, M_HSYNC = 5, M_KDONE = 6, M_BDMA = 7, M_WMAX = 8, M_LSUM = 12, M_CNORM = 16, M_PH = 20 };

__device__ __forceinline__ float dot2(unsigned a, unsigned b, float c) {
  return __builtin_amdgcn_fdot2_f32_bf16(__builtin_bit_cast(bf16x2, a), __builtin_bit_cast(bf16x2, b), c, false);
}
__device__ __forceinline__ unsigned cvtpk(float lo, float hi) { return __builtin_bit_cast(unsigned, __builtin_convertvector(f32x2{lo, hi}, bf16x2)); }
__device__ __forceinline__ float bflo(unsigned u) { return __uint_as_float(u << 16); }
__device__ __forceinline__ float bfhi(unsigned u) { return __uint_as_float(u & 0xffff0000u); }
__device__ __forceinline__ float rbf(float x) { return bflo(cvtpk(x, 0.f)); }
__device__ __forceinline__ float ex2(float x) { return __builtin_amdgcn_exp2f(x); }
template <int CTRL> __device__ __forceinline__ float dpp(float x) {
  return __builtin_bit_cast(float, __builtin_amdgcn_mov_dpp(__builtin_bit_cast(int, x), CTRL, 0xf, 0xf, true));
}
constexpr int XOR1 = 0xB1, XOR2 = 0x4E, XOR7 = 0x141, XOR8 = 0x128;
__device__ __forceinline__ float row16_sum(float x) { x += dpp<XOR1>(x); x += dpp<XOR2>(x); x += dpp<XOR7>(x); x += dpp<XOR8>(x); return x; }
__device__ __forceinline__ float row16_max(float x) { x = fmaxf(x, dpp<XOR1>(x)); x = fmaxf(x, dpp<XOR2>(x)); x = fmaxf(x, dpp<XOR7>(x)); return fmaxf(x, dpp<XOR8>(x)); }
template <int CTRL> __device__ __forceinline__ float foldd(float a, float b, bool hi) { return (hi ? b : a) + dpp<CTRL>(hi ? a : b); }
__device__ __forceinline__ float fold32(float x, float y) {
  auto t = __builtin_amdgcn_permlane32_swap(__float_as_uint(x), __float_as_uint(y), false, false);
  return __uint_as_float(t[0]) + __uint_as_float(t[1]);
}
__device__ __forceinline__ float fold16(float x, float y) {
  auto t = __builtin_amdgcn_permlane16_swap(__float_as_uint(x), __float_as_uint(y), false, false);
  return __uint_as_float(t[0]) + __uint_as_float(t[1]);
}
__device__ __forceinline__ float xrow_sum(float x) { x = fold16(x, x); return fold32(x, x); }
__device__ __forceinline__ float xrow_max(float x) {
  auto s = __builtin_amdgcn_permlane16_swap(__float_as_uint(x), __float_as_uint(x), false, false);
  x = fmaxf(__uint_as_float(s[0]), __uint_as_float(s[1]));
  auto t = __builtin_amdgcn_permlane32_swap(__float_as_uint(x), __float_as_uint(x), false, false);
  return fmaxf(__uint_as_float(t[0]), __uint_as_float(t[1]));
}
__device__ __forceinline__ float wave_sum(float x) { return xrow_sum(row16_sum(x)); }
__device__ __forceinline__ rsrc_t mk_rsrc(const void* p, int bytes) { return __builtin_amdgcn_make_buffer_rsrc(const_cast<void*>(p), 0, bytes, RSRC_W3); }
template <int AUX> __device__ __forceinline__ u32x4 bld(rsrc_t r, int voff, int soff) { return __builtin_amdgcn_raw_buffer_load_b128(r, voff, soff, AUX); }
template <int AUX> __device__ __forceinline__ u32x2 bld2(rsrc_t r, int voff, int soff) { return __builtin_amdgcn_raw_buffer_load_b64(r, voff, soff, AUX); }
#define QP_AUX 18
#define QF_AUX 18
#define PB_AUX 18
#define PM_AUX 18
#define ST_AUX 16
#define LD_AUX 16
#define Y_AUX 16
template <int AUX = ST_AUX> __device__ __forceinline__ void bst(u32x4 v, rsrc_t r, int voff, int soff) { __builtin_amdgcn_raw_buffer_store_b128(v, r, voff, soff, AUX); }
__device__ __forceinline__ unsigned plo(unsigned hi, unsigned lo) { return __builtin_amdgcn_perm(hi, lo, 0x05040100u); }
__device__ __forceinline__ unsigned phi(unsigned hi, unsigned lo) { return __builtin_amdgcn_perm(hi, lo, 0x07060302u); }
#define CAPWAIT(n) do { __builtin_amdgcn_sched_barrier(0); asm volatile("s_waitcnt vmcnt(%0)" :: "i"((n) - 1) : "memory"); } while (0)

#define ADAPT 1
#define TAU 111
#define TK0 560
#define XGAIN 10
#define XCENTER 12
#define XMIN 6
#define T_GOSSIP 900
#define WLOG 1
#define ROT 1
#define DITH(t) ((((t) & 1) << 2) | ((t) & 2) | (((t) & 4) >> 2))
#define WINWAIT(x, t) do { __builtin_amdgcn_sched_barrier(0); asm volatile("s_cmp_gt_i32 %0, %2\n\ts_cbranch_scc1 2f\n\ts_cmp_gt_i32 %0, %1\n\ts_cbranch_scc1 1f\n\ts_waitcnt vmcnt(3)\n1:\n\ts_waitcnt vmcnt(4)\n" \
    "2:\n\ts_cmp_gt_i32 %0, %3\n\ts_cbranch_scc1 3f\n\ts_waitcnt vmcnt(5)\n3:\n\ts_waitcnt vmcnt(6)" :: "s"(x), "i"(7 - DITH(t)), "i"(15 - DITH(t)), "i"(23 - DITH(t)) : "memory", "scc"); } while (0)
__device__ __forceinline__ void glds16(const GAS unsigned char* src, unsigned dst) {
  unsigned keep;
  asm volatile("s_mov_b32 %0, m0\n\ts_mov_b32 m0, %2\n\ts_nop 0\n\tglobal_load_lds_dwordx4 %1, off nt\n\ts_mov_b32 m0, %0" : "=&s"(keep) : "v"(src), "s"(dst) : "memory");
}

#define STAMP_AT(i) do {} while (0)
#define STAMP_LDS(i) do {} while (0)
#define PROG(i) do {} while (0)

__device__ __forceinline__ void wait_time(u64 t0, unsigned ticks) {
#pragma nounroll
  while (__builtin_amdgcn_s_memrealtime() - t0 < ticks) __builtin_amdgcn_s_sleep(4);
}
__device__ __forceinline__ void lds_wait_ge(LAS unsigned* p, unsigned target) {
#pragma nounroll
  for (unsigned spins = 0; spins < 32 * SPIN_CAP && (unsigned)__builtin_amdgcn_readfirstlane(__hip_atomic_load(p, RLX_WG)) < target; ++spins) __builtin_amdgcn_s_sleep(2);
  asm volatile("" ::: "memory");
}
__device__ __forceinline__ void lds_signal(LAS unsigned* p, int lane) {
  asm volatile("s_waitcnt lgkmcnt(0)" ::: "memory");
  if (lane == 0) __builtin_amdgcn_atomic_inc32(p, 0xffffffffu, __ATOMIC_RELAXED, "workgroup");
}
__device__ __forceinline__ void hsync(LAS unsigned* misc, unsigned n, int lane) { lds_signal(misc + M_HSYNC, lane); lds_wait_ge(misc + M_HSYNC, 4 * n); }
template <int N, int AUX = LD_AUX> __device__ __forceinline__ void poll(u32x4 (&g)[N], rsrc_t rws, int voff, int stride, int soff, unsigned tagA, unsigned tagB) {
#pragma nounroll
  for (unsigned spins = 0; spins < SPIN_CAP; ++spins) {
    asm volatile("" ::: "memory");
    bool ok = true;
#pragma unroll
    for (int i = 0; i < N; ++i) g[i] = bld<AUX>(rws, voff + i * stride, soff);
#pragma unroll
    for (int i = 0; i < N; ++i) ok &= (g[i][1] == tagA) & (g[i][3] == tagB);
    if (__all(ok)) break;
    __builtin_amdgcn_s_sleep(2);
  }
  __builtin_amdgcn_fence(__ATOMIC_ACQUIRE, "wavefront");
  asm volatile("" ::: "memory");
}

__device__ __forceinline__ void gossip(int k, LAS unsigned* misc, rsrc_t rws, int bid, unsigned tagA, int lane) {
  asm volatile("" ::: "memory");
  const u32x4 wp = *(LAS u32x4*)(misc + M_PH);
  const unsigned ph = wp[0] + (((int)(wp[1] - wp[0]) + (int)(wp[2] - wp[0]) + (int)(wp[3] - wp[0])) >> 2);
  constexpr int SH = GMODE == 1 ? 2 : GMODE + 1, STR = GMODE == 1 ? 8 : 64, AUX = GMODE == 1 ? 16 : 18;
  const int me = (bid & 7) + ((bid >> (3 + SH)) << 3);
  if ((((bid >> 3) + k) & ((1 << SH) - 1)) == 0 && lane < STR / 8) __builtin_amdgcn_raw_buffer_store_b64(u32x2{ph, tagA}, rws, lane * 8, WS_PH + k * 2048 + me * STR, AUX);
  if (k > 0) {
    asm volatile("" ::: "memory");
    const u32x2 g = bld2<AUX>(rws, (lane & ((256 >> SH) - 1)) * STR, WS_PH + (k - 1) * 2048);
    const bool ok = g[1] == tagA;
    const float d = wave_sum(ok ? (float)(int)(g[0] - ph) : 0.f), n = wave_sum(ok ? 1.f : 0.f);
    if (n >= 32.f && lane == 0) __hip_atomic_store(misc + M_ANCH, ph + (int)(d / n), RLX_WG);
  }
}

__device__ __forceinline__ void gqa_step(const u32x4 (&k)[4], const u32x4 (&v)[4], const u32x4 (&q)[4], float (&m)[4], float (&l)[4], float (&acc)[4][8]) {
#pragma unroll
  for (int g = 0; g < 4; ++g) {
    float s[4];
#pragma unroll
    for (int t = 0; t < 4; ++t) { float d = dot2(k[t][0], q[g][0], 0.f); d = dot2(k[t][1], q[g][1], d); d = dot2(k[t][2], q[g][2], d); s[t] = dot2(k[t][3], q[g][3], d); }
#pragma unroll
    for (int t = 0; t < 4; t += 2) {
      const unsigned r1 = cvtpk(row16_sum(s[t]), row16_sum(s[t + 1]));
      const unsigned r2 = cvtpk(bflo(r1) * SCALE, bfhi(r1) * SCALE);
      s[t] = bflo(r2); s[t + 1] = bfhi(r2);
    }
    const float mn = fmaxf(fmaxf(m[g], fmaxf(s[0], s[1])), fmaxf(s[2], s[3]));
    const float al = ex2((m[g] - mn) * L2E), mL = -mn * L2E;
    m[g] = mn;
    float ls = l[g] * al;
#pragma unroll
    for (int j = 0; j < 8; ++j) acc[g][j] *= al;
#pragma unroll
    for (int t = 0; t < 4; t += 2) {
      const float p0 = ex2(fmaf(s[t], L2E, mL)), p1 = ex2(fmaf(s[t + 1], L2E, mL));
      ls += p0 + p1;
      const unsigned w = cvtpk(p0, p1);
#pragma unroll
      for (int j = 0; j < 4; ++j) {
        acc[g][2 * j] = dot2(v[t + 1][j], w >> 16, dot2(v[t][j], w & 0xffffu, acc[g][2 * j]));
        acc[g][2 * j + 1] = dot2(v[t + 1][j], w & 0xffff0000u, dot2(v[t][j], w << 16, acc[g][2 * j + 1]));
      }
    }
    l[g] = ls;
  }
}
__device__ __forceinline__ f32x2 fold_rows(const float (&a)[8]) {
  const float u0 = fold32(a[0], a[4]), u1 = fold32(a[1], a[5]), u2 = fold32(a[2], a[6]), u3 = fold32(a[3], a[7]);
  return f32x2{fold16(u0, u2), fold16(u1, u3)};
}
__device__ __forceinline__ void q_partial(const u32x4 (&w)[4], const u32x2 (&xv)[NB], LAS float* part, int lane) {
  float a[NB][8];
#pragma unroll
  for (int j = 0; j < 4; ++j) {
    const unsigned p0 = plo(w[1][j], w[0][j]), p1 = phi(w[1][j], w[0][j]), p2 = plo(w[3][j], w[2][j]), p3 = phi(w[3][j], w[2][j]);
#pragma unroll
    for (int bb = 0; bb < NB; ++bb) { a[bb][2 * j] = dot2(p2, xv[bb][1], dot2(p0, xv[bb][0], 0.f)); a[bb][2 * j + 1] = dot2(p3, xv[bb][1], dot2(p1, xv[bb][0], 0.f)); }
  }
  float f[8];
#pragma unroll
  for (int j = 0; j < 8; ++j) f[j] = fold16(fold32(a[0][j], a[2][j]), fold32(a[1][j], a[3][j]));
#pragma unroll
  for (int j = 0; j < 4; ++j) f[j] = foldd<XOR8>(f[j], f[j + 4], lane & 8);
  *(LAS f32x4*)(part + (lane >> 4) * 64 + (lane & 7) * 8 + ((lane >> 3) & 1) * 4) = f32x4{f[0], f[1], f[2], f[3]};
}
__device__ __forceinline__ void q_fetch(u32x2 (&p)[4], rsrc_t rws, int br, int b, int hq, int lane) {
#pragma unroll
  for (int i = 0; i < 4; ++i) p[i] = bld2<QP_AUX>(rws, lane * 8 + i * 16384, WS_QP + br * 262144 + b * 65536 + hq * 512);
}
__device__ __forceinline__ unsigned q_sum(const u32x2 (&p)[4]) {
  const f32x2 s = (__builtin_bit_cast(f32x2, p[0]) + __builtin_bit_cast(f32x2, p[1])) + (__builtin_bit_cast(f32x2, p[2]) + __builtin_bit_cast(f32x2, p[3]));
  return cvtpk(s[0], s[1]);
}
template <int J> __device__ __forceinline__ unsigned bcast16(unsigned x) { return __builtin_amdgcn_mov_dpp(x, 0x150 + J, 0xf, 0xf, true); }
template <int I, int N> struct Proj {
  static __device__ __forceinline__ void run(float (&y)[8], const unsigned (&w)[N][8], unsigned ob) {
    const unsigned op = bcast16<I>(ob);
#pragma unroll
    for (int j = 0; j < 8; ++j) y[j] = dot2(op, w[I][j], y[j]);
    if constexpr (I + 1 < N) Proj<I + 1, N>::run(y, w, ob);
  }
};

__global__ void __launch_bounds__(512, 2) k_fused(const u16* __restrict__ x, const u16* __restrict__ ka, const u16* __restrict__ va, const u16* __restrict__ wqa, const u16* __restrict__ woa,
                                                  const u16* __restrict__ kb, const u16* __restrict__ vb, const u16* __restrict__ wqb, const u16* __restrict__ wob,
                                                  const u16* __restrict__ gw, u16* __restrict__ out, unsigned char* __restrict__ ws) {
  __shared__ __attribute__((aligned(1024))) unsigned char smem[L_BYTES];
  LAS unsigned char* L = (LAS unsigned char*)smem;
  LAS unsigned* misc = (LAS unsigned*)(L + L_MISC);
  LAS float* miscf = (LAS float*)(L + L_MISC);
  const unsigned lds0 = (unsigned)(size_t)L;
  const int tid = threadIdx.x, lane = tid & 63, wave = __builtin_amdgcn_readfirstlane(tid >> 6), r = lane >> 4, c = lane & 15;
  const int bid = blockIdx.x, xg = bid & 7, idx = bid >> 3, b = idx & 3, sp = idx >> 2;
  const int side = sp & 1, h = xg * 4 + (idx >> 3), jp = h * 2 + side;
  asm volatile("" :: "s"(x), "s"(ka), "s"(va), "s"(wqa), "s"(woa), "s"(kb), "s"(vb), "s"(wqb), "s"(wob), "s"(gw), "s"(out), "s"(ws));
  if (wave == 0) STAMP_AT(0);

  if (wave >= 4) {
    __builtin_amdgcn_s_setprio(1);
    const int sw = wave - 4, voff = lane * 16;
    if (tid == 256) { const unsigned a0 = (unsigned)__builtin_amdgcn_s_memrealtime() + TK0; *(LAS u32x4*)(misc + 4) = u32x4{0u, 0u, 0u, 0u}; *(LAS u32x2*)(misc + 2) = u32x2{a0, 0u}; *(LAS u32x4*)(misc + M_PH) = u32x4{a0, a0, a0, a0}; }
    LDS_BARRIER();
    {
      const size_t o = ((size_t)(b * 8 + xg) * SK + (SK - WIN) + sp * 64 + sw * 16) * DH;
#define BOFF(i) (((((i) >> 2) + (BPAT == 2 ? xg : BPAT == 3 ? (xg >> 1) : 0)) & 3) * 131072 + ((i) & 3) * 1024)
      const GAS unsigned char* pk = (const GAS unsigned char*)(kb + o) + voff;
      const GAS unsigned char* pv = (const GAS unsigned char*)(vb + o) + voff;
#pragma unroll 4
      for (int i = 0; i < 16; ++i) { CAPWAIT(CAPB); glds16(pk + BOFF(i), lds0 + L_BIG + sw * 4096 + (i >> 2) * 16384 + (i & 3) * 1024); }
      if (sw == 0) STAMP_LDS(20);
#pragma unroll 4
      for (int i = 0; i < 16; ++i) { CAPWAIT(CAPB2); glds16(pv + BOFF(i), lds0 + L_BIG + 65536 + sw * 4096 + (i >> 2) * 16384 + (i & 3) * 1024); }
      if (sw == 0) STAMP_LDS(21);
    }
    const size_t kvoff = (size_t)(b * NH + h) * SK * DH;
    const u16* kp = ka + kvoff; const u16* vp = va + kvoff;
#define SOFF(i) ((((((i) + ROT * h) & (NS - 1)) * 2 + side) << 15) + (sw << (WLOG + 10)))
#define TOFF(t) (((((t) >> WLOG) << (WLOG + 2)) + ((t) & ((1 << WLOG) - 1))) << 10)
#define RELOAD(t) d[t] = bld<AUX_NT>(rn, voff + (TOFF(t) & 4095), on + (TOFF(t) & ~4095)); __builtin_amdgcn_sched_barrier(0)
    u32x4 d[8], e[8];
    {
      const rsrc_t rn = mk_rsrc(kp, SK * 256);
#pragma unroll
      for (int t = 0; t < 8; ++t) { CAPWAIT(CAPB2); e[t] = bld<AUX_NT>(rn, voff + (TOFF(t) & 4095), SOFF(0) + (TOFF(t) & ~4095)); __builtin_amdgcn_sched_barrier(0); }
      static_assert(CAPB2 <= 8, "the 8 loads above must push the LDS-DMA out of the window");
      lds_signal(misc + M_BDMA, lane);
      const int on = SOFF(1);
#pragma unroll
      for (int t = 0; t < 8; ++t) { if (t < 8 - OPEN0) CAPWAIT(CAPB2); RELOAD(t); }
    }
    if (sw == 0) STAMP_AT(15);
    lds_wait_ge(misc + M_QRDY, 1);
    u32x4 q = *(LAS u32x4*)(L + L_QA + c * 16);
    asm volatile("" : "+v"(q));
    if (sw == 0) STAMP_AT(1);
    u64 now = __builtin_amdgcn_s_memrealtime();
    unsigned anch = __hip_atomic_load(misc + M_ANCH, RLX_WG);
#define PACE(i) \
      asm volatile("" : "+s"(now)); \
      const unsigned ph = (unsigned)now - (i) * TAU; \
      const int xw = max(XCENTER + (((int)(ph - __builtin_amdgcn_readfirstlane(anch)) * ((i) >= 2 * NS - EG_STEPS ? EG_GAIN : XGAIN)) >> 8), XMIN); \
      now = __builtin_amdgcn_s_memrealtime(); \
      anch = __hip_atomic_load(misc + M_ANCH, RLX_WG); \
      __hip_atomic_store(misc + M_PH + sw, ph, RLX_WG); \
      PROG((i) + 1)
    float mx = -INFINITY;
#define QK(v) dot2(v[3], q[3], dot2(v[2], q[2], dot2(v[1], q[1], dot2(v[0], q[0], 0.f))))
#define SCORES(i) \
      _Pragma("unroll") for (int j = 0; j < 4; ++j) s[j] = foldd<XOR7>(s[j], s[j + 4], lane & 4); \
      _Pragma("unroll") for (int j = 0; j < 2; ++j) s[j] = foldd<XOR2>(s[j], s[j + 2], lane & 2); \
      s[0] = foldd<XOR1>(s[0], s[1], lane & 1); \
      s[0] += dpp<XOR8>(s[0]);                                              \
      const unsigned sb = cvtpk(rbf(s[0]) * SCALE, 0.f);                    \
      mx = fmaxf(mx, bflo(sb)); \
      *(LAS u16*)(L + L_SC + (i) * 256 + sw * 64 + r * 16 + (c & 7) * 2) = (u16)sb
    {
      float s[8];
#pragma unroll
      for (int t = 0; t < 8; ++t) s[t] = QK(e[t]);
      SCORES(0);
    }
#pragma nounroll
    for (int i = 1; i < NS; ++i) {
      PACE(i);
      const rsrc_t rn = mk_rsrc(i + 1 < NS ? kp : vp, SK * 256);
      const int on = SOFF(i + 1);
      const int xe = i + 1 < NS ? xw : xw + OPEN1;
      float s[8];
#pragma unroll
      for (int t = 0; t < 8; ++t) { s[t] = QK(d[t]); WINWAIT(t < 6 ? xw : xe, t); RELOAD(t); }
      SCORES(i);
    }
    mx = xrow_max(row16_max(mx));
    if (lane == 0) miscf[M_WMAX + sw] = mx;
    lds_signal(misc + M_KDONE, lane);
    if (sw == 0) STAMP_AT(2);
    float acc[8];
#pragma unroll
    for (int j = 0; j < 8; ++j) acc[j] = 0.f;
    const float mL = -mx * L2E;
    u32x4 scn = *(LAS u32x4*)(L + L_SC + sw * 64 + r * 16);
#pragma nounroll
    for (int i = 0; i < NS; ++i) {
      u32x4 sc = scn;
      asm volatile("" : "+v"(sc));
      PACE(NS + i);
      if (sw == 0) __hip_atomic_store(misc + M_STEP, (unsigned)i, RLX_WG);
      scn = *(LAS u32x4*)(L + L_SC + ((i + 1) & (NS - 1)) * 256 + sw * 64 + r * 16);
      const rsrc_t rn = mk_rsrc(vp, i + 1 < NS ? SK * 256 : 0);
      const int on = SOFF(i + 1);
#pragma unroll
      for (int t = 0; t < 8; t += 2) {
        const unsigned w = cvtpk(ex2(fmaf(bflo(sc[t >> 1]), L2E, mL)), ex2(fmaf(bfhi(sc[t >> 1]), L2E, mL)));
#pragma unroll
        for (int j = 0; j < 4; ++j) { acc[2 * j] = dot2(d[t][j], w & 0xffffu, acc[2 * j]); acc[2 * j + 1] = dot2(d[t][j], w << 16, acc[2 * j + 1]); }
        WINWAIT(xw, t); RELOAD(t);
#pragma unroll
        for (int j = 0; j < 4; ++j) { acc[2 * j] = dot2(d[t + 1][j], w >> 16, acc[2 * j]); acc[2 * j + 1] = dot2(d[t + 1][j], w & 0xffff0000u, acc[2 * j + 1]); }
        WINWAIT(xw, t + 1); RELOAD(t + 1);
      }
    }
    asm volatile("" :: "s"(now), "v"(scn), "v"(anch));
    STAMP_AT(8 + sw);
    *(LAS f32x2*)(L + L_REC + sw * 512 + c * 32 + r * 8) = fold_rows(acc);
    LDS_BARRIER();
    return;
  }

  __builtin_amdgcn_s_setprio(2);
  const int hw = wave;
  const rsrc_t rws = mk_rsrc(ws, WS_BYTES);
  const u64 t0 = __builtin_amdgcn_s_memrealtime();
  LAS float* tmp = (LAS float*)(L + L_TMP);
  float gate = 0.f;
  u64 epoch = 0;
  const int band = xg >> 1, cg = (xg & 1) * 32 + idx;
  {
    const rsrc_t rx = mk_rsrc(x, NB * DM * 2), rwa = mk_rsrc(wqa, DM * 4096 * 2), rwb = mk_rsrc(wqb, DM * 4096 * 2);
    const int row0 = band * 128 + hw * 32, vo = (lane >> 3) * 4 * 8192 + (lane & 7) * 16;
    u32x4 wA[4], wB[4]; u32x2 xv[NB];
#pragma unroll
    for (int i = 0; i < 4; ++i) wA[i] = bld<AUX_NT>(rwa, vo, (row0 + i) * 8192 + cg * 128);
#pragma unroll
    for (int bb = 0; bb < NB; ++bb) xv[bb] = bld2<AUX_NT>(rx, (lane >> 3) * 8, (bb * DM + row0) * 2);
    if (hw == 0) epoch = __hip_atomic_load((GAS u64*)(ws + WS_EPOCH), RLX_AGENT);
    __builtin_amdgcn_sched_barrier(0);
#pragma unroll
    for (int i = 0; i < WB_EARLY; ++i) wB[i] = bld<AUX_NT>(rwb, vo, (row0 + i) * 8192 + cg * 128);
    __builtin_amdgcn_sched_barrier(0);
    LDS_BARRIER();
#pragma unroll
    for (int i = WB_EARLY; i < 4; ++i) wB[i] = bld<AUX_NT>(rwb, vo, (row0 + i) * 8192 + cg * 128);
    asm volatile("" : "+v"(wA[0]));
    __builtin_amdgcn_sched_barrier(0);
    q_partial(wA, xv, tmp + hw * 256, lane);
    asm volatile("" : "+v"(epoch));
    epoch += 1;
    if (tid == 0) { misc[M_TAGA] = (unsigned)epoch; misc[M_TAGB] = (unsigned)(epoch >> 32) ^ ((unsigned)epoch * 0x9E3779B1u); }
    if (hw == 0) STAMP_LDS(19);
    q_partial(wB, xv, tmp + 1024 + hw * 256, lane);
  }
  hsync(misc, 1, lane);
  const unsigned tagA = misc[M_TAGA], tagB = misc[M_TAGB];
  if (hw == 0) {
#pragma unroll
    for (int br = 0; br < 2; ++br) {
      LAS float* part = tmp + br * 1024 + lane * 4;
      const f32x4 s = (*(LAS f32x4*)part + *(LAS f32x4*)(part + 256)) + (*(LAS f32x4*)(part + 512) + *(LAS f32x4*)(part + 768));
      bst<QP_AUX>(__builtin_bit_cast(u32x4, s), rws, (lane >> 4) * 65536 + (lane & 15) * 16, WS_QP + br * 262144 + band * 16384 + cg * 256);
    }
    STAMP_LDS(18);
    DRAIN();
    if (lane < 4) bst<QF_AUX>(u32x4{0u, tagA, 0u, tagB}, rws, lane * 16, WS_QF + (cg * 4 + band) * 64);
    STAMP_AT(16);
    const u32x4 xv = __builtin_nontemporal_load((const u32x4*)(x + b * DM + lane * 8)), gv = __builtin_nontemporal_load((const u32x4*)(gw + lane * 8));
    float s = 0.f;
#pragma unroll
    for (int j = 0; j < 4; ++j) { const unsigned p = cvtpk(bflo(xv[j]) * bflo(gv[j]), bfhi(xv[j]) * bfhi(gv[j])); s += bflo(p); s += bfhi(p); }
    s = rbf(wave_sum(s));
    gate = rbf(1.f / (1.f + __expf(-s)));
  }
  {
    wait_time(t0, T_QG);
    u32x4 f[1];
    poll<1, QF_AUX>(f, rws, (lane & 7) * 64, 0, WS_QF + (xg * 4 + hw) * 512, tagA, tagB);
    u32x2 pb[4];
    if (hw == (h & 3)) {
      u32x2 pa[4];
      q_fetch(pa, rws, 0, b, h, lane);
      q_fetch(pb, rws, 1, b, xg * 4 + hw, lane);
      *(LAS unsigned*)(L + L_QA + lane * 4) = q_sum(pa);
      lds_signal(misc + M_QRDY, lane);
      STAMP_AT(17);
    } else q_fetch(pb, rws, 1, b, xg * 4 + hw, lane);
    *(LAS unsigned*)(L + L_QB + hw * 256 + lane * 4) = q_sum(pb);
    lds_wait_ge(misc + M_BDMA, 4);
    if (hw == 0) STAMP_AT(3);
  }
  unsigned zpk = 0;
  __builtin_amdgcn_s_setprio(0);
  hsync(misc, 2, lane);
  {
    u32x4 q[4];
#pragma unroll
    for (int g = 0; g < 4; ++g) q[g] = *(LAS u32x4*)(L + L_QB + g * 256 + c * 16);
    float m[4], l[4], acc[4][8];
#pragma unroll
    for (int g = 0; g < 4; ++g) { m[g] = -INFINITY; l[g] = 0.f;
#pragma unroll
      for (int j = 0; j < 8; ++j) acc[g][j] = 0.f; }
#pragma nounroll
    for (int s = 0; s < BSTEPS; ++s) {
      u32x4 k[4], v[4];
#pragma unroll
      for (int t = 0; t < 4; ++t) { k[t] = *(LAS u32x4*)(L + L_BIG + (hw * 16 + s * 4 + t) * 1024 + lane * 16); v[t] = *(LAS u32x4*)(L + L_BIG + 65536 + (hw * 16 + s * 4 + t) * 1024 + lane * 16); }
      gqa_step(k, v, q, m, l, acc);
    }
#pragma unroll
    for (int g = 0; g < 4; ++g) {
      const float M = xrow_max(m[g]), w = ex2((m[g] - M) * L2E), Ls = xrow_sum(l[g] * w);
#pragma unroll
      for (int j = 0; j < 8; ++j) acc[g][j] *= w;
      LAS float* rec = tmp + (g * 4 + hw) * REC_F;
      *(LAS f32x2*)(rec + 4 + c * 8 + r * 2) = fold_rows(acc[g]);
      if (lane == 0) *(LAS f32x2*)rec = f32x2{M, Ls};
    }
  }
  hsync(misc, 3, lane);
  {
    LAS float* rec = tmp + hw * 4 * REC_F;
    f32x2 ml[4], av[4];
#pragma unroll
    for (int i = 0; i < 4; ++i) { ml[i] = *(LAS f32x2*)(rec + i * REC_F); av[i] = *(LAS f32x2*)(rec + i * REC_F + 4 + lane * 2); }
    const float M = fmaxf(fmaxf(ml[0][0], ml[1][0]), fmaxf(ml[2][0], ml[3][0]));
    float Ls = 0.f, o0 = 0.f, o1 = 0.f;
#pragma unroll
    for (int i = 0; i < 4; ++i) { const float wt = ex2((ml[i][0] - M) * L2E); Ls = fmaf(wt, ml[i][1], Ls); o0 = fmaf(wt, av[i][0], o0); o1 = fmaf(wt, av[i][1], o1); }
    *(LAS unsigned*)(L + L_PUB + hw * 256 + lane * 4) = cvtpk(o0 / Ls, o1 / Ls);
    if (lane == 0) *(LAS f32x2*)(L + L_PUB + 1024 + hw * 8) = f32x2{M, Ls};
  }
  __builtin_amdgcn_s_setprio(2);
  hsync(misc, 4, lane);
  if (hw == 0) {
    const int pi = (b * 8 + xg) * 8 + sp;
    bst<PB_AUX>(*(LAS u32x4*)(L + L_PUB + lane * 16), rws, lane * 16, WS_PBP + pi * 1024);
    const u32x2 ml = *(LAS u32x2*)(L + L_PUB + 1024 + (lane & 3) * 8);
    DRAIN();
    if (lane < 4) bst<PM_AUX>(u32x4{ml[0], tagA, ml[1], tagB}, rws, lane * 16, WS_PBM + pi * 64);
    STAMP_AT(4);
  }
  if (hw == 3) gossip(0, misc, rws, bid, tagA, lane);
  wait_time(t0, T_WO + (b ? WO_LAG : 0) + b * WO_STAG);
  {
    const rsrc_t rw = mk_rsrc(wob, ABL_WSZ);
    u32x4 w[16];
#pragma unroll
    for (int i = 0; i < 16; ++i) { CAPWAIT(WO_CAP + 1); w[i] = bld<AUX_NT>(rw, lane * 16, (h * 128 + side * 64 + (WOI ? (i >> 1) * 8 + hw * 2 + (i & 1) : hw * 16 + i)) * 1024); }
    __builtin_amdgcn_sched_barrier(0);
    u32x4 ml[1];
    poll<1, PM_AUX>(ml, rws, (lane >> 3) * 64, 0, WS_PBM + (b * 8 + xg) * 512 + (h & 3) * 16, tagA, tagB);
    const unsigned av = __builtin_amdgcn_raw_buffer_load_b32(rws, (lane >> 3) * 1024 + (lane & 7) * (WOI ? 16 : 4), WS_PBP + (b * 8 + xg) * 8192 + (h & 3) * 256 + side * 128 + hw * (WOI ? 4 : 32), PB_AUX);
    const float pm = __uint_as_float(ml[0][0]), M = xrow_max(fmaxf(pm, dpp<XOR8>(pm)));
    float Ls = ex2((pm - M) * L2E) * __uint_as_float(ml[0][2]), o0 = Ls * bflo(av), o1 = Ls * bfhi(av);
    Ls = xrow_sum(Ls + dpp<XOR8>(Ls)); o0 = xrow_sum(o0 + dpp<XOR8>(o0)); o1 = xrow_sum(o1 + dpp<XOR8>(o1));
    const unsigned ob = cvtpk(o0 / Ls, o1 / Ls);
    float z[8];
#pragma unroll
    for (int j = 0; j < 8; ++j) z[j] = 0.f;
#pragma unroll
    for (int i = 0; i < 16; i += 2) {
      const unsigned op = __builtin_amdgcn_readlane(ob, i >> 1);
#pragma unroll
      for (int j = 0; j < 4; ++j) { z[2 * j] = dot2(plo(w[i + 1][j], w[i][j]), op, z[2 * j]); z[2 * j + 1] = dot2(phi(w[i + 1][j], w[i][j]), op, z[2 * j + 1]); }
    }
    *(LAS f32x4*)(tmp + hw * 512 + lane * 8) = f32x4{z[0], z[1], z[2], z[3]};
    *(LAS f32x4*)(tmp + hw * 512 + lane * 8 + 4) = f32x4{z[4], z[5], z[6], z[7]};
    hsync(misc, 5, lane);
    f32x2 zs = {0.f, 0.f};
#pragma unroll
    for (int i = 0; i < 4; ++i) zs += *(LAS f32x2*)(tmp + i * 512 + tid * 2);
    zpk = cvtpk(zs[0], zs[1]);
  }
  if (hw == 0) STAMP_AT(5);
  if (hw == 3) gossip(1, misc, rws, bid, tagA, lane);
  constexpr int HS = 5;
  if (T_WOA) wait_time(t0, T_WOA + ((idx & 7) ? WO_LAG : 0) + (idx & 7) * WO_STAG);
  unsigned wa[16][8];
  {
    const rsrc_t rw = mk_rsrc(woa, ABL_WSZ);
    u32x4 w[32];
#pragma unroll
    for (int i = 0; i < 32; ++i) { CAPWAIT(WO_CAP + 1); w[i] = bld<AUX_NT>(rw, lane * 16, (h * 128 + (WOI ? (i >> 1) * 8 + hw * 2 + (i & 1) : hw * 32 + i)) * 1024); }
    __builtin_amdgcn_sched_barrier(0);
#pragma unroll
    for (int i = 0; i < 16; ++i)
#pragma unroll
      for (int j = 0; j < 4; ++j) { wa[i][2 * j] = plo(w[2 * i + 1][j], w[2 * i][j]); wa[i][2 * j + 1] = phi(w[2 * i + 1][j], w[2 * i][j]); asm volatile("" : "+v"(wa[i][2 * j]), "+v"(wa[i][2 * j + 1])); }
  }
  if (hw == 0) STAMP_AT(7);
  if (hw == 3) gossip(2, misc, rws, bid, tagA, lane);
  const u64 t3 = __builtin_amdgcn_s_memrealtime();
  {
    lds_wait_ge(misc + M_KDONE, 4);
    const f32x4 wm = *(LAS f32x4*)(miscf + M_WMAX);
    const float M = fmaxf(fmaxf(wm[0], wm[1]), fmaxf(wm[2], wm[3])), mL = -M * L2E;
    float ls = 0.f;
#pragma unroll
    for (int i = 0; i < 2; ++i) {
      const u32x4 sc = *(LAS u32x4*)(L + L_SC + hw * 2048 + i * 1024 + lane * 16);
#pragma unroll
      for (int j = 0; j < 4; ++j) ls += ex2(fmaf(bflo(sc[j]), L2E, mL)) + ex2(fmaf(bfhi(sc[j]), L2E, mL));
    }
    ls = wave_sum(ls);
    if (lane == 0) miscf[M_LSUM + hw] = ls;
    hsync(misc, HS + 1, lane);
    if (hw == 0) {
      const f32x4 lw = *(LAS f32x4*)(miscf + M_LSUM);
      const float Ls = (lw[0] + lw[1]) + (lw[2] + lw[3]);
      if (lane < 4) bst<PM_AUX>(u32x4{__float_as_uint(M), tagA, __float_as_uint(Ls), tagB}, rws, lane * 16, WS_ST + (b * 64 + jp) * 64);
      wait_time(__builtin_amdgcn_s_memrealtime(), T_ST);
      u32x4 o[1];
      poll<1, PM_AUX>(o, rws, 0, 0, WS_ST + (b * 64 + (jp ^ 1)) * 64, tagA, tagB);
      const float Mo = __uint_as_float(o[0][0]), MM = fmaxf(M, Mo), wt = ex2((M - MM) * L2E);
      if (lane < 4) miscf[M_CNORM + lane] = ex2((miscf[M_WMAX + lane] - MM) * L2E) / (wt * Ls + ex2((Mo - MM) * L2E) * __uint_as_float(o[0][2]));
      if (bid == 0) {
        u32x4 all[4];
        poll<4>(all, rws, lane * 64, 4096, WS_ST, tagA, tagB);
        if (lane == 0) __hip_atomic_store((GAS u64*)(ws + WS_EPOCH), epoch, RLX_AGENT);
      }
      STAMP_AT(6);
    } else if (hw == 3) {
#pragma nounroll
      for (int k = 3; k < NCP && (unsigned)__builtin_amdgcn_readfirstlane(__hip_atomic_load(misc + M_STEP, RLX_WG)) < NS - 4 - T_GOSSIP / TAU; ++k) {
        wait_time(t3, (k - 2) * T_GOSSIP);
        gossip(k, misc, rws, bid, tagA, lane);
      }
    }
  }
  int pass = 1 - DRY;
  asm volatile("" : "+s"(pass));
#pragma nounroll
  for (; pass < 2; ++pass) {
    const rsrc_t rt = mk_rsrc(ws, pass ? WS_BYTES : 0);
    if (pass) { LDS_BARRIER(); if (hw == 1) STAMP_AT(12); }
    {
      const f32x2 av = *(LAS f32x2*)(L + L_REC + r * 512 + (WOI ? c * 32 + hw * 8 : hw * 128 + c * 8));
      const float cn = miscf[M_CNORM + r];
      const unsigned ob = cvtpk(xrow_sum(av[0] * cn), xrow_sum(av[1] * cn));
      float y[8];
#pragma unroll
      for (int j = 0; j < 8; ++j) y[j] = 0.f;
      Proj<0, 16>::run(y, wa, ob);
      *(LAS f32x4*)(tmp + hw * 512 + lane * 8) = f32x4{y[0], y[1], y[2], y[3]};
      *(LAS f32x4*)(tmp + hw * 512 + lane * 8 + 4) = f32x4{y[4], y[5], y[6], y[7]};
    }
    if (pass) LDS_BARRIER();
    asm volatile("" ::: "memory");
    {
      f32x2 ys = {0.f, 0.f};
#pragma unroll
      for (int i = 0; i < 4; ++i) ys += *(LAS f32x2*)(tmp + i * 512 + tid * 2);
      const unsigned ypk = cvtpk(ys[0], ys[1]);
      __builtin_amdgcn_raw_buffer_store_b128(u32x4{plo(zpk, ypk), tagA, phi(zpk, ypk), tagB}, rt, (tid >> 2) * 4096 + (tid & 3) * 16, WS_YG + b * 64 * 4096 + jp * 64, Y_AUX);
    }
    if (hw == 1 && pass) STAMP_AT(13);
    if (hw == 0) {
      u32x4 g[4];
      if (YDELAY) __builtin_amdgcn_s_sleep(YDELAY);
      unsigned np = 0;
#pragma nounroll
      for (unsigned spins = pass ? 0 : SPIN_CAP - 1; spins < SPIN_CAP; ++spins) {
        asm volatile("" ::: "memory");
        bool ok = true;
#pragma unroll
        for (int i = 0; i < 4; ++i) g[i] = bld<AUX_SC1>(rt, lane * 64 + i * 16, WS_YG + (b * 64 + jp) * 4096);
#pragma unroll
        for (int i = 0; i < 4; ++i) ok &= (g[i][1] == tagA) & (g[i][3] == tagB);
        const bool all = __all(ok);
        if (all) break;
        __builtin_amdgcn_s_sleep(YSLEEP);
      }
      float a[8], z[8];
#pragma unroll
      for (int j = 0; j < 8; ++j) { a[j] = bflo(g[j >> 1][(j & 1) * 2]); z[j] = bfhi(g[j >> 1][(j & 1) * 2]); }
      float u[8];
#pragma unroll
      for (int j = 0; j < 8; ++j) u[j] = fold32(a[j], z[j]);
#pragma unroll
      for (int j = 0; j < 4; ++j) u[j] = fold16(u[j], u[j + 4]);
#pragma unroll
      for (int j = 0; j < 2; ++j) u[j] = foldd<XOR8>(u[j], u[j + 2], lane & 8);
      u[0] = foldd<XOR7>(u[0], u[1], lane & 4);
      u[0] += dpp<XOR2>(u[0]); u[0] += dpp<XOR1>(u[0]);
      const float t = rbf(rbf(u[0]) * (lane < 32 ? gate : rbf(1.f - gate)));
      const unsigned res = cvtpk(fold32(t, t), 0.f);
      if (lane < 32 && (lane & 3) == 0) __builtin_amdgcn_raw_buffer_store_b16((u16)res, mk_rsrc(out, pass ? NB * DM * 2 : 0), (lane >> 2) * 2, (b * DM + jp * 8) * 2, 0);
      if (pass) STAMP_AT(14);
    }
  }
}

extern "C" void kernel_launch(void* const* d_in, const int* in_sizes, int n_in, void* d_out, int out_size, void* d_ws, size_t ws_size, hipStream_t stream) {
  k_fused<<<256, 512, 0, stream>>>((const u16*)d_in[0], (const u16*)d_in[1], (const u16*)d_in[2], (const u16*)d_in[3], (const u16*)d_in[4], (const u16*)d_in[6], (const u16*)d_in[7],
                                   (const u16*)d_in[8], (const u16*)d_in[9], (const u16*)d_in[10], (u16*)d_out, (unsigned char*)d_ws);
}
```

```cpp
#include <hip/hip_runtime.h>
#include <hip/hip_bf16.h>
#include <cstdint>

typedef unsigned u32x4 __attribute__((ext_vector_type(4)));
typedef unsigned u32x2 __attribute__((ext_vector_type(2)));
typedef float f32x4 __attribute__((ext_vector_type(4)));
typedef float f32x2 __attribute__((ext_vector_type(2)));
typedef __attribute__((ext_vector_type(2))) __bf16 bf16x2;
typedef unsigned long long u64;
typedef unsigned short u16;
typedef __amdgpu_buffer_rsrc_t rsrc_t;
#define GAS __attribute__((address_space(1)))
#define LAS __attribute__((address_space(3)))
#define RLX_AGENT __ATOMIC_RELAXED, __HIP_MEMORY_SCOPE_AGENT
#define RLX_WG __ATOMIC_RELAXED, __HIP_MEMORY_SCOPE_WORKGROUP
#define DRAIN() asm volatile("s_waitcnt vmcnt(0)" ::: "memory")
#define LDS_BARRIER() do { asm volatile("s_waitcnt lgkmcnt(0)" ::: "memory"); __builtin_amdgcn_s_barrier(); asm volatile("" ::: "memory"); } while (0)
#define CAPWAIT(n) do { __builtin_amdgcn_sched_barrier(0); asm volatile("s_waitcnt vmcnt(%0)" :: "i"((n) - 1) : "memory"); } while (0)

constexpr int NB = 4, DM = 512, NH = 32, SK = 8192, DH = 128, WIN = 2048;
constexpr float SCALE = 0.088388347648318440f, L2E = 1.4426950408889634f;
constexpr int NT = 2, SC1 = 16, SC1NT = 18, RSRC_W3 = 0x00020000;
constexpr int NS = 32;
constexpr unsigned SPIN_CAP = 1u << 18;

constexpr int CAP0 = 5;
constexpr int TAU = 108, TAU_W = 116;
constexpr int W0 = 10, W1 = 32;
constexpr int TK0 = 520;
constexpr int XCENTER = 11, XMIN = 6;
constexpr int XGAIN = 10, XGAIN_END = 30, END_STEPS = 8;
constexpr int T_Q = 540, T_W = 1800, T_ST = 500, T_GOSSIP = 900;
constexpr int T_Y = 7280, Y_MIN = 40, Y_MAX = 72;

constexpr int WS_EPOCH = 0;
constexpr int WS_PH = 4096, NCP = 14;
constexpr int WS_QF = 32768;
constexpr int WS_QP = WS_QF + 16384;
constexpr int WS_PBM = WS_QP + 524288;
constexpr int WS_ST = WS_PBM + 16384;
constexpr int WS_PBP = WS_ST + 16384;
constexpr int WS_YG = WS_PBP + 262144;
constexpr int WS_BYTES = WS_YG + 256 * 4096;

constexpr int L_MISC = 0, L_QA = 256, L_QB = 512, L_REC = 1536, L_SC = 4096, L_TMP = 12288, L_PUB = L_TMP + 8704, L_BIG = 22528, L_BYTES = L_BIG + 131072;
constexpr int REC_F = 132;
enum { M_TAGA = 0, M_TAGB = 1, M_ANCH = 2, M_STEP = 3, M_QRDY = 4, M_HSYNC = 5, M_KDONE = 6, M_BDMA = 7, M_WMAX = 8, M_LSUM = 12, M_CNORM = 16, M_PH = 20 };

__device__ __forceinline__ float dot2(unsigned a, unsigned b, float c) {
  return __builtin_amdgcn_fdot2_f32_bf16(__builtin_bit_cast(bf16x2, a), __builtin_bit_cast(bf16x2, b), c, false);
}
__device__ __forceinline__ unsigned cvtpk(float lo, float hi) { return __builtin_bit_cast(unsigned, __builtin_convertvector(f32x2{lo, hi}, bf16x2)); }
__device__ __forceinline__ float bflo(unsigned u) { return __uint_as_float(u << 16); }
__device__ __forceinline__ float bfhi(unsigned u) { return __uint_as_float(u & 0xffff0000u); }
__device__ __forceinline__ float rbf(float x) { return bflo(cvtpk(x, 0.f)); }
__device__ __forceinline__ float ex(float x) { return __builtin_amdgcn_exp2f(x * L2E); }
__device__ __forceinline__ unsigned plo(unsigned hi, unsigned lo) { return __builtin_amdgcn_perm(hi, lo, 0x05040100u); }
__device__ __forceinline__ unsigned phi(unsigned hi, unsigned lo) { return __builtin_amdgcn_perm(hi, lo, 0x07060302u); }
template <int CTRL> __device__ __forceinline__ float dpp(float x) {
  return __builtin_bit_cast(float, __builtin_amdgcn_mov_dpp(__builtin_bit_cast(int, x), CTRL, 0xf, 0xf, true));
}
constexpr int XOR1 = 0xB1, XOR2 = 0x4E, XOR7 = 0x141, XOR8 = 0x128;
__device__ __forceinline__ float row16_sum(float x) { x += dpp<XOR1>(x); x += dpp<XOR2>(x); x += dpp<XOR7>(x); x += dpp<XOR8>(x); return x; }
__device__ __forceinline__ float row16_max(float x) { x = fmaxf(x, dpp<XOR1>(x)); x = fmaxf(x, dpp<XOR2>(x)); x = fmaxf(x, dpp<XOR7>(x)); return fmaxf(x, dpp<XOR8>(x)); }
template <int CTRL> __device__ __forceinline__ float foldd(float a, float b, bool hi) { return (hi ? b : a) + dpp<CTRL>(hi ? a : b); }
__device__ __forceinline__ float fold32(float x, float y) {
  auto t = __builtin_amdgcn_permlane32_swap(__float_as_uint(x), __float_as_uint(y), false, false);
  return __uint_as_float(t[0]) + __uint_as_float(t[1]);
}
__device__ __forceinline__ float fold16(float x, float y) {
  auto t = __builtin_amdgcn_permlane16_swap(__float_as_uint(x), __float_as_uint(y), false, false);
  return __uint_as_float(t[0]) + __uint_as_float(t[1]);
}
__device__ __forceinline__ float xrow_sum(float x) { x = fold16(x, x); return fold32(x, x); }
__device__ __forceinline__ float xrow_max(float x) {
  auto s = __builtin_amdgcn_permlane16_swap(__float_as_uint(x), __float_as_uint(x), false, false);
  x = fmaxf(__uint_as_float(s[0]), __uint_as_float(s[1]));
  auto t = __builtin_amdgcn_permlane32_swap(__float_as_uint(x), __float_as_uint(x), false, false);
  return fmaxf(__uint_as_float(t[0]), __uint_as_float(t[1]));
}
__device__ __forceinline__ float wave_sum(float x) { return xrow_sum(row16_sum(x)); }
__device__ __forceinline__ f32x2 fold_rows(const float (&a)[8]) {
  const float u0 = fold32(a[0], a[4]), u1 = fold32(a[1], a[5]), u2 = fold32(a[2], a[6]), u3 = fold32(a[3], a[7]);
  return f32x2{fold16(u0, u2), fold16(u1, u3)};
}

__device__ __forceinline__ rsrc_t mk_rsrc(const void* p, int bytes) { return __builtin_amdgcn_make_buffer_rsrc(const_cast<void*>(p), 0, bytes, RSRC_W3); }
template <int AUX> __device__ __forceinline__ u32x4 bld(rsrc_t r, int voff, int soff) { return __builtin_amdgcn_raw_buffer_load_b128(r, voff, soff, AUX); }
template <int AUX> __device__ __forceinline__ u32x2 bld2(rsrc_t r, int voff, int soff) { return __builtin_amdgcn_raw_buffer_load_b64(r, voff, soff, AUX); }
template <int AUX> __device__ __forceinline__ void bst(u32x4 v, rsrc_t r, int voff, int soff) { __builtin_amdgcn_raw_buffer_store_b128(v, r, voff, soff, AUX); }
__device__ __forceinline__ void glds16(const GAS unsigned char* src, unsigned dst) {
  unsigned keep;
  asm volatile("s_mov_b32 %0, m0\n\ts_mov_b32 m0, %2\n\ts_nop 0\n\tglobal_load_lds_dwordx4 %1, off nt\n\ts_mov_b32 m0, %0" : "=&s"(keep) : "v"(src), "s"(dst) : "memory");
}

#define DITH(t) ((((t) & 1) << 2) | ((t) & 2) | (((t) & 4) >> 2))
#define WINWAIT(x, t) do { __builtin_amdgcn_sched_barrier(0); asm volatile("s_cmp_gt_i32 %0, %2\n\ts_cbranch_scc1 2f\n\ts_cmp_gt_i32 %0, %1\n\ts_cbranch_scc1 1f\n\ts_waitcnt vmcnt(3)\n1:\n\ts_waitcnt vmcnt(4)\n" \
    "2:\n\ts_cmp_gt_i32 %0, %3\n\ts_cbranch_scc1 3f\n\ts_waitcnt vmcnt(5)\n3:\n\ts_waitcnt vmcnt(6)" :: "s"(x), "i"(7 - DITH(t)), "i"(15 - DITH(t)), "i"(23 - DITH(t)) : "memory", "scc"); } while (0)

template <int SLEEP> __device__ __forceinline__ void wait_time(u64 t0, unsigned ticks) {
#pragma nounroll
  while (__builtin_amdgcn_s_memrealtime() - t0 < ticks) __builtin_amdgcn_s_sleep(SLEEP);
}
template <int SLEEP> __device__ __forceinline__ void lds_wait_ge(LAS unsigned* p, unsigned target) {
#pragma nounroll
  for (unsigned spins = 0; spins < 32 * SPIN_CAP && (unsigned)__builtin_amdgcn_readfirstlane(__hip_atomic_load(p, RLX_WG)) < target; ++spins) __builtin_amdgcn_s_sleep(SLEEP);
  asm volatile("" ::: "memory");
}
__device__ __forceinline__ void lds_signal(LAS unsigned* p, int lane) {
  asm volatile("s_waitcnt lgkmcnt(0)" ::: "memory");
  if (lane == 0) __builtin_amdgcn_atomic_inc32(p, 0xffffffffu, __ATOMIC_RELAXED, "workgroup");
}
__device__ __forceinline__ void hsync(LAS unsigned* misc, unsigned n, int lane) { lds_signal(misc + M_HSYNC, lane); lds_wait_ge<2>(misc + M_HSYNC, 4 * n); }
template <int N, int AUX> __device__ __forceinline__ void poll(u32x4 (&g)[N], rsrc_t rws, int voff, int stride, int soff, unsigned tagA, unsigned tagB) {
#pragma nounroll
  for (unsigned spins = 0; spins < SPIN_CAP; ++spins) {
    asm volatile("" ::: "memory");
    bool ok = true;
#pragma unroll
    for (int i = 0; i < N; ++i) g[i] = bld<AUX>(rws, voff + i * stride, soff);
#pragma unroll
    for (int i = 0; i < N; ++i) ok &= (g[i][1] == tagA) & (g[i][3] == tagB);
    if (__all(ok)) break;
    __builtin_amdgcn_s_sleep(2);
  }
  __builtin_amdgcn_fence(__ATOMIC_ACQUIRE, "wavefront");
  asm volatile("" ::: "memory");
}
__device__ __forceinline__ void gossip(int k, LAS unsigned* misc, rsrc_t rws, int bid, unsigned tagA, int lane) {
  asm volatile("" ::: "memory");
  const u32x4 wp = *(LAS u32x4*)(misc + M_PH);
  const unsigned ph = wp[0] + (((int)(wp[1] - wp[0]) + (int)(wp[2] - wp[0]) + (int)(wp[3] - wp[0])) >> 2);
  if ((((bid >> 3) + k) & 15) == 0 && lane < 8) __builtin_amdgcn_raw_buffer_store_b64(u32x2{ph, tagA}, rws, lane * 8, WS_PH + k * 2048 + ((bid & 7) + ((bid >> 7) << 3)) * 64, SC1NT);
  if (k > 0) {
    asm volatile("" ::: "memory");
    const u32x2 g = bld2<SC1NT>(rws, (lane & 15) * 64, WS_PH + (k - 1) * 2048);
    const bool ok = g[1] == tagA;
    const float d = wave_sum(ok ? (float)(int)(g[0] - ph) : 0.f), n = wave_sum(ok ? 1.f : 0.f);
    if (n >= 32.f && lane == 0) __hip_atomic_store(misc + M_ANCH, ph + (int)(d / n), RLX_WG);
  }
}

__device__ __forceinline__ void q_partial(const u32x4 (&w)[4], const u32x2 (&xv)[NB], LAS float* part, int lane) {
  float a[NB][8];
#pragma unroll
  for (int j = 0; j < 4; ++j) {
    const unsigned p0 = plo(w[1][j], w[0][j]), p1 = phi(w[1][j], w[0][j]), p2 = plo(w[3][j], w[2][j]), p3 = phi(w[3][j], w[2][j]);
#pragma unroll
    for (int bb = 0; bb < NB; ++bb) { a[bb][2 * j] = dot2(p2, xv[bb][1], dot2(p0, xv[bb][0], 0.f)); a[bb][2 * j + 1] = dot2(p3, xv[bb][1], dot2(p1, xv[bb][0], 0.f)); }
  }
  float f[8];
#pragma unroll
  for (int j = 0; j < 8; ++j) f[j] = fold16(fold32(a[0][j], a[2][j]), fold32(a[1][j], a[3][j]));
#pragma unroll
  for (int j = 0; j < 4; ++j) f[j] = foldd<XOR8>(f[j], f[j + 4], lane & 8);
  *(LAS f32x4*)(part + (lane >> 4) * 64 + (lane & 7) * 8 + ((lane >> 3) & 1) * 4) = f32x4{f[0], f[1], f[2], f[3]};
}
__device__ __forceinline__ void q_fetch(u32x2 (&p)[4], rsrc_t rws, int br, int b, int hq, int lane) {
#pragma unroll
  for (int i = 0; i < 4; ++i) p[i] = bld2<SC1NT>(rws, lane * 8 + i * 16384, WS_QP + br * 262144 + b * 65536 + hq * 512);
}
__device__ __forceinline__ unsigned q_sum(const u32x2 (&p)[4]) {
  const f32x2 s = (__builtin_bit_cast(f32x2, p[0]) + __builtin_bit_cast(f32x2, p[1])) + (__builtin_bit_cast(f32x2, p[2]) + __builtin_bit_cast(f32x2, p[3]));
  return cvtpk(s[0], s[1]);
}
__device__ __forceinline__ void gqa_step(const u32x4 (&k)[4], const u32x4 (&v)[4], const u32x4 (&q)[4], float (&m)[4], float (&l)[4], float (&acc)[4][8]) {
#pragma unroll
  for (int g = 0; g < 4; ++g) {
    float s[4];
#pragma unroll
    for (int t = 0; t < 4; ++t) s[t] = dot2(k[t][3], q[g][3], dot2(k[t][2], q[g][2], dot2(k[t][1], q[g][1], dot2(k[t][0], q[g][0], 0.f))));
#pragma unroll
    for (int t = 0; t < 4; t += 2) {
      const unsigned r1 = cvtpk(row16_sum(s[t]), row16_sum(s[t + 1]));
      const unsigned r2 = cvtpk(bflo(r1) * SCALE, bfhi(r1) * SCALE);
      s[t] = bflo(r2); s[t + 1] = bfhi(r2);
    }
    const float mn = fmaxf(fmaxf(m[g], fmaxf(s[0], s[1])), fmaxf(s[2], s[3]));
    const float al = ex(m[g] - mn);
    m[g] = mn;
    float ls = l[g] * al;
#pragma unroll
    for (int j = 0; j < 8; ++j) acc[g][j] *= al;
#pragma unroll
    for (int t = 0; t < 4; t += 2) {
      const float p0 = ex(s[t] - mn), p1 = ex(s[t + 1] - mn);
      ls += p0 + p1;
      const unsigned w = cvtpk(p0, p1);
#pragma unroll
      for (int j = 0; j < 4; ++j) {
        acc[g][2 * j] = dot2(v[t + 1][j], w >> 16, dot2(v[t][j], w & 0xffffu, acc[g][2 * j]));
        acc[g][2 * j + 1] = dot2(v[t + 1][j], w & 0xffff0000u, dot2(v[t][j], w << 16, acc[g][2 * j + 1]));
      }
    }
    l[g] = ls;
  }
}
template <int C> struct Proj {
  static __device__ __forceinline__ void run(float& y0, float& y1, float& y2, float& y3, const unsigned (&w0)[64], const unsigned (&w1)[64], const unsigned (&ob)[4]) {
#define DOTB(y, o, w) "v_dot2c_f32_bf16_dpp " y ", " o ", " w " row_newbcast:%16 row_mask:0xf bank_mask:0xf\n\t"
    asm volatile(DOTB("%0", "%4", "%8") DOTB("%1", "%4", "%12") DOTB("%2", "%5", "%9") DOTB("%3", "%5", "%13") DOTB("%0", "%6", "%10") DOTB("%1", "%6", "%14") DOTB("%2", "%7", "%11") DOTB("%3", "%7", "%15")
                 : "+v"(y0), "+v"(y1), "+v"(y2), "+v"(y3)
                 : "v"(ob[0]), "v"(ob[1]), "v"(ob[2]), "v"(ob[3]), "v"(w0[4 * C]), "v"(w0[4 * C + 1]), "v"(w0[4 * C + 2]), "v"(w0[4 * C + 3]),
                   "v"(w1[4 * C]), "v"(w1[4 * C + 1]), "v"(w1[4 * C + 2]), "v"(w1[4 * C + 3]), "i"(C));
#undef DOTB
    if constexpr (C + 1 < 16) Proj<C + 1>::run(y0, y1, y2, y3, w0, w1, ob);
  }
};

__global__ void __launch_bounds__(512, 2) k_fused(const u16* __restrict__ x, const u16* __restrict__ ka, const u16* __restrict__ va, const u16* __restrict__ wqa, const u16* __restrict__ woa,
                                                  const u16* __restrict__ kb, const u16* __restrict__ vb, const u16* __restrict__ wqb, const u16* __restrict__ wob,
                                                  const u16* __restrict__ gw, u16* __restrict__ out, unsigned char* __restrict__ ws) {
  __shared__ __attribute__((aligned(1024))) unsigned char smem[L_BYTES];
  LAS unsigned char* L = (LAS unsigned char*)smem;
  LAS unsigned* misc = (LAS unsigned*)(L + L_MISC);
  LAS float* miscf = (LAS float*)(L + L_MISC);
  const unsigned lds0 = (unsigned)(size_t)L;
  const int tid = threadIdx.x, lane = tid & 63, wave = __builtin_amdgcn_readfirstlane(tid >> 6), r = lane >> 4, c = lane & 15;
  const int bid = blockIdx.x, xg = bid & 7, idx = bid >> 3, b = idx & 3, sp = idx >> 2;
  const int side = sp & 1, h = xg * 4 + (idx >> 3), jp = h * 2 + side;
  asm volatile("" :: "s"(x), "s"(ka), "s"(va), "s"(wqa), "s"(woa), "s"(kb), "s"(vb), "s"(wqb), "s"(wob), "s"(gw), "s"(out), "s"(ws));

  if (wave >= 4) {
    __builtin_amdgcn_s_setprio(1);
    const int sw = wave - 4, voff = lane * 16;
    if (tid == 256) {
      const unsigned a0 = (unsigned)__builtin_amdgcn_s_memrealtime() + TK0;
      *(LAS u32x2*)(misc + M_ANCH) = u32x2{a0, 0u}; *(LAS u32x4*)(misc + M_QRDY) = u32x4{0u, 0u, 0u, 0u}; *(LAS u32x4*)(misc + M_PH) = u32x4{a0, a0, a0, a0};
    }
    LDS_BARRIER();
    {
      const size_t o = ((size_t)(b * 8 + xg) * SK + (SK - WIN) + sp * 64 + sw * 16) * DH;
      const GAS unsigned char* pk = (const GAS unsigned char*)(kb + o) + voff;
      const GAS unsigned char* pv = (const GAS unsigned char*)(vb + o) + voff;
#pragma unroll 4
      for (int i = 0; i < 16; ++i) { CAPWAIT(CAP0); glds16(pk + (((i >> 2) + xg) & 3) * 131072 + (i & 3) * 1024, lds0 + L_BIG + sw * 4096 + (i >> 2) * 16384 + (i & 3) * 1024); }
#pragma unroll 4
      for (int i = 0; i < 16; ++i) { CAPWAIT(CAP0); glds16(pv + (((i >> 2) + xg) & 3) * 131072 + (i & 3) * 1024, lds0 + L_BIG + 65536 + sw * 4096 + (i >> 2) * 16384 + (i & 3) * 1024); }
    }
    const u16* kp = ka + (size_t)(b * NH + h) * SK * DH;
    const u16* vp = va + (size_t)(b * NH + h) * SK * DH;
#define SOFF(i) ((((((i) + h) & (NS - 1)) * 2 + side) << 15) + sw * 2048)
#define LOAD(dst, t) dst = bld<NT>(rn, voff + ((t) & 1) * 1024, on + ((t) >> 1) * 8192); __builtin_amdgcn_sched_barrier(0)
    u32x4 d[8], e[8];
    {
      const rsrc_t rn = mk_rsrc(kp, SK * 256);
      int on = SOFF(0);
#pragma unroll
      for (int t = 0; t < 8; ++t) { CAPWAIT(CAP0); LOAD(e[t], t); }
      static_assert(CAP0 <= 8, "the 8 loads above must push the LDS-DMA out of the window");
      lds_signal(misc + M_BDMA, lane);
      on = SOFF(1);
#pragma unroll
      for (int t = 0; t < 8; ++t) { if (t < 6) CAPWAIT(CAP0); LOAD(d[t], t); }
    }
    lds_wait_ge<2>(misc + M_QRDY, 1);
    u32x4 q = *(LAS u32x4*)(L + L_QA + c * 16);
    asm volatile("" : "+v"(q));
    u64 now = __builtin_amdgcn_s_memrealtime();
    unsigned anch = __hip_atomic_load(misc + M_ANCH, RLX_WG), sch = 0;
#define PACE(i)        \
      asm volatile("" : "+s"(now)); \
      sch += ((i) >= W0 && (i) < W1) ? TAU_W : TAU; \
      const unsigned ph = (unsigned)now - sch; \
      const int xw = max(XCENTER + (((int)(ph - __builtin_amdgcn_readfirstlane(anch)) * ((i) >= 2 * NS - END_STEPS ? XGAIN_END : XGAIN)) >> 8), XMIN); \
      now = __builtin_amdgcn_s_memrealtime(); \
      anch = __hip_atomic_load(misc + M_ANCH, RLX_WG); \
      __hip_atomic_store(misc + M_PH + sw, ph, RLX_WG)
    float mx = -INFINITY;
#define QK(v) dot2(v[3], q[3], dot2(v[2], q[2], dot2(v[1], q[1], dot2(v[0], q[0], 0.f))))
#define SCORES(i) \
      _Pragma("unroll") for (int j = 0; j < 4; ++j) s[j] = foldd<XOR7>(s[j], s[j + 4], lane & 4); \
      _Pragma("unroll") for (int j = 0; j < 2; ++j) s[j] = foldd<XOR2>(s[j], s[j + 2], lane & 2); \
      s[0] = foldd<XOR1>(s[0], s[1], lane & 1); \
      s[0] += dpp<XOR8>(s[0]);                                              \
      const unsigned sb = cvtpk(rbf(s[0]) * SCALE, 0.f);                    \
      mx = fmaxf(mx, bflo(sb)); \
      *(LAS u16*)(L + L_SC + (i) * 256 + sw * 64 + r * 16 + (c & 7) * 2) = (u16)sb
    {
      float s[8];
#pragma unroll
      for (int t = 0; t < 8; ++t) s[t] = QK(e[t]);
      SCORES(0);
    }
#pragma nounroll
    for (int i = 1; i < NS; ++i) {
      PACE(i);
      const rsrc_t rn = mk_rsrc(i + 1 < NS ? kp : vp, SK * 256);
      const int on = SOFF(i + 1), xe = i + 1 < NS ? xw : xw + 16;
      float s[8];
#pragma unroll
      for (int t = 0; t < 8; ++t) { s[t] = QK(d[t]); WINWAIT(t < 6 ? xw : xe, t); LOAD(d[t], t); }
      SCORES(i);
    }
    mx = xrow_max(row16_max(mx));
    if (lane == 0) miscf[M_WMAX + sw] = mx;
    lds_signal(misc + M_KDONE, lane);
    float acc[8];
#pragma unroll
    for (int j = 0; j < 8; ++j) acc[j] = 0.f;
    u32x4 scn = *(LAS u32x4*)(L + L_SC + sw * 64 + r * 16);
#pragma nounroll
    for (int i = 0; i < NS; ++i) {
      u32x4 sc = scn;
      asm volatile("" : "+v"(sc));
      PACE(NS + i);
      if (sw == 0) __hip_atomic_store(misc + M_STEP, (unsigned)i, RLX_WG);
      scn = *(LAS u32x4*)(L + L_SC + ((i + 1) & (NS - 1)) * 256 + sw * 64 + r * 16);
      const rsrc_t rn = mk_rsrc(vp, i + 1 < NS ? SK * 256 : 0);
      const int on = SOFF(i + 1), xv = i + 1 < NS ? xw : xw + 99;
#pragma unroll
      for (int t = 0; t < 8; t += 2) {
        const unsigned w = cvtpk(ex(bflo(sc[t >> 1]) - mx), ex(bfhi(sc[t >> 1]) - mx));
#pragma unroll
        for (int j = 0; j < 4; ++j) { acc[2 * j] = dot2(d[t][j], w & 0xffffu, acc[2 * j]); acc[2 * j + 1] = dot2(d[t][j], w << 16, acc[2 * j + 1]); }
        WINWAIT(xv, t); LOAD(d[t], t);
#pragma unroll
        for (int j = 0; j < 4; ++j) { acc[2 * j] = dot2(d[t + 1][j], w >> 16, acc[2 * j]); acc[2 * j + 1] = dot2(d[t + 1][j], w & 0xffff0000u, acc[2 * j + 1]); }
        WINWAIT(xv, t + 1); LOAD(d[t + 1], t + 1);
      }
    }
    asm volatile("" :: "s"(now), "v"(scn), "v"(anch));
    *(LAS f32x2*)(L + L_REC + sw * 512 + c * 32 + r * 8) = fold_rows(acc);
    LDS_BARRIER();
    return;
  }

  __builtin_amdgcn_s_setprio(2);
  const int hw = wave;
  const rsrc_t rws = mk_rsrc(ws, WS_BYTES);
  const u64 t0 = __builtin_amdgcn_s_memrealtime();
  LAS float* tmp = (LAS float*)(L + L_TMP);
  float gate = 0.f;
  u64 epoch = 0;
  const int band = xg >> 1, cg = (xg & 1) * 32 + idx;
  {
    const rsrc_t rx = mk_rsrc(x, NB * DM * 2), rwa = mk_rsrc(wqa, DM * 4096 * 2), rwb = mk_rsrc(wqb, DM * 4096 * 2);
    const int row0 = band * 128 + hw * 32, vo = (lane >> 3) * 4 * 8192 + (lane & 7) * 16;
    u32x4 wA[4], wB[4]; u32x2 xv[NB];
#pragma unroll
    for (int i = 0; i < 4; ++i) wA[i] = bld<NT>(rwa, vo, (row0 + i) * 8192 + cg * 128);
#pragma unroll
    for (int bb = 0; bb < NB; ++bb) xv[bb] = bld2<NT>(rx, (lane >> 3) * 8, (bb * DM + row0) * 2);
    if (hw == 0) epoch = __hip_atomic_load((GAS u64*)(ws + WS_EPOCH), RLX_AGENT);
    __builtin_amdgcn_sched_barrier(0);
#pragma unroll
    for (int i = 0; i < 4; ++i) wB[i] = bld<NT>(rwb, vo, (row0 + i) * 8192 + cg * 128);
    __builtin_amdgcn_sched_barrier(0);
    LDS_BARRIER();
    asm volatile("" : "+v"(wA[0]));
    __builtin_amdgcn_sched_barrier(0);
    q_partial(wA, xv, tmp + hw * 256, lane);
    asm volatile("" : "+v"(epoch));
    epoch += 1;
    if (tid == 0) { misc[M_TAGA] = (unsigned)epoch; misc[M_TAGB] = (unsigned)(epoch >> 32) ^ ((unsigned)epoch * 0x9E3779B1u); }
    q_partial(wB, xv, tmp + 1024 + hw * 256, lane);
  }
  hsync(misc, 1, lane);
  const unsigned tagA = misc[M_TAGA], tagB = misc[M_TAGB];
  if (hw == 0) {
#pragma unroll
    for (int br = 0; br < 2; ++br) {
      LAS float* part = tmp + br * 1024 + lane * 4;
      const f32x4 s = (*(LAS f32x4*)part + *(LAS f32x4*)(part + 256)) + (*(LAS f32x4*)(part + 512) + *(LAS f32x4*)(part + 768));
      bst<SC1NT>(__builtin_bit_cast(u32x4, s), rws, (lane >> 4) * 65536 + (lane & 15) * 16, WS_QP + br * 262144 + band * 16384 + cg * 256);
    }
    DRAIN();
    if (lane < 4) bst<SC1NT>(u32x4{0u, tagA, 0u, tagB}, rws, lane * 16, WS_QF + (cg * 4 + band) * 64);
    const u32x4 xv = __builtin_nontemporal_load((const u32x4*)(x + b * DM + lane * 8)), gv = __builtin_nontemporal_load((const u32x4*)(gw + lane * 8));
    float s = 0.f;
#pragma unroll
    for (int j = 0; j < 4; ++j) { const unsigned p = cvtpk(bflo(xv[j]) * bflo(gv[j]), bfhi(xv[j]) * bfhi(gv[j])); s += bflo(p); s += bfhi(p); }
    s = rbf(wave_sum(s));
    gate = rbf(1.f / (1.f + __expf(-s)));
  }
  {
    wait_time<4>(t0, T_Q);
    u32x4 f[1];
    poll<1, SC1NT>(f, rws, (lane & 7) * 64, 0, WS_QF + (xg * 4 + hw) * 512, tagA, tagB);
    u32x2 pb[4];
    if (hw == (h & 3)) {
      u32x2 pa[4];
      q_fetch(pa, rws, 0, b, h, lane);
      q_fetch(pb, rws, 1, b, xg * 4 + hw, lane);
      *(LAS unsigned*)(L + L_QA + lane * 4) = q_sum(pa);
      lds_signal(misc + M_QRDY, lane);
    } else q_fetch(pb, rws, 1, b, xg * 4 + hw, lane);
    *(LAS unsigned*)(L + L_QB + hw * 256 + lane * 4) = q_sum(pb);
    lds_wait_ge<2>(misc + M_BDMA, 4);
  }
  __builtin_amdgcn_s_setprio(0);
  hsync(misc, 2, lane);
  {
    u32x4 q[4];
#pragma unroll
    for (int g = 0; g < 4; ++g) q[g] = *(LAS u32x4*)(L + L_QB + g * 256 + c * 16);
    float m[4], l[4], acc[4][8];
#pragma unroll
    for (int g = 0; g < 4; ++g) { m[g] = -INFINITY; l[g] = 0.f;
#pragma unroll
      for (int j = 0; j < 8; ++j) acc[g][j] = 0.f; }
#pragma nounroll
    for (int s = 0; s < 4; ++s) {
      u32x4 k[4], v[4];
#pragma unroll
      for (int t = 0; t < 4; ++t) { k[t] = *(LAS u32x4*)(L + L_BIG + (hw * 16 + s * 4 + t) * 1024 + lane * 16); v[t] = *(LAS u32x4*)(L + L_BIG + 65536 + (hw * 16 + s * 4 + t) * 1024 + lane * 16); }
      gqa_step(k, v, q, m, l, acc);
    }
#pragma unroll
    for (int g = 0; g < 4; ++g) {
      const float M = xrow_max(m[g]), w = ex(m[g] - M), Ls = xrow_sum(l[g] * w);
#pragma unroll
      for (int j = 0; j < 8; ++j) acc[g][j] *= w;
      LAS float* rec = tmp + (g * 4 + hw) * REC_F;
      *(LAS f32x2*)(rec + 4 + c * 8 + r * 2) = fold_rows(acc[g]);
      if (lane == 0) *(LAS f32x2*)rec = f32x2{M, Ls};
    }
  }
  hsync(misc, 3, lane);
  {
    LAS float* rec = tmp + hw * 4 * REC_F;
    f32x2 ml[4], av[4];
#pragma unroll
    for (int i = 0; i < 4; ++i) { ml[i] = *(LAS f32x2*)(rec + i * REC_F); av[i] = *(LAS f32x2*)(rec + i * REC_F + 4 + lane * 2); }
    const float M = fmaxf(fmaxf(ml[0][0], ml[1][0]), fmaxf(ml[2][0], ml[3][0]));
    float Ls = 0.f, o0 = 0.f, o1 = 0.f;
#pragma unroll
    for (int i = 0; i < 4; ++i) { const float wt = ex(ml[i][0] - M); Ls = fmaf(wt, ml[i][1], Ls); o0 = fmaf(wt, av[i][0], o0); o1 = fmaf(wt, av[i][1], o1); }
    *(LAS unsigned*)(L + L_PUB + hw * 256 + lane * 4) = cvtpk(o0 / Ls, o1 / Ls);
    if (lane == 0) *(LAS f32x2*)(L + L_PUB + 1024 + hw * 8) = f32x2{M, Ls};
  }
  __builtin_amdgcn_s_setprio(2);
  hsync(misc, 4, lane);
  if (hw == 0) {
    const int pi = (b * 8 + xg) * 8 + sp;
    bst<SC1NT>(*(LAS u32x4*)(L + L_PUB + lane * 16), rws, lane * 16, WS_PBP + pi * 1024);
    const u32x2 ml = *(LAS u32x2*)(L + L_PUB + 1024 + (lane & 3) * 8);
    DRAIN();
    if (lane < 4) bst<SC1NT>(u32x4{ml[0], tagA, ml[1], tagB}, rws, lane * 16, WS_PBM + pi * 64);
  }
  if (hw == 3) gossip(0, misc, rws, bid, tagA, lane);
  wait_time<4>(t0, T_W);
  unsigned zpk;
  {
    const rsrc_t rw = mk_rsrc(wob, 4096 * DM * 2);
    u32x4 w[16];
#pragma unroll
    for (int i = 0; i < 16; ++i) { CAPWAIT(2); w[i] = bld<NT>(rw, lane * 16, (h * 128 + side * 64 + (i >> 1) * 8 + hw * 2 + (i & 1)) * 1024); }
    __builtin_amdgcn_sched_barrier(0);
    u32x4 ml[1];
    poll<1, SC1NT>(ml, rws, (lane >> 3) * 64, 0, WS_PBM + (b * 8 + xg) * 512 + (h & 3) * 16, tagA, tagB);
    const unsigned av = __builtin_amdgcn_raw_buffer_load_b32(rws, (lane >> 3) * 1024 + (lane & 7) * 16, WS_PBP + (b * 8 + xg) * 8192 + (h & 3) * 256 + side * 128 + hw * 4, SC1NT);
    const float pm = __uint_as_float(ml[0][0]), M = xrow_max(fmaxf(pm, dpp<XOR8>(pm)));
    float Ls = ex(pm - M) * __uint_as_float(ml[0][2]), o0 = Ls * bflo(av), o1 = Ls * bfhi(av);
    Ls = xrow_sum(Ls + dpp<XOR8>(Ls)); o0 = xrow_sum(o0 + dpp<XOR8>(o0)); o1 = xrow_sum(o1 + dpp<XOR8>(o1));
    const unsigned ob = cvtpk(o0 / Ls, o1 / Ls);
    float z[8];
#pragma unroll
    for (int j = 0; j < 8; ++j) z[j] = 0.f;
#pragma unroll
    for (int i = 0; i < 16; i += 2) {
      const unsigned op = __builtin_amdgcn_readlane(ob, i >> 1);
#pragma unroll
      for (int j = 0; j < 4; ++j) { z[2 * j] = dot2(plo(w[i + 1][j], w[i][j]), op, z[2 * j]); z[2 * j + 1] = dot2(phi(w[i + 1][j], w[i][j]), op, z[2 * j + 1]); }
    }
    *(LAS f32x4*)(tmp + hw * 512 + lane * 8) = f32x4{z[0], z[1], z[2], z[3]};
    *(LAS f32x4*)(tmp + hw * 512 + lane * 8 + 4) = f32x4{z[4], z[5], z[6], z[7]};
    hsync(misc, 5, lane);
    f32x2 zs = {0.f, 0.f};
#pragma unroll
    for (int i = 0; i < 4; ++i) zs += *(LAS f32x2*)(tmp + i * 512 + tid * 2);
    zpk = cvtpk(zs[0], zs[1]);
    asm volatile("" : "+v"(zpk));
  }
  if (hw == 3) gossip(1, misc, rws, bid, tagA, lane);
  unsigned w0[64], w1[64];
  {
    const GAS unsigned char* src = (const GAS unsigned char*)(woa + (size_t)(h * 128 + hw * 2) * DM) + lane * 16;
#pragma unroll 2
    for (int i = 0; i < 32; ++i) { const int o = (i >> 1) * 8192 + (i & 1) * 1024; CAPWAIT(2); glds16(src + o, lds0 + L_BIG + hw * 2048 + o); }
    DRAIN();
    hsync(misc, 6, lane);
#pragma unroll
    for (int p = 0; p < 64; ++p) {
      const unsigned a = *(LAS unsigned*)(L + L_BIG + p * 2048 + tid * 4), bb = *(LAS unsigned*)(L + L_BIG + p * 2048 + 1024 + tid * 4);
      w0[p] = plo(bb, a); w1[p] = phi(bb, a);
      asm volatile("" : "+v"(w0[p]), "+v"(w1[p]));
    }
  }
  if (hw == 3) gossip(2, misc, rws, bid, tagA, lane);
  const u64 t3 = __builtin_amdgcn_s_memrealtime();
  {
    lds_wait_ge<32>(misc + M_KDONE, 4);
    const f32x4 wm = *(LAS f32x4*)(miscf + M_WMAX);
    const float M = fmaxf(fmaxf(wm[0], wm[1]), fmaxf(wm[2], wm[3]));
    float ls = 0.f;
#pragma unroll
    for (int i = 0; i < 2; ++i) {
      const u32x4 sc = *(LAS u32x4*)(L + L_SC + hw * 2048 + i * 1024 + lane * 16);
#pragma unroll
      for (int j = 0; j < 4; ++j) ls += ex(bflo(sc[j]) - M) + ex(bfhi(sc[j]) - M);
    }
    ls = wave_sum(ls);
    if (lane == 0) miscf[M_LSUM + hw] = ls;
    hsync(misc, 7, lane);
    if (hw == 0) {
      const f32x4 lw = *(LAS f32x4*)(miscf + M_LSUM);
      const float Ls = (lw[0] + lw[1]) + (lw[2] + lw[3]);
      if (lane < 4) bst<SC1NT>(u32x4{__float_as_uint(M), tagA, __float_as_uint(Ls), tagB}, rws, lane * 16, WS_ST + (b * 64 + jp) * 64);
      wait_time<32>(__builtin_amdgcn_s_memrealtime(), T_ST);
      u32x4 o[1];
      poll<1, SC1NT>(o, rws, 0, 0, WS_ST + (b * 64 + (jp ^ 1)) * 64, tagA, tagB);
      const float Mo = __uint_as_float(o[0][0]), MM = fmaxf(M, Mo);
      if (lane < 4) miscf[M_CNORM + lane] = ex(miscf[M_WMAX + lane] - MM) / (ex(M - MM) * Ls + ex(Mo - MM) * __uint_as_float(o[0][2]));
      if (bid == 0) {
        u32x4 all[4];
        wait_time<32>(__builtin_amdgcn_s_memrealtime(), T_ST);
        poll<4, SC1>(all, rws, lane * 64, 4096, WS_ST, tagA, tagB);
        if (lane == 0) __hip_atomic_store((GAS u64*)(ws + WS_EPOCH), epoch, RLX_AGENT);
      }
    } else if (hw == 3) {
#pragma nounroll
      for (int k = 3; k < NCP && (unsigned)__builtin_amdgcn_readfirstlane(__hip_atomic_load(misc + M_STEP, RLX_WG)) < NS - 4 - T_GOSSIP / TAU; ++k) {
        wait_time<32>(t3, (k - 2) * T_GOSSIP);
        gossip(k, misc, rws, bid, tagA, lane);
      }
    }
  }
  int yoff = (tid >> 2) * 4096 + (tid & 3) * 16, goff = lane * 64;
  float gsel = lane < 32 ? gate : rbf(1.f - gate);
  asm volatile("" : "+v"(yoff), "+v"(goff), "+v"(gsel));
  LDS_BARRIER();
  const unsigned yanch = misc[M_ANCH];
  {
    const f32x4 cn = *(LAS f32x4*)(miscf + M_CNORM);
    f32x4 rl[4], rh[4];
#pragma unroll
    for (int w = 0; w < 4; ++w) { rl[w] = *(LAS f32x4*)(L + L_REC + w * 512 + c * 32); rh[w] = *(LAS f32x4*)(L + L_REC + w * 512 + c * 32 + 16); }
    __builtin_amdgcn_sched_barrier(0);
    f32x4 lo = cn[0] * rl[0], hi = cn[0] * rh[0];
#pragma unroll
    for (int w = 1; w < 4; ++w) { lo += cn[w] * rl[w]; hi += cn[w] * rh[w]; }
    unsigned ob[4] = {cvtpk(lo[0], lo[1]), cvtpk(lo[2], lo[3]), cvtpk(hi[0], hi[1]), cvtpk(hi[2], hi[3])};
    float y0 = 0.f, y1 = 0.f, y2 = 0.f, y3 = 0.f;
    asm volatile("s_nop 1" : "+v"(ob[0]), "+v"(ob[1]), "+v"(ob[2]), "+v"(ob[3]), "+v"(y0), "+v"(y1), "+v"(y2), "+v"(y3));
    Proj<0>::run(y0, y1, y2, y3, w0, w1, ob);
    asm volatile("s_nop 2" : "+v"(y0), "+v"(y1), "+v"(y2), "+v"(y3));
    const unsigned ypk = cvtpk(y0 + y2, y1 + y3);
    bst<SC1>(u32x4{plo(zpk, ypk), tagA, phi(zpk, ypk), tagB}, rws, yoff, WS_YG + b * 64 * 4096 + jp * 64);
  }
  if (hw == 0) {
    {
      const u64 tp = __builtin_amdgcn_s_memrealtime();
      const int dl = min(max((int)(__builtin_amdgcn_readfirstlane(yanch) + T_Y - (unsigned)tp), Y_MIN), Y_MAX);
#pragma nounroll
      while ((int)(__builtin_amdgcn_s_memrealtime() - tp) < dl) {}
    }
    u32x4 g[4];
    poll<4, SC1>(g, rws, goff, 16, WS_YG + (b * 64 + jp) * 4096, tagA, tagB);
    float u[8];
#pragma unroll
    for (int j = 0; j < 8; ++j) u[j] = fold32(bflo(g[j >> 1][(j & 1) * 2]), bfhi(g[j >> 1][(j & 1) * 2]));
#pragma unroll
    for (int j = 0; j < 4; ++j) u[j] = fold16(u[j], u[j + 4]);
#pragma unroll
    for (int j = 0; j < 2; ++j) u[j] = foldd<XOR8>(u[j], u[j + 2], lane & 8);
    u[0] = foldd<XOR7>(u[0], u[1], lane & 4);
    u[0] += dpp<XOR2>(u[0]); u[0] += dpp<XOR1>(u[0]);
    const float t = rbf(rbf(u[0]) * gsel);
    const unsigned res = cvtpk(fold32(t, t), 0.f);
    if (lane < 32 && (lane & 3) == 0) out[b * DM + jp * 8 + (lane >> 2)] = (u16)res;
  }
}

extern "C" void kernel_launch(void* const* d_in, const int* in_sizes, int n_in, void* d_out, int out_size, void* d_ws, size_t ws_size, hipStream_t stream) {
  k_fused<<<256, 512, 0, stream>>>((const u16*)d_in[0], (const u16*)d_in[1], (const u16*)d_in[2], (const u16*)d_in[3], (const u16*)d_in[4], (const u16*)d_in[6], (const u16*)d_in[7],
                                   (const u16*)d_in[8], (const u16*)d_in[9], (const u16*)d_in[10], (u16*)d_out, (unsigned char*)d_ws);
}
```

```cpp
#include <hip/hip_runtime.h>
#include <hip/hip_bf16.h>
#include <cstdint>

typedef unsigned u32x4 __attribute__((ext_vector_type(4)));
typedef unsigned u32x2 __attribute__((ext_vector_type(2)));
typedef float f32x4 __attribute__((ext_vector_type(4)));
typedef float f32x2 __attribute__((ext_vector_type(2)));
typedef __attribute__((ext_vector_type(2))) __bf16 bf16x2;
typedef unsigned long long u64;
typedef unsigned short u16;
typedef __amdgpu_buffer_rsrc_t rsrc_t;
#define GAS __attribute__((address_space(1)))
#define LAS __attribute__((address_space(3)))
#define RLX_AGENT __ATOMIC_RELAXED, __HIP_MEMORY_SCOPE_AGENT
#define RLX_WG __ATOMIC_RELAXED, __HIP_MEMORY_SCOPE_WORKGROUP
#define DRAIN() asm volatile("s_waitcnt vmcnt(0)" ::: "memory")
#define LDS_BARRIER() do { asm volatile("s_waitcnt lgkmcnt(0)" ::: "memory"); __builtin_amdgcn_s_barrier(); asm volatile("" ::: "memory"); } while (0)
#define CAPWAIT(n) do { __builtin_amdgcn_sched_barrier(0); asm volatile("s_waitcnt vmcnt(%0)" :: "i"((n) - 1) : "memory"); } while (0)

constexpr int NB = 4, DM = 512, NH = 32, SK = 8192, DH = 128, WIN = 2048;
constexpr float SCALE = 0.088388347648318440f, L2E = 1.4426950408889634f;
constexpr int NT = 2, SC1 = 16, SC1NT = 18, RSRC_W3 = 0x00020000;
constexpr int NS = 32;
constexpr unsigned SPIN_CAP = 1u << 18;

constexpr int CAP0 = 5;
constexpr int TAU_K = 110, TAU_V = 108;
constexpr int TK0 = 710;
constexpr int XCENTER = 11, XMIN = 6, XGAIN = 10;
constexpr int END_STEPS = 8, XCENTER_END = 8, XMIN_END = 2, XGAIN_END = 30;
constexpr int T_Q = 540, T_W = 1850, T_ST = 500;
constexpr int T_CP0 = 4700, T_CP = 500, T_CPLAG = 200, RHO_MIN = -20, RHO_MAX = 60;
constexpr int T_Y = 31 * TAU_K + 33 * TAU_V + 215, Y_MIN = 40, Y_MAX = 72;

constexpr int WS_EPOCH = 0;
constexpr int WS_PH = 4096, NCP = 14;
constexpr int WS_QF = 32768;
constexpr int WS_QP = WS_QF + 16384;
constexpr int WS_PBM = WS_QP + 524288;
constexpr int WS_ST = WS_PBM + 16384;
constexpr int WS_PBP = WS_ST + 16384;
constexpr int WS_YG = WS_PBP + 262144;
constexpr int WS_BYTES = WS_YG + 256 * 4096;

constexpr int L_MISC = 0, L_QA = 256, L_QB = 512, L_REC = 1536, L_SC = 4096, L_TMP = 12288, L_PUB = L_TMP + 8704, L_BIG = 22528, L_BYTES = L_BIG + 131072;
constexpr int REC_F = 132;
enum { M_TAGA = 0, M_TAGB = 1, M_ANCH = 2, M_STEP = 3, M_QRDY = 4, M_HSYNC = 5, M_KDONE = 6, M_BDMA = 7, M_WMAX = 8, M_LSUM = 12, M_CNORM = 16, M_PH = 20 };

__device__ __forceinline__ float dot2(unsigned a, unsigned b, float c) {
  return __builtin_amdgcn_fdot2_f32_bf16(__builtin_bit_cast(bf16x2, a), __builtin_bit_cast(bf16x2, b), c, false);
}
__device__ __forceinline__ unsigned cvtpk(float lo, float hi) { return __builtin_bit_cast(unsigned, __builtin_convertvector(f32x2{lo, hi}, bf16x2)); }
__device__ __forceinline__ float bflo(unsigned u) { return __uint_as_float(u << 16); }
__device__ __forceinline__ float bfhi(unsigned u) { return __uint_as_float(u & 0xffff0000u); }
__device__ __forceinline__ float rbf(float x) { return bflo(cvtpk(x, 0.f)); }
__device__ __forceinline__ float ex(float x) { return __builtin_amdgcn_exp2f(x * L2E); }
__device__ __forceinline__ unsigned plo(unsigned hi, unsigned lo) { return __builtin_amdgcn_perm(hi, lo, 0x05040100u); }
__device__ __forceinline__ unsigned phi(unsigned hi, unsigned lo) { return __builtin_amdgcn_perm(hi, lo, 0x07060302u); }
template <int CTRL> __device__ __forceinline__ float dpp(float x) {
  return __builtin_bit_cast(float, __builtin_amdgcn_mov_dpp(__builtin_bit_cast(int, x), CTRL, 0xf, 0xf, true));
}
constexpr int XOR1 = 0xB1, XOR2 = 0x4E, XOR7 = 0x141, XOR8 = 0x128;
__device__ __forceinline__ float row16_sum(float x) { x += dpp<XOR1>(x); x += dpp<XOR2>(x); x += dpp<XOR7>(x); x += dpp<XOR8>(x); return x; }
__device__ __forceinline__ float row16_max(float x) { x = fmaxf(x, dpp<XOR1>(x)); x = fmaxf(x, dpp<XOR2>(x)); x = fmaxf(x, dpp<XOR7>(x)); return fmaxf(x, dpp<XOR8>(x)); }
template <int CTRL> __device__ __forceinline__ float foldd(float a, float b, bool hi) { return (hi ? b : a) + dpp<CTRL>(hi ? a : b); }
__device__ __forceinline__ float fold32(float x, float y) {
  auto t = __builtin_amdgcn_permlane32_swap(__float_as_uint(x), __float_as_uint(y), false, false);
  return __uint_as_float(t[0]) + __uint_as_float(t[1]);
}
__device__ __forceinline__ float fold16(float x, float y) {
  auto t = __builtin_amdgcn_permlane16_swap(__float_as_uint(x), __float_as_uint(y), false, false);
  return __uint_as_float(t[0]) + __uint_as_float(t[1]);
}
__device__ __forceinline__ float xrow_sum(float x) { x = fold16(x, x); return fold32(x, x); }
__device__ __forceinline__ float xrow_max(float x) {
  auto s = __builtin_amdgcn_permlane16_swap(__float_as_uint(x), __float_as_uint(x), false, false);
  x = fmaxf(__uint_as_float(s[0]), __uint_as_float(s[1]));
  auto t = __builtin_amdgcn_permlane32_swap(__float_as_uint(x), __float_as_uint(x), false, false);
  return fmaxf(__uint_as_float(t[0]), __uint_as_float(t[1]));
}
__device__ __forceinline__ float wave_sum(float x) { return xrow_sum(row16_sum(x)); }
__device__ __forceinline__ f32x2 fold_rows(const float (&a)[8]) {
  const float u0 = fold32(a[0], a[4]), u1 = fold32(a[1], a[5]), u2 = fold32(a[2], a[6]), u3 = fold32(a[3], a[7]);
  return f32x2{fold16(u0, u2), fold16(u1, u3)};
}

__device__ __forceinline__ rsrc_t mk_rsrc(const void* p, int bytes) { return __builtin_amdgcn_make_buffer_rsrc(const_cast<void*>(p), 0, bytes, RSRC_W3); }
template <int AUX> __device__ __forceinline__ u32x4 bld(rsrc_t r, int voff, int soff) { return __builtin_amdgcn_raw_buffer_load_b128(r, voff, soff, AUX); }
template <int AUX> __device__ __forceinline__ u32x2 bld2(rsrc_t r, int voff, int soff) { return __builtin_amdgcn_raw_buffer_load_b64(r, voff, soff, AUX); }
template <int AUX> __device__ __forceinline__ void bst(u32x4 v, rsrc_t r, int voff, int soff) { __builtin_amdgcn_raw_buffer_store_b128(v, r, voff, soff, AUX); }
template <int AUX> __device__ __forceinline__ void glds16(const GAS unsigned char* src, unsigned dst) {
  unsigned keep;
  if (AUX == NT) asm volatile("s_mov_b32 %0, m0\n\ts_mov_b32 m0, %2\n\ts_nop 0\n\tglobal_load_lds_dwordx4 %1, off nt\n\ts_mov_b32 m0, %0" : "=&s"(keep) : "v"(src), "s"(dst) : "memory");
  else asm volatile("s_mov_b32 %0, m0\n\ts_mov_b32 m0, %2\n\ts_nop 0\n\tglobal_load_lds_dwordx4 %1, off sc1\n\ts_mov_b32 m0, %0" : "=&s"(keep) : "v"(src), "s"(dst) : "memory");
}
__device__ __forceinline__ const GAS unsigned char* code_here() { u64 pc; asm volatile("s_getpc_b64 %0" : "=s"(pc)); return (const GAS unsigned char*)(pc & ~127ull); }

#define DITH(t) ((((t) & 1) << 2) | ((t) & 2) | (((t) & 4) >> 2))
#define WINWAIT(x, t) do { __builtin_amdgcn_sched_barrier(0); asm volatile("s_cmp_gt_i32 %0, %2\n\ts_cbranch_scc1 2f\n\ts_cmp_gt_i32 %0, %1\n\ts_cbranch_scc1 1f\n\ts_waitcnt vmcnt(3)\n1:\n\ts_waitcnt vmcnt(4)\n" \
    "2:\n\ts_cmp_gt_i32 %0, %3\n\ts_cbranch_scc1 3f\n\ts_waitcnt vmcnt(5)\n3:\n\ts_waitcnt vmcnt(6)" :: "s"(x), "i"(7 - DITH(t)), "i"(15 - DITH(t)), "i"(23 - DITH(t)) : "memory", "scc"); } while (0)

template <int SLEEP> __device__ __forceinline__ void wait_time(u64 t0, unsigned ticks) {
#pragma nounroll
  while (__builtin_amdgcn_s_memrealtime() - t0 < ticks) __builtin_amdgcn_s_sleep(SLEEP);
}
template <int SLEEP> __device__ __forceinline__ void lds_wait_ge(LAS unsigned* p, unsigned target) {
#pragma nounroll
  for (unsigned spins = 0; spins < 32 * SPIN_CAP && (unsigned)__builtin_amdgcn_readfirstlane(__hip_atomic_load(p, RLX_WG)) < target; ++spins) __builtin_amdgcn_s_sleep(SLEEP);
  asm volatile("" ::: "memory");
}
__device__ __forceinline__ void lds_signal(LAS unsigned* p, int lane) {
  asm volatile("s_waitcnt lgkmcnt(0)" ::: "memory");
  if (lane == 0) __builtin_amdgcn_atomic_inc32(p, 0xffffffffu, __ATOMIC_RELAXED, "workgroup");
}
__device__ __forceinline__ void hsync(LAS unsigned* misc, unsigned n, int lane) { lds_signal(misc + M_HSYNC, lane); lds_wait_ge<2>(misc + M_HSYNC, 4 * n); }
template <int N, int AUX> __device__ __forceinline__ void poll(u32x4 (&g)[N], rsrc_t rws, int voff, int stride, int soff, unsigned tagA, unsigned tagB) {
#pragma nounroll
  for (unsigned spins = 0; spins < SPIN_CAP; ++spins) {
    asm volatile("" ::: "memory");
    bool ok = true;
#pragma unroll
    for (int i = 0; i < N; ++i) g[i] = bld<AUX>(rws, voff + i * stride, soff);
#pragma unroll
    for (int i = 0; i < N; ++i) ok &= (g[i][1] == tagA) & (g[i][3] == tagB);
    if (__all(ok)) break;
    __builtin_amdgcn_s_sleep(2);
  }
  __builtin_amdgcn_fence(__ATOMIC_ACQUIRE, "wavefront");
  asm volatile("" ::: "memory");
}
__device__ __forceinline__ void cp_publish(int k, LAS unsigned* misc, rsrc_t rws, int bid, unsigned tagA, int lane) {
  if ((((bid >> 3) + k) & 15) == 0) {
    asm volatile("" ::: "memory");
    const u32x4 wp = *(LAS u32x4*)(misc + M_PH);
    const unsigned ph = wp[0] + (((int)(wp[1] - wp[0]) + (int)(wp[2] - wp[0]) + (int)(wp[3] - wp[0])) >> 2);
    if (lane < 8) __builtin_amdgcn_raw_buffer_store_b64(u32x2{ph, tagA}, rws, lane * 8, WS_PH + k * 2048 + ((bid & 7) + ((bid >> 7) << 3)) * 64, SC1);
  }
}
__device__ __forceinline__ unsigned cp_mean(int k, rsrc_t rws, unsigned ref, unsigned tagA, int lane) {
  asm volatile("" ::: "memory");
  const u32x2 g = bld2<SC1>(rws, (lane & 15) * 64, WS_PH + k * 2048);
  const bool ok = g[1] == tagA;
  const float d = wave_sum(ok ? (float)(int)(g[0] - ref) : 0.f), n = wave_sum(ok ? 1.f : 0.f);
  return __builtin_amdgcn_readfirstlane(n >= 32.f ? ref + (int)(d / n) : ref);
}
__device__ __forceinline__ void cp_consume(int k, LAS unsigned* misc, rsrc_t rws, unsigned tagA, int lane) {
  const unsigned a = cp_mean(k, rws, __hip_atomic_load(misc + M_ANCH, RLX_WG), tagA, lane);
  if (lane == 0) __hip_atomic_store(misc + M_ANCH, a, RLX_WG);
}

__device__ __forceinline__ void q_partial(const u32x4 (&w)[4], const u32x2 (&xv)[NB], LAS float* part, int lane) {
  float a[NB][8];
#pragma unroll
  for (int j = 0; j < 4; ++j) {
    const unsigned p0 = plo(w[1][j], w[0][j]), p1 = phi(w[1][j], w[0][j]), p2 = plo(w[3][j], w[2][j]), p3 = phi(w[3][j], w[2][j]);
#pragma unroll
    for (int bb = 0; bb < NB; ++bb) { a[bb][2 * j] = dot2(p2, xv[bb][1], dot2(p0, xv[bb][0], 0.f)); a[bb][2 * j + 1] = dot2(p3, xv[bb][1], dot2(p1, xv[bb][0], 0.f)); }
  }
  float f[8];
#pragma unroll
  for (int j = 0; j < 8; ++j) f[j] = fold16(fold32(a[0][j], a[2][j]), fold32(a[1][j], a[3][j]));
#pragma unroll
  for (int j = 0; j < 4; ++j) f[j] = foldd<XOR8>(f[j], f[j + 4], lane & 8);
  *(LAS f32x4*)(part + (lane >> 4) * 64 + (lane & 7) * 8 + ((lane >> 3) & 1) * 4) = f32x4{f[0], f[1], f[2], f[3]};
}
__device__ __forceinline__ void q_fetch(u32x2 (&p)[4], rsrc_t rws, int br, int b, int hq, int lane) {
#pragma unroll
  for (int i = 0; i < 4; ++i) p[i] = bld2<SC1NT>(rws, lane * 8 + i * 16384, WS_QP + br * 262144 + b * 65536 + hq * 512);
}
__device__ __forceinline__ unsigned q_sum(const u32x2 (&p)[4]) {
  const f32x2 s = (__builtin_bit_cast(f32x2, p[0]) + __builtin_bit_cast(f32x2, p[1])) + (__builtin_bit_cast(f32x2, p[2]) + __builtin_bit_cast(f32x2, p[3]));
  return cvtpk(s[0], s[1]);
}
__device__ __forceinline__ void gqa_step(const u32x4 (&k)[4], const u32x4 (&v)[4], const u32x4 (&q)[4], float (&m)[4], float (&l)[4], float (&acc)[4][8]) {
#pragma unroll
  for (int g = 0; g < 4; ++g) {
    float s[4];
#pragma unroll
    for (int t = 0; t < 4; ++t) s[t] = dot2(k[t][3], q[g][3], dot2(k[t][2], q[g][2], dot2(k[t][1], q[g][1], dot2(k[t][0], q[g][0], 0.f))));
#pragma unroll
    for (int t = 0; t < 4; t += 2) {
      const unsigned r1 = cvtpk(row16_sum(s[t]), row16_sum(s[t + 1]));
      const unsigned r2 = cvtpk(bflo(r1) * SCALE, bfhi(r1) * SCALE);
      s[t] = bflo(r2); s[t + 1] = bfhi(r2);
    }
    const float mn = fmaxf(fmaxf(m[g], fmaxf(s[0], s[1])), fmaxf(s[2], s[3]));
    const float al = ex(m[g] - mn);
    m[g] = mn;
    float ls = l[g] * al;
#pragma unroll
    for (int j = 0; j < 8; ++j) acc[g][j] *= al;
#pragma unroll
    for (int t = 0; t < 4; t += 2) {
      const float p0 = ex(s[t] - mn), p1 = ex(s[t + 1] - mn);
      ls += p0 + p1;
      const unsigned w = cvtpk(p0, p1);
#pragma unroll
      for (int j = 0; j < 4; ++j) {
        acc[g][2 * j] = dot2(v[t + 1][j], w >> 16, dot2(v[t][j], w & 0xffffu, acc[g][2 * j]));
        acc[g][2 * j + 1] = dot2(v[t + 1][j], w & 0xffff0000u, dot2(v[t][j], w << 16, acc[g][2 * j + 1]));
      }
    }
    l[g] = ls;
  }
}
template <int C> struct Proj {
  static __device__ __forceinline__ void run(float& y0, float& y1, float& y2, float& y3, const unsigned (&w0)[64], const unsigned (&w1)[64], const unsigned (&ob)[4]) {
#define DOTB(y, o, w) "v_dot2c_f32_bf16_dpp " y ", " o ", " w " row_newbcast:%16 row_mask:0xf bank_mask:0xf\n\t"
    asm volatile(DOTB("%0", "%4", "%8") DOTB("%1", "%4", "%12") DOTB("%2", "%5", "%9") DOTB("%3", "%5", "%13") DOTB("%0", "%6", "%10") DOTB("%1", "%6", "%14") DOTB("%2", "%7", "%11") DOTB("%3", "%7", "%15")
                 : "+v"(y0), "+v"(y1), "+v"(y2), "+v"(y3)
                 : "v"(ob[0]), "v"(ob[1]), "v"(ob[2]), "v"(ob[3]), "v"(w0[4 * C]), "v"(w0[4 * C + 1]), "v"(w0[4 * C + 2]), "v"(w0[4 * C + 3]),
                   "v"(w1[4 * C]), "v"(w1[4 * C + 1]), "v"(w1[4 * C + 2]), "v"(w1[4 * C + 3]), "i"(C));
#undef DOTB
    if constexpr (C + 1 < 16) Proj<C + 1>::run(y0, y1, y2, y3, w0, w1, ob);
  }
};

__global__ void __launch_bounds__(512, 2) k_fused(const u16* __restrict__ x, const u16* __restrict__ ka, const u16* __restrict__ va, const u16* __restrict__ wqa, const u16* __restrict__ woa,
                                                  const u16* __restrict__ kb, const u16* __restrict__ vb, const u16* __restrict__ wqb, const u16* __restrict__ wob,
                                                  const u16* __restrict__ gw, u16* __restrict__ out, unsigned char* __restrict__ ws) {
  __shared__ __attribute__((aligned(1024))) unsigned char smem[L_BYTES];
  LAS unsigned char* L = (LAS unsigned char*)smem;
  LAS unsigned* misc = (LAS unsigned*)(L + L_MISC);
  LAS float* miscf = (LAS float*)(L + L_MISC);
  const unsigned lds0 = (unsigned)(size_t)L;
  const int tid = threadIdx.x, lane = tid & 63, wave = __builtin_amdgcn_readfirstlane(tid >> 6), r = lane >> 4, c = lane & 15;
  const int bid = blockIdx.x, xg = bid & 7, idx = bid >> 3, b = idx & 3, sp = idx >> 2;
  const int side = sp & 1, h = xg * 4 + (idx >> 3), jp = h * 2 + side;
  asm volatile("" :: "s"(x), "s"(ka), "s"(va), "s"(wqa), "s"(woa), "s"(kb), "s"(vb), "s"(wqb), "s"(wob), "s"(gw), "s"(out), "s"(ws));

  if (wave >= 4) {
    __builtin_amdgcn_s_setprio(1);
    const int sw = wave - 4, voff = lane * 16;
    if (tid == 256) {
      const unsigned a0 = (unsigned)__builtin_amdgcn_s_memrealtime() + TK0;
      *(LAS u32x2*)(misc + M_ANCH) = u32x2{a0, 0u}; *(LAS u32x4*)(misc + M_QRDY) = u32x4{0u, 0u, 0u, 0u}; *(LAS u32x4*)(misc + M_PH) = u32x4{a0, a0, a0, a0};
    }
    u32x4 junk = {0u, 0u, 0u, 0u}; unsigned junk1 = 0;
    if (sw == 3 && idx < 4) asm volatile("global_load_dword %0, %1, off" : "+v"(junk1) : "v"(code_here() + (lane & 31) * 128) : "memory");
    LDS_BARRIER();
    {
      const size_t o = ((size_t)(b * 8 + xg) * SK + (SK - WIN) + sp * 64 + sw * 16) * DH;
      const GAS unsigned char* pk = (const GAS unsigned char*)(kb + o) + voff;
      const GAS unsigned char* pv = (const GAS unsigned char*)(vb + o) + voff;
#pragma unroll 4
      for (int i = 0; i < 16; ++i) { CAPWAIT(CAP0); glds16<NT>(pk + (((i >> 2) + xg) & 3) * 131072 + (i & 3) * 1024, lds0 + L_BIG + sw * 4096 + (i >> 2) * 16384 + (i & 3) * 1024); }
#pragma unroll 4
      for (int i = 0; i < 16; ++i) { CAPWAIT(CAP0); glds16<NT>(pv + (((i >> 2) + xg) & 3) * 131072 + (i & 3) * 1024, lds0 + L_BIG + 65536 + sw * 4096 + (i >> 2) * 16384 + (i & 3) * 1024); }
    }
    {
      const size_t o = (size_t)(h * 128 + (side * 4 + b) * 16 + sw * 4) * DM;
#pragma unroll
      for (int i = 0; i < 8; ++i) {
        const GAS unsigned char* p = (const GAS unsigned char*)((i < 4 ? wob : woa) + o) + (i & 3) * 1024;
        CAPWAIT(i < 4 ? CAP0 + i : 7);
        asm volatile("global_load_dwordx4 %0, %2, off nt\n\tglobal_load_dword %1, %3, off sc1" : "+v"(junk), "+v"(junk1) : "v"(p + voff), "v"(p + (lane & 7) * 128) : "memory");
      }
    }
    lds_signal(misc + M_BDMA, lane);
    const u16* kp = ka + (size_t)(b * NH + h) * SK * DH;
    const u16* vp = va + (size_t)(b * NH + h) * SK * DH;
#define SOFF(i) ((((((i) + h) & (NS - 1)) * 2 + side) << 15) + sw * 2048)
#define LOAD(dst, t) dst = bld<NT>(rn, voff + ((t) & 1) * 1024, on + ((t) >> 1) * 8192); __builtin_amdgcn_sched_barrier(0)
    u32x4 d[8], e[8];
    {
      const rsrc_t rn = mk_rsrc(kp, SK * 256);
      int on = SOFF(0);
#pragma unroll
      for (int t = 0; t < 8; ++t) { CAPWAIT(t < 4 ? CAP0 + 4 - t : CAP0); LOAD(e[t], t); }
      static_assert(CAP0 <= 8, "the 8 loads above must push the dropped loads out of the window");
      asm volatile("" :: "v"(junk), "v"(junk1));
      on = SOFF(1);
#pragma unroll
      for (int t = 0; t < 8; ++t) { if (t < 6) CAPWAIT(CAP0); LOAD(d[t], t); }
    }
    lds_wait_ge<2>(misc + M_QRDY, 1);
    u32x4 q = *(LAS u32x4*)(L + L_QA + c * 16);
    asm volatile("" : "+v"(q));
    u64 now = __builtin_amdgcn_s_memrealtime();
    unsigned anch = __hip_atomic_load(misc + M_ANCH, RLX_WG), sch = 0;
#define PACE(i)        \
      asm volatile("" : "+s"(now)); \
      sch += (i) < NS ? TAU_K : TAU_V; \
      const unsigned ph = (unsigned)now - sch; \
      const bool eg = (i) >= 2 * NS - END_STEPS; \
      const int xw = max((eg ? XCENTER_END : XCENTER) + (((int)(ph - __builtin_amdgcn_readfirstlane(anch)) * (eg ? XGAIN_END : XGAIN)) >> 8), eg ? XMIN_END : XMIN); \
      now = __builtin_amdgcn_s_memrealtime(); \
      anch = __hip_atomic_load(misc + M_ANCH, RLX_WG); \
      __hip_atomic_store(misc + M_PH + sw, ph, RLX_WG)
    float mx = -INFINITY;
#define QK(v) dot2(v[3], q[3], dot2(v[2], q[2], dot2(v[1], q[1], dot2(v[0], q[0], 0.f))))
#define SCORES(i) \
      _Pragma("unroll") for (int j = 0; j < 4; ++j) s[j] = foldd<XOR7>(s[j], s[j + 4], lane & 4); \
      _Pragma("unroll") for (int j = 0; j < 2; ++j) s[j] = foldd<XOR2>(s[j], s[j + 2], lane & 2); \
      s[0] = foldd<XOR1>(s[0], s[1], lane & 1); \
      s[0] += dpp<XOR8>(s[0]);                                              \
      const unsigned sb = cvtpk(rbf(s[0]) * SCALE, 0.f);                    \
      mx = fmaxf(mx, bflo(sb)); \
      *(LAS u16*)(L + L_SC + (i) * 256 + sw * 64 + r * 16 + (c & 7) * 2) = (u16)sb
    {
      float s[8];
#pragma unroll
      for (int t = 0; t < 8; ++t) s[t] = QK(e[t]);
      SCORES(0);
    }
#pragma nounroll
    for (int i = 1; i < NS; ++i) {
      PACE(i);
      const rsrc_t rn = mk_rsrc(i + 1 < NS ? kp : vp, SK * 256);
      const int on = SOFF(i + 1), xe = i + 1 < NS ? xw : xw + 16;
      float s[8];
#pragma unroll
      for (int t = 0; t < 8; ++t) { s[t] = QK(d[t]); WINWAIT(t < 6 ? xw : xe, t); LOAD(d[t], t); }
      SCORES(i);
    }
    mx = xrow_max(row16_max(mx));
    if (lane == 0) miscf[M_WMAX + sw] = mx;
    lds_signal(misc + M_KDONE, lane);
    float acc[8];
#pragma unroll
    for (int j = 0; j < 8; ++j) acc[j] = 0.f;
    u32x4 scn = *(LAS u32x4*)(L + L_SC + sw * 64 + r * 16);
#pragma nounroll
    for (int i = 0; i < NS; ++i) {
      u32x4 sc = scn;
      asm volatile("" : "+v"(sc));
      PACE(NS + i);
      if (sw == 0) __hip_atomic_store(misc + M_STEP, (unsigned)i, RLX_WG);
      scn = *(LAS u32x4*)(L + L_SC + ((i + 1) & (NS - 1)) * 256 + sw * 64 + r * 16);
      const rsrc_t rn = mk_rsrc(vp, i + 1 < NS ? SK * 256 : 0);
      const int on = SOFF(i + 1), xv = i + 1 < NS ? xw : xw + 99;
#pragma unroll
      for (int t = 0; t < 8; t += 2) {
        const unsigned w = cvtpk(ex(bflo(sc[t >> 1]) - mx), ex(bfhi(sc[t >> 1]) - mx));
#pragma unroll
        for (int j = 0; j < 4; ++j) { acc[2 * j] = dot2(d[t][j], w & 0xffffu, acc[2 * j]); acc[2 * j + 1] = dot2(d[t][j], w << 16, acc[2 * j + 1]); }
        WINWAIT(xv, t); LOAD(d[t], t);
#pragma unroll
        for (int j = 0; j < 4; ++j) { acc[2 * j] = dot2(d[t + 1][j], w >> 16, acc[2 * j]); acc[2 * j + 1] = dot2(d[t + 1][j], w & 0xffff0000u, acc[2 * j + 1]); }
        WINWAIT(xv, t + 1); LOAD(d[t + 1], t + 1);
      }
    }
    asm volatile("" :: "s"(now), "v"(scn), "v"(anch));
    *(LAS f32x2*)(L + L_REC + sw * 512 + c * 32 + r * 8) = fold_rows(acc);
    LDS_BARRIER();
    return;
  }

  __builtin_amdgcn_s_setprio(2);
  const int hw = wave;
  const rsrc_t rws = mk_rsrc(ws, WS_BYTES);
  const u64 t0 = __builtin_amdgcn_s_memrealtime();
  LAS float* tmp = (LAS float*)(L + L_TMP);
  float gate = 0.f;
  u64 epoch = 0;
  const int band = xg >> 1, cg = (xg & 1) * 32 + idx;
  {
    const rsrc_t rx = mk_rsrc(x, NB * DM * 2), rwa = mk_rsrc(wqa, DM * 4096 * 2), rwb = mk_rsrc(wqb, DM * 4096 * 2);
    const int row0 = band * 128 + hw * 32, vo = (lane >> 3) * 4 * 8192 + (lane & 7) * 16;
    u32x4 wA[4], wB[4]; u32x2 xv[NB];
#pragma unroll
    for (int i = 0; i < 4; ++i) wA[i] = bld<NT>(rwa, vo, (row0 + i) * 8192 + cg * 128);
#pragma unroll
    for (int bb = 0; bb < NB; ++bb) xv[bb] = bld2<0>(rx, (lane >> 3) * 8, (bb * DM + row0) * 2);
    if (hw == 0) epoch = __hip_atomic_load((GAS u64*)(ws + WS_EPOCH), RLX_AGENT);
    __builtin_amdgcn_sched_barrier(0);
#pragma unroll
    for (int i = 0; i < 4; ++i) wB[i] = bld<NT>(rwb, vo, (row0 + i) * 8192 + cg * 128);
    __builtin_amdgcn_sched_barrier(0);
    LDS_BARRIER();
    asm volatile("" : "+v"(wA[0]));
    __builtin_amdgcn_sched_barrier(0);
    q_partial(wA, xv, tmp + hw * 256, lane);
    asm volatile("" : "+v"(epoch));
    epoch += 1;
    if (tid == 0) { misc[M_TAGA] = (unsigned)epoch; misc[M_TAGB] = (unsigned)(epoch >> 32) ^ ((unsigned)epoch * 0x9E3779B1u); }
    q_partial(wB, xv, tmp + 1024 + hw * 256, lane);
  }
  hsync(misc, 1, lane);
  const unsigned tagA = misc[M_TAGA], tagB = misc[M_TAGB];
  if (hw == 0) {
#pragma unroll
    for (int br = 0; br < 2; ++br) {
      LAS float* part = tmp + br * 1024 + lane * 4;
      const f32x4 s = (*(LAS f32x4*)part + *(LAS f32x4*)(part + 256)) + (*(LAS f32x4*)(part + 512) + *(LAS f32x4*)(part + 768));
      bst<SC1NT>(__builtin_bit_cast(u32x4, s), rws, (lane >> 4) * 65536 + (lane & 15) * 16, WS_QP + br * 262144 + band * 16384 + cg * 256);
    }
    DRAIN();
    if (lane < 4) bst<SC1>(u32x4{0u, tagA, 0u, tagB}, rws, lane * 16, WS_QF + (cg * 4 + band) * 64);
    const u32x4 xv = bld<0>(mk_rsrc(x, NB * DM * 2), lane * 16, b * DM * 2), gv = bld<0>(mk_rsrc(gw, DM * 2), lane * 16, 0);
    float s = 0.f;
#pragma unroll
    for (int j = 0; j < 4; ++j) { const unsigned p = cvtpk(bflo(xv[j]) * bflo(gv[j]), bfhi(xv[j]) * bfhi(gv[j])); s += bflo(p); s += bfhi(p); }
    s = rbf(wave_sum(s));
    gate = rbf(1.f / (1.f + __expf(-s)));
  }
  {
    wait_time<4>(t0, T_Q);
    u32x4 f[1];
    poll<1, SC1>(f, rws, (lane & 7) * 64, 0, WS_QF + (xg * 4 + hw) * 512, tagA, tagB);
    u32x2 pb[4];
    if (hw == (h & 3)) {
      u32x2 pa[4];
      q_fetch(pa, rws, 0, b, h, lane);
      q_fetch(pb, rws, 1, b, xg * 4 + hw, lane);
      *(LAS unsigned*)(L + L_QA + lane * 4) = q_sum(pa);
      lds_signal(misc + M_QRDY, lane);
    } else q_fetch(pb, rws, 1, b, xg * 4 + hw, lane);
    *(LAS unsigned*)(L + L_QB + hw * 256 + lane * 4) = q_sum(pb);
    lds_wait_ge<2>(misc + M_BDMA, 4);
  }
  __builtin_amdgcn_s_setprio(0);
  hsync(misc, 2, lane);
  {
    u32x4 q[4];
#pragma unroll
    for (int g = 0; g < 4; ++g) q[g] = *(LAS u32x4*)(L + L_QB + g * 256 + c * 16);
    float m[4], l[4], acc[4][8];
#pragma unroll
    for (int g = 0; g < 4; ++g) { m[g] = -INFINITY; l[g] = 0.f;
#pragma unroll
      for (int j = 0; j < 8; ++j) acc[g][j] = 0.f; }
#pragma nounroll
    for (int s = 0; s < 4; ++s) {
      u32x4 k[4], v[4];
#pragma unroll
      for (int t = 0; t < 4; ++t) { k[t] = *(LAS u32x4*)(L + L_BIG + (hw * 16 + s * 4 + t) * 1024 + lane * 16); v[t] = *(LAS u32x4*)(L + L_BIG + 65536 + (hw * 16 + s * 4 + t) * 1024 + lane * 16); }
      gqa_step(k, v, q, m, l, acc);
    }
#pragma unroll
    for (int g = 0; g < 4; ++g) {
      const float M = xrow_max(m[g]), w = ex(m[g] - M), Ls = xrow_sum(l[g] * w);
#pragma unroll
      for (int j = 0; j < 8; ++j) acc[g][j] *= w;
      LAS float* rec = tmp + (g * 4 + hw) * REC_F;
      *(LAS f32x2*)(rec + 4 + c * 8 + r * 2) = fold_rows(acc[g]);
      if (lane == 0) *(LAS f32x2*)rec = f32x2{M, Ls};
    }
  }
  hsync(misc, 3, lane);
  {
    LAS float* rec = tmp + hw * 4 * REC_F;
    f32x2 ml[4], av[4];
#pragma unroll
    for (int i = 0; i < 4; ++i) { ml[i] = *(LAS f32x2*)(rec + i * REC_F); av[i] = *(LAS f32x2*)(rec + i * REC_F + 4 + lane * 2); }
    const float M = fmaxf(fmaxf(ml[0][0], ml[1][0]), fmaxf(ml[2][0], ml[3][0]));
    float Ls = 0.f, o0 = 0.f, o1 = 0.f;
#pragma unroll
    for (int i = 0; i < 4; ++i) { const float wt = ex(ml[i][0] - M); Ls = fmaf(wt, ml[i][1], Ls); o0 = fmaf(wt, av[i][0], o0); o1 = fmaf(wt, av[i][1], o1); }
    *(LAS unsigned*)(L + L_PUB + hw * 256 + lane * 4) = cvtpk(o0 / Ls, o1 / Ls);
    if (lane == 0) *(LAS f32x2*)(L + L_PUB + 1024 + hw * 8) = f32x2{M, Ls};
  }
  __builtin_amdgcn_s_setprio(2);
  hsync(misc, 4, lane);
  if (hw == 0) {
    const int pi = (b * 8 + xg) * 8 + sp;
    bst<SC1NT>(*(LAS u32x4*)(L + L_PUB + lane * 16), rws, lane * 16, WS_PBP + pi * 1024);
    const u32x2 ml = *(LAS u32x2*)(L + L_PUB + 1024 + (lane & 3) * 8);
    DRAIN();
    if (lane < 4) bst<SC1NT>(u32x4{ml[0], tagA, ml[1], tagB}, rws, lane * 16, WS_PBM + pi * 64);
  }
  if (hw == 3) cp_publish(0, misc, rws, bid, tagA, lane);
  wait_time<4>(t0, T_W);
  if (hw == 3) cp_consume(0, misc, rws, tagA, lane);
  unsigned zpk;
  {
    const rsrc_t rw = mk_rsrc(wob, 4096 * DM * 2);
    u32x4 w[16];
#pragma unroll
    for (int i = 0; i < 16; ++i) { CAPWAIT(2); w[i] = bld<SC1>(rw, lane * 16, (h * 128 + side * 64 + (i >> 1) * 8 + hw * 2 + (i & 1)) * 1024); }
    __builtin_amdgcn_sched_barrier(0);
    u32x4 ml[1];
    poll<1, SC1>(ml, rws, (lane >> 3) * 64, 0, WS_PBM + (b * 8 + xg) * 512 + (h & 3) * 16, tagA, tagB);
    const unsigned av = __builtin_amdgcn_raw_buffer_load_b32(rws, (lane >> 3) * 1024 + (lane & 7) * 16, WS_PBP + (b * 8 + xg) * 8192 + (h & 3) * 256 + side * 128 + hw * 4, SC1NT);
    const float pm = __uint_as_float(ml[0][0]), M = xrow_max(fmaxf(pm, dpp<XOR8>(pm)));
    float Ls = ex(pm - M) * __uint_as_float(ml[0][2]), o0 = Ls * bflo(av), o1 = Ls * bfhi(av);
    Ls = xrow_sum(Ls + dpp<XOR8>(Ls)); o0 = xrow_sum(o0 + dpp<XOR8>(o0)); o1 = xrow_sum(o1 + dpp<XOR8>(o1));
    const unsigned ob = cvtpk(o0 / Ls, o1 / Ls);
    float z[8];
#pragma unroll
    for (int j = 0; j < 8; ++j) z[j] = 0.f;
#pragma unroll
    for (int i = 0; i < 16; i += 2) {
      const unsigned op = __builtin_amdgcn_readlane(ob, i >> 1);
#pragma unroll
      for (int j = 0; j < 4; ++j) { z[2 * j] = dot2(plo(w[i + 1][j], w[i][j]), op, z[2 * j]); z[2 * j + 1] = dot2(phi(w[i + 1][j], w[i][j]), op, z[2 * j + 1]); }
    }
    *(LAS f32x4*)(tmp + hw * 512 + lane * 8) = f32x4{z[0], z[1], z[2], z[3]};
    *(LAS f32x4*)(tmp + hw * 512 + lane * 8 + 4) = f32x4{z[4], z[5], z[6], z[7]};
    hsync(misc, 5, lane);
    f32x2 zs = {0.f, 0.f};
#pragma unroll
    for (int i = 0; i < 4; ++i) zs += *(LAS f32x2*)(tmp + i * 512 + tid * 2);
    zpk = cvtpk(zs[0], zs[1]);
    asm volatile("" : "+v"(zpk));
  }
  if (hw == 3) cp_publish(1, misc, rws, bid, tagA, lane);
  unsigned w0[64], w1[64];
  {
    const GAS unsigned char* src = (const GAS unsigned char*)(woa + (size_t)(h * 128 + hw * 2) * DM) + lane * 16;
#pragma nounroll
    for (int i0 = 0; i0 < 32; i0 += 8) {
#pragma unroll
      for (int i = i0; i < i0 + 8; ++i) { const int o = (i >> 1) * 8192 + (i & 1) * 1024; CAPWAIT(2); glds16<SC1>(src + o, lds0 + L_BIG + hw * 2048 + o); }
      if (hw == 3) { if (i0 == 0) cp_consume(1, misc, rws, tagA, lane); else if (i0 == 8) cp_publish(2, misc, rws, bid, tagA, lane); else if (i0 == 16) cp_consume(2, misc, rws, tagA, lane); }
    }
    DRAIN();
    hsync(misc, 6, lane);
#pragma unroll
    for (int p = 0; p < 64; ++p) {
      const unsigned a = *(LAS unsigned*)(L + L_BIG + p * 2048 + tid * 4), bb = *(LAS unsigned*)(L + L_BIG + p * 2048 + 1024 + tid * 4);
      w0[p] = plo(bb, a); w1[p] = phi(bb, a);
      asm volatile("" : "+v"(w0[p]), "+v"(w1[p]));
    }
  }
  {
    lds_wait_ge<32>(misc + M_KDONE, 4);
    const f32x4 wm = *(LAS f32x4*)(miscf + M_WMAX);
    const float M = fmaxf(fmaxf(wm[0], wm[1]), fmaxf(wm[2], wm[3]));
    float ls = 0.f;
#pragma unroll
    for (int i = 0; i < 2; ++i) {
      const u32x4 sc = *(LAS u32x4*)(L + L_SC + hw * 2048 + i * 1024 + lane * 16);
#pragma unroll
      for (int j = 0; j < 4; ++j) ls += ex(bflo(sc[j]) - M) + ex(bfhi(sc[j]) - M);
    }
    ls = wave_sum(ls);
    if (lane == 0) miscf[M_LSUM + hw] = ls;
    hsync(misc, 7, lane);
    if (hw == 0) {
      const f32x4 lw = *(LAS f32x4*)(miscf + M_LSUM);
      const float Ls = (lw[0] + lw[1]) + (lw[2] + lw[3]);
      if (lane < 4) bst<SC1NT>(u32x4{__float_as_uint(M), tagA, __float_as_uint(Ls), tagB}, rws, lane * 16, WS_ST + (b * 64 + jp) * 64);
      wait_time<32>(__builtin_amdgcn_s_memrealtime(), T_ST);
      u32x4 o[1];
      poll<1, SC1NT>(o, rws, 0, 0, WS_ST + (b * 64 + (jp ^ 1)) * 64, tagA, tagB);
      const float Mo = __uint_as_float(o[0][0]), MM = fmaxf(M, Mo);
      if (lane < 4) miscf[M_CNORM + lane] = ex(miscf[M_WMAX + lane] - MM) / (ex(M - MM) * Ls + ex(Mo - MM) * __uint_as_float(o[0][2]));
      if (bid == 0) {
        u32x4 all[4];
        wait_time<32>(__builtin_amdgcn_s_memrealtime(), T_ST);
        poll<4, SC1>(all, rws, lane * 64, 4096, WS_ST, tagA, tagB);
        if (lane == 0) __hip_atomic_store((GAS u64*)(ws + WS_EPOCH), epoch, RLX_AGENT);
      }
    } else if (hw == 3) {
      int k = 3, state = 0, rho = 0, rprev = RHO_MAX, have = 0, TP = 0;
      unsigned P = __builtin_amdgcn_readfirstlane(__hip_atomic_load(misc + M_ANCH, RLX_WG));
#pragma nounroll
      for (unsigned spins = 0; spins < SPIN_CAP && (unsigned)__builtin_amdgcn_readfirstlane(__hip_atomic_load(misc + M_STEP, RLX_WG)) < NS - 3; ++spins) {
        const int t = (int)(__builtin_amdgcn_s_memrealtime() - t0), tk = T_CP0 + (k - 3) * T_CP;
        if (state == 0 && t >= tk && k < NCP) { if (t < tk + T_CPLAG / 2) cp_publish(k, misc, rws, bid, tagA, lane); state = 1; }
        else if (state == 1 && t >= tk + T_CPLAG) {
          const unsigned Pe = P + (((tk - TP) * rho) >> 10), Pn = cp_mean(k, rws, Pe, tagA, lane);
          if (have) { const int rn = min(max((int)(Pn - P) * 1024 / T_CP, RHO_MIN), RHO_MAX); rho = min(rn, rprev); rprev = rn; }
          have = have || Pn != Pe; P = Pn; TP = tk; state = 0; ++k;
        }
        if (lane == 0) __hip_atomic_store(misc + M_ANCH, P + (((t - TP) * rho) >> 10), RLX_WG);
        __builtin_amdgcn_s_sleep(16);
      }
    }
  }
  int yoff = (tid >> 2) * 4096 + (tid & 3) * 16, goff = lane * 64;
  float gsel = lane < 32 ? gate : rbf(1.f - gate);
  asm volatile("" : "+v"(yoff), "+v"(goff), "+v"(gsel));
  if (hw == 3) {
    const unsigned pf = *(const GAS unsigned*)(code_here() + (lane & 31) * 128);
    asm volatile("" :: "v"(pf));
  }
  LDS_BARRIER();
  const unsigned yanch = misc[M_ANCH];
  {
    const f32x4 cn = *(LAS f32x4*)(miscf + M_CNORM);
    f32x4 rl[4], rh[4];
#pragma unroll
    for (int w = 0; w < 4; ++w) { rl[w] = *(LAS f32x4*)(L + L_REC + w * 512 + c * 32); rh[w] = *(LAS f32x4*)(L + L_REC + w * 512 + c * 32 + 16); }
    __builtin_amdgcn_sched_barrier(0);
    f32x4 lo = cn[0] * rl[0], hi = cn[0] * rh[0];
#pragma unroll
    for (int w = 1; w < 4; ++w) { lo += cn[w] * rl[w]; hi += cn[w] * rh[w]; }
    unsigned ob[4] = {cvtpk(lo[0], lo[1]), cvtpk(lo[2], lo[3]), cvtpk(hi[0], hi[1]), cvtpk(hi[2], hi[3])};
    float y0 = 0.f, y1 = 0.f, y2 = 0.f, y3 = 0.f;
    asm volatile("s_nop 1" : "+v"(ob[0]), "+v"(ob[1]), "+v"(ob[2]), "+v"(ob[3]), "+v"(y0), "+v"(y1), "+v"(y2), "+v"(y3));
    Proj<0>::run(y0, y1, y2, y3, w0, w1, ob);
    asm volatile("s_nop 2" : "+v"(y0), "+v"(y1), "+v"(y2), "+v"(y3));
    const unsigned ypk = cvtpk(y0 + y2, y1 + y3);
    bst<SC1>(u32x4{plo(zpk, ypk), tagA, phi(zpk, ypk), tagB}, rws, yoff, WS_YG + b * 64 * 4096 + jp * 64);
  }
  if (hw == 0) {
    {
      const u64 tp = __builtin_amdgcn_s_memrealtime();
      const int dl = min(max((int)(__builtin_amdgcn_readfirstlane(yanch) + T_Y - (unsigned)tp), Y_MIN), Y_MAX);
#pragma nounroll
      while ((int)(__builtin_amdgcn_s_memrealtime() - tp) < dl) {}
    }
    u32x4 g[4];
    poll<4, SC1>(g, rws, goff, 16, WS_YG + (b * 64 + jp) * 4096, tagA, tagB);
    float u[8];
#pragma unroll
    for (int j = 0; j < 8; ++j) u[j] = fold32(bflo(g[j >> 1][(j & 1) * 2]), bfhi(g[j >> 1][(j & 1) * 2]));
#pragma unroll
    for (int j = 0; j < 4; ++j) u[j] = fold16(u[j], u[j + 4]);
#pragma unroll
    for (int j = 0; j < 2; ++j) u[j] = foldd<XOR8>(u[j], u[j + 2], lane & 8);
    u[0] = foldd<XOR7>(u[0], u[1], lane & 4);
    u[0] += dpp<XOR2>(u[0]); u[0] += dpp<XOR1>(u[0]);
    const float t = rbf(rbf(u[0]) * gsel);
    const unsigned res = cvtpk(fold32(t, t), 0.f);
    if (lane < 32 && (lane & 3) == 0) out[b * DM + jp * 8 + (lane >> 2)] = (u16)res;
  }
}

extern "C" void kernel_launch(void* const* d_in, const int* in_sizes, int n_in, void* d_out, int out_size, void* d_ws, size_t ws_size, hipStream_t stream) {
  k_fused<<<256, 512, 0, stream>>>((const u16*)d_in[0], (const u16*)d_in[1], (const u16*)d_in[2], (const u16*)d_in[3], (const u16*)d_in[4], (const u16*)d_in[6], (const u16*)d_in[7],
                                   (const u16*)d_in[8], (const u16*)d_in[9], (const u16*)d_in[10], (u16*)d_out, (unsigned char*)d_ws);
}
```

```cpp
#include <hip/hip_runtime.h>
#include <hip/hip_bf16.h>
#include <cstdint>

typedef unsigned u32x4 __attribute__((ext_vector_type(4)));
typedef unsigned u32x2 __attribute__((ext_vector_type(2)));
typedef float f32x4 __attribute__((ext_vector_type(4)));
typedef float f32x2 __attribute__((ext_vector_type(2)));
typedef __attribute__((ext_vector_type(2))) __bf16 bf16x2;
typedef unsigned long long u64;
typedef unsigned short u16;
typedef __amdgpu_buffer_rsrc_t rsrc_t;
#define GAS __attribute__((address_space(1)))
#define LAS __attribute__((address_space(3)))
#define RLX_AGENT __ATOMIC_RELAXED, __HIP_MEMORY_SCOPE_AGENT
#define RLX_WG __ATOMIC_RELAXED, __HIP_MEMORY_SCOPE_WORKGROUP
#define DRAIN() asm volatile("s_waitcnt vmcnt(0)" ::: "memory")
#define LDS_BARRIER() do { asm volatile("s_waitcnt lgkmcnt(0)" ::: "memory"); __builtin_amdgcn_s_barrier(); asm volatile("" ::: "memory"); } while (0)
#define CAPWAIT(n) do { __builtin_amdgcn_sched_barrier(0); asm volatile("s_waitcnt vmcnt(%0)" :: "i"((n) - 1) : "memory"); } while (0)

constexpr int NB = 4, DM = 512, NH = 32, SK = 8192, DH = 128, WIN = 2048;
constexpr float SCALE = 0.088388347648318440f, L2E = 1.4426950408889634f;
constexpr int NT = 2, SC1 = 16, SC1NT = 18, RSRC_W3 = 0x00020000;
constexpr int NS = 32;
constexpr unsigned SPIN_CAP = 1u << 18;

constexpr int CAP0 = 5;
constexpr int TAU = 108;
constexpr int TK0 = 813;
constexpr int XCENTER = 11, XMIN = 6, XGAIN = 10;
constexpr int END_STEPS = 8, XCENTER_END = 8, XMIN_END = 2, XGAIN_END = 45;
constexpr int T_Q = 540, T_PB = 1680, T_W = 1850, T_ST = 500, SLOT = 64;
constexpr int T_CP0 = 4600, T_CP = 500, T_CPLAG = 200, RHO_MIN = -20, RHO_MAX = 60;
constexpr int T_Y = 63 * TAU + 200, Y_MIN = 40, Y_MAX = 72;

constexpr int WS_EPOCH = 0;
constexpr int WS_PH = 4096, NCP = 14;
constexpr int WS_QF = 32768;
constexpr int WS_QP = WS_QF + 16384;
constexpr int WS_PBM = WS_QP + 524288;
constexpr int WS_ST = WS_PBM + 16384;
constexpr int WS_PBP = WS_ST + 16384;
constexpr int WS_YG = WS_PBP + 262144;
constexpr int WS_BYTES = WS_YG + 256 * 4096;

constexpr int L_MISC = 0, L_QA = 256, L_QB = 512, L_REC = 1536, L_SC = 4096, L_TMP = 12288, L_PUB = L_TMP + 8704, L_BIG = 22528, L_BYTES = L_BIG + 131072;
constexpr int REC_F = 132;
enum { M_TAGA = 0, M_TAGB = 1, M_ANCH = 2, M_STEP = 3, M_QRDY = 4, M_HSYNC = 5, M_KDONE = 6, M_BDMA = 7, M_WMAX = 8, M_LSUM = 12, M_CNORM = 16, M_PH = 20 };

__device__ __forceinline__ float dot2(unsigned a, unsigned b, float c) {
  return __builtin_amdgcn_fdot2_f32_bf16(__builtin_bit_cast(bf16x2, a), __builtin_bit_cast(bf16x2, b), c, false);
}
__device__ __forceinline__ unsigned cvtpk(float lo, float hi) { return __builtin_bit_cast(unsigned, __builtin_convertvector(f32x2{lo, hi}, bf16x2)); }
__device__ __forceinline__ float bflo(unsigned u) { return __uint_as_float(u << 16); }
__device__ __forceinline__ float bfhi(unsigned u) { return __uint_as_float(u & 0xffff0000u); }
__device__ __forceinline__ float rbf(float x) { return bflo(cvtpk(x, 0.f)); }
__device__ __forceinline__ float ex(float x) { return __builtin_amdgcn_exp2f(x * L2E); }
__device__ __forceinline__ unsigned plo(unsigned hi, unsigned lo) { return __builtin_amdgcn_perm(hi, lo, 0x05040100u); }
__device__ __forceinline__ unsigned phi(unsigned hi, unsigned lo) { return __builtin_amdgcn_perm(hi, lo, 0x07060302u); }
template <int CTRL> __device__ __forceinline__ float dpp(float x) {
  return __builtin_bit_cast(float, __builtin_amdgcn_mov_dpp(__builtin_bit_cast(int, x), CTRL, 0xf, 0xf, true));
}
constexpr int XOR1 = 0xB1, XOR2 = 0x4E, XOR7 = 0x141, XOR8 = 0x128;
__device__ __forceinline__ float row16_sum(float x) { x += dpp<XOR1>(x); x += dpp<XOR2>(x); x += dpp<XOR7>(x); x += dpp<XOR8>(x); return x; }
__device__ __forceinline__ float row16_max(float x) { x = fmaxf(x, dpp<XOR1>(x)); x = fmaxf(x, dpp<XOR2>(x)); x = fmaxf(x, dpp<XOR7>(x)); return fmaxf(x, dpp<XOR8>(x)); }
template <int CTRL> __device__ __forceinline__ float foldd(float a, float b, bool hi) { return (hi ? b : a) + dpp<CTRL>(hi ? a : b); }
__device__ __forceinline__ float fold32(float x, float y) {
  auto t = __builtin_amdgcn_permlane32_swap(__float_as_uint(x), __float_as_uint(y), false, false);
  return __uint_as_float(t[0]) + __uint_as_float(t[1]);
}
__device__ __forceinline__ float fold16(float x, float y) {
  auto t = __builtin_amdgcn_permlane16_swap(__float_as_uint(x), __float_as_uint(y), false, false);
  return __uint_as_float(t[0]) + __uint_as_float(t[1]);
}
__device__ __forceinline__ float xrow_sum(float x) { x = fold16(x, x); return fold32(x, x); }
__device__ __forceinline__ float xrow_max(float x) {
  auto s = __builtin_amdgcn_permlane16_swap(__float_as_uint(x), __float_as_uint(x), false, false);
  x = fmaxf(__uint_as_float(s[0]), __uint_as_float(s[1]));
  auto t = __builtin_amdgcn_permlane32_swap(__float_as_uint(x), __float_as_uint(x), false, false);
  return fmaxf(__uint_as_float(t[0]), __uint_as_float(t[1]));
}
__device__ __forceinline__ float wave_sum(float x) { return xrow_sum(row16_sum(x)); }
__device__ __forceinline__ f32x2 fold_rows(const float (&a)[8]) {
  const float u0 = fold32(a[0], a[4]), u1 = fold32(a[1], a[5]), u2 = fold32(a[2], a[6]), u3 = fold32(a[3], a[7]);
  return f32x2{fold16(u0, u2), fold16(u1, u3)};
}

__device__ __forceinline__ rsrc_t mk_rsrc(const void* p, int bytes) { return __builtin_amdgcn_make_buffer_rsrc(const_cast<void*>(p), 0, bytes, RSRC_W3); }
template <int AUX> __device__ __forceinline__ u32x4 bld(rsrc_t r, int voff, int soff) { return __builtin_amdgcn_raw_buffer_load_b128(r, voff, soff, AUX); }
template <int AUX> __device__ __forceinline__ u32x2 bld2(rsrc_t r, int voff, int soff) { return __builtin_amdgcn_raw_buffer_load_b64(r, voff, soff, AUX); }
template <int AUX> __device__ __forceinline__ void bst(u32x4 v, rsrc_t r, int voff, int soff) { __builtin_amdgcn_raw_buffer_store_b128(v, r, voff, soff, AUX); }
template <int AUX> __device__ __forceinline__ void glds16(const GAS unsigned char* src, unsigned dst) {
  unsigned keep;
  if (AUX == NT) asm volatile("s_mov_b32 %0, m0\n\ts_mov_b32 m0, %2\n\ts_nop 0\n\tglobal_load_lds_dwordx4 %1, off nt\n\ts_mov_b32 m0, %0" : "=&s"(keep) : "v"(src), "s"(dst) : "memory");
  else asm volatile("s_mov_b32 %0, m0\n\ts_mov_b32 m0, %2\n\ts_nop 0\n\tglobal_load_lds_dwordx4 %1, off sc1\n\ts_mov_b32 m0, %0" : "=&s"(keep) : "v"(src), "s"(dst) : "memory");
}
__device__ __forceinline__ const GAS unsigned char* code_here() { u64 pc; asm volatile("s_getpc_b64 %0" : "=s"(pc)); return (const GAS unsigned char*)(pc & ~127ull); }

#define DITH(t) ((((t) & 1) << 2) | ((t) & 2) | (((t) & 4) >> 2))
#define WINWAIT(x, t) do { __builtin_amdgcn_sched_barrier(0); asm volatile("s_cmp_gt_i32 %0, %2\n\ts_cbranch_scc1 2f\n\ts_cmp_gt_i32 %0, %1\n\ts_cbranch_scc1 1f\n\ts_waitcnt vmcnt(3)\n1:\n\ts_waitcnt vmcnt(4)\n" \
    "2:\n\ts_cmp_gt_i32 %0, %3\n\ts_cbranch_scc1 3f\n\ts_waitcnt vmcnt(5)\n3:\n\ts_waitcnt vmcnt(6)" :: "s"(x), "i"(7 - DITH(t)), "i"(15 - DITH(t)), "i"(23 - DITH(t)) : "memory", "scc"); } while (0)

template <int SLEEP> __device__ __forceinline__ void wait_time(u64 t0, unsigned ticks) {
#pragma nounroll
  while (__builtin_amdgcn_s_memrealtime() - t0 < ticks) __builtin_amdgcn_s_sleep(SLEEP);
}
__device__ __forceinline__ void next_slot() {
  const unsigned n0 = (unsigned)__builtin_amdgcn_s_memrealtime(), left = SLOT - n0 % SLOT;
#pragma nounroll
  while ((unsigned)__builtin_amdgcn_s_memrealtime() - n0 < left) __builtin_amdgcn_s_sleep(1);
  asm volatile("" ::: "memory");
}
template <int SLEEP> __device__ __forceinline__ void lds_wait_ge(LAS unsigned* p, unsigned target) {
#pragma nounroll
  for (unsigned spins = 0; spins < 32 * SPIN_CAP && (unsigned)__builtin_amdgcn_readfirstlane(__hip_atomic_load(p, RLX_WG)) < target; ++spins) __builtin_amdgcn_s_sleep(SLEEP);
  asm volatile("" ::: "memory");
}
__device__ __forceinline__ void lds_signal(LAS unsigned* p, int lane) {
  asm volatile("s_waitcnt lgkmcnt(0)" ::: "memory");
  if (lane == 0) __builtin_amdgcn_atomic_inc32(p, 0xffffffffu, __ATOMIC_RELAXED, "workgroup");
}
__device__ __forceinline__ void hsync(LAS unsigned* misc, unsigned n, int lane) { lds_signal(misc + M_HSYNC, lane); lds_wait_ge<2>(misc + M_HSYNC, 4 * n); }
template <int N, int AUX> __device__ __forceinline__ void poll(u32x4 (&g)[N], rsrc_t rws, int voff, int stride, int soff, unsigned tagA, unsigned tagB) {
#pragma nounroll
  for (unsigned spins = 0; spins < SPIN_CAP; ++spins) {
    asm volatile("" ::: "memory");
    bool ok = true;
#pragma unroll
    for (int i = 0; i < N; ++i) g[i] = bld<AUX>(rws, voff + i * stride, soff);
#pragma unroll
    for (int i = 0; i < N; ++i) ok &= (g[i][1] == tagA) & (g[i][3] == tagB);
    if (__all(ok)) break;
    __builtin_amdgcn_s_sleep(2);
  }
  __builtin_amdgcn_fence(__ATOMIC_ACQUIRE, "wavefront");
  asm volatile("" ::: "memory");
}
__device__ __forceinline__ void cp_publish(int k, LAS unsigned* misc, rsrc_t rws, int bid, unsigned tagA, int lane) {
  if ((((bid >> 3) + k) & 15) == 0) {
    asm volatile("" ::: "memory");
    const u32x4 wp = *(LAS u32x4*)(misc + M_PH);
    const unsigned ph = wp[0] + (((int)(wp[1] - wp[0]) + (int)(wp[2] - wp[0]) + (int)(wp[3] - wp[0])) >> 2);
    if (lane < 8) __builtin_amdgcn_raw_buffer_store_b64(u32x2{ph, tagA}, rws, lane * 8, WS_PH + k * 2048 + ((bid & 7) + ((bid >> 7) << 3)) * 64, SC1);
  }
}
__device__ __forceinline__ unsigned cp_mean(int k, rsrc_t rws, unsigned ref, unsigned tagA, int lane) {
  asm volatile("" ::: "memory");
  const u32x2 g = bld2<SC1>(rws, (lane & 15) * 64, WS_PH + k * 2048);
  const bool ok = g[1] == tagA;
  const float d = wave_sum(ok ? (float)(int)(g[0] - ref) : 0.f), n = wave_sum(ok ? 1.f : 0.f);
  return __builtin_amdgcn_readfirstlane(n >= 32.f ? ref + (int)(d / n) : ref);
}
__device__ __forceinline__ void cp_consume(int k, LAS unsigned* misc, rsrc_t rws, unsigned tagA, int lane) {
  const unsigned a = cp_mean(k, rws, __hip_atomic_load(misc + M_ANCH, RLX_WG), tagA, lane);
  if (lane == 0) __hip_atomic_store(misc + M_ANCH, a, RLX_WG);
}

__device__ __forceinline__ void q_partial(const u32x4 (&w)[4], const u32x2 (&xv)[NB], LAS float* part, int lane) {
  float a[NB][8];
#pragma unroll
  for (int j = 0; j < 4; ++j) {
    const unsigned p0 = plo(w[1][j], w[0][j]), p1 = phi(w[1][j], w[0][j]), p2 = plo(w[3][j], w[2][j]), p3 = phi(w[3][j], w[2][j]);
#pragma unroll
    for (int bb = 0; bb < NB; ++bb) { a[bb][2 * j] = dot2(p2, xv[bb][1], dot2(p0, xv[bb][0], 0.f)); a[bb][2 * j + 1] = dot2(p3, xv[bb][1], dot2(p1, xv[bb][0], 0.f)); }
  }
  float f[8];
#pragma unroll
  for (int j = 0; j < 8; ++j) f[j] = fold16(fold32(a[0][j], a[2][j]), fold32(a[1][j], a[3][j]));
#pragma unroll
  for (int j = 0; j < 4; ++j) f[j] = foldd<XOR8>(f[j], f[j + 4], lane & 8);
  *(LAS f32x4*)(part + (lane >> 4) * 64 + (lane & 7) * 8 + ((lane >> 3) & 1) * 4) = f32x4{f[0], f[1], f[2], f[3]};
}
__device__ __forceinline__ void q_fetch(u32x2 (&p)[4], rsrc_t rws, int br, int b, int hq, int lane) {
#pragma unroll
  for (int i = 0; i < 4; ++i) p[i] = bld2<SC1NT>(rws, lane * 8 + i * 16384, WS_QP + br * 262144 + b * 65536 + hq * 512);
}
__device__ __forceinline__ unsigned q_sum(const u32x2 (&p)[4]) {
  const f32x2 s = (__builtin_bit_cast(f32x2, p[0]) + __builtin_bit_cast(f32x2, p[1])) + (__builtin_bit_cast(f32x2, p[2]) + __builtin_bit_cast(f32x2, p[3]));
  return cvtpk(s[0], s[1]);
}
__device__ __forceinline__ void gqa_step(const u32x4 (&k)[4], const u32x4 (&v)[4], const u32x4 (&q)[4], float (&m)[4], float (&l)[4], float (&acc)[4][8]) {
#pragma unroll
  for (int g = 0; g < 4; ++g) {
    float s[4];
#pragma unroll
    for (int t = 0; t < 4; ++t) s[t] = dot2(k[t][3], q[g][3], dot2(k[t][2], q[g][2], dot2(k[t][1], q[g][1], dot2(k[t][0], q[g][0], 0.f))));
#pragma unroll
    for (int t = 0; t < 4; t += 2) {
      const unsigned r1 = cvtpk(row16_sum(s[t]), row16_sum(s[t + 1]));
      const unsigned r2 = cvtpk(bflo(r1) * SCALE, bfhi(r1) * SCALE);
      s[t] = bflo(r2); s[t + 1] = bfhi(r2);
    }
    const float mn = fmaxf(fmaxf(m[g], fmaxf(s[0], s[1])), fmaxf(s[2], s[3]));
    const float al = ex(m[g] - mn);
    m[g] = mn;
    float ls = l[g] * al;
#pragma unroll
    for (int j = 0; j < 8; ++j) acc[g][j] *= al;
#pragma unroll
    for (int t = 0; t < 4; t += 2) {
      const float p0 = ex(s[t] - mn), p1 = ex(s[t + 1] - mn);
      ls += p0 + p1;
      const unsigned w = cvtpk(p0, p1);
#pragma unroll
      for (int j = 0; j < 4; ++j) {
        acc[g][2 * j] = dot2(v[t + 1][j], w >> 16, dot2(v[t][j], w & 0xffffu, acc[g][2 * j]));
        acc[g][2 * j + 1] = dot2(v[t + 1][j], w & 0xffff0000u, dot2(v[t][j], w << 16, acc[g][2 * j + 1]));
      }
    }
    l[g] = ls;
  }
}
template <int C> struct Proj {
  static __device__ __forceinline__ void run(float& y0, float& y1, float& y2, float& y3, const unsigned (&w0)[64], const unsigned (&w1)[64], const unsigned (&ob)[4]) {
#define DOTB(y, o, w) "v_dot2c_f32_bf16_dpp " y ", " o ", " w " row_newbcast:%16 row_mask:0xf bank_mask:0xf\n\t"
    asm volatile(DOTB("%0", "%4", "%8") DOTB("%1", "%4", "%12") DOTB("%2", "%5", "%9") DOTB("%3", "%5", "%13") DOTB("%0", "%6", "%10") DOTB("%1", "%6", "%14") DOTB("%2", "%7", "%11") DOTB("%3", "%7", "%15")
                 : "+v"(y0), "+v"(y1), "+v"(y2), "+v"(y3)
                 : "v"(ob[0]), "v"(ob[1]), "v"(ob[2]), "v"(ob[3]), "v"(w0[4 * C]), "v"(w0[4 * C + 1]), "v"(w0[4 * C + 2]), "v"(w0[4 * C + 3]),
                   "v"(w1[4 * C]), "v"(w1[4 * C + 1]), "v"(w1[4 * C + 2]), "v"(w1[4 * C + 3]), "i"(C));
#undef DOTB
    if constexpr (C + 1 < 16) Proj<C + 1>::run(y0, y1, y2, y3, w0, w1, ob);
  }
};

__global__ void __launch_bounds__(512, 2) k_fused(const u16* __restrict__ x, const u16* __restrict__ ka, const u16* __restrict__ va, const u16* __restrict__ wqa, const u16* __restrict__ woa,
                                                  const u16* __restrict__ kb, const u16* __restrict__ vb, const u16* __restrict__ wqb, const u16* __restrict__ wob,
                                                  const u16* __restrict__ gw, u16* __restrict__ out, unsigned char* __restrict__ ws) {
  __shared__ __attribute__((aligned(1024))) unsigned char smem[L_BYTES];
  LAS unsigned char* L = (LAS unsigned char*)smem;
  LAS unsigned* misc = (LAS unsigned*)(L + L_MISC);
  LAS float* miscf = (LAS float*)(L + L_MISC);
  const unsigned lds0 = (unsigned)(size_t)L;
  const int tid = threadIdx.x, lane = tid & 63, wave = __builtin_amdgcn_readfirstlane(tid >> 6), r = lane >> 4, c = lane & 15;
  const int bid = blockIdx.x, xg = bid & 7, idx = bid >> 3, b = idx & 3, sp = idx >> 2;
  const int side = sp & 1, h = xg * 4 + (idx >> 3), jp = h * 2 + side;
  asm volatile("" :: "s"(x), "s"(ka), "s"(va), "s"(wqa), "s"(woa), "s"(kb), "s"(vb), "s"(wqb), "s"(wob), "s"(gw), "s"(out), "s"(ws));

  if (wave >= 4) {
    __builtin_amdgcn_s_setprio(1);
    const int sw = wave - 4, voff = lane * 16;
    if (tid == 256) {
      const unsigned a0 = (unsigned)__builtin_amdgcn_s_memrealtime() + TK0;
      *(LAS u32x2*)(misc + M_ANCH) = u32x2{a0, 0u}; *(LAS u32x4*)(misc + M_QRDY) = u32x4{0u, 0u, 0u, 0u}; *(LAS u32x4*)(misc + M_PH) = u32x4{a0, a0, a0, a0};
    }
    u32x4 junk = {0u, 0u, 0u, 0u}; unsigned junk1 = 0;
    if (sw == 3 && idx < 4) asm volatile("global_load_dword %0, %1, off" : "+v"(junk1) : "v"(code_here() + (lane & 31) * 128) : "memory");
    LDS_BARRIER();
    {
      const size_t o = ((size_t)(b * 8 + xg) * SK + (SK - WIN) + sp * 64 + sw * 16) * DH;
      const GAS unsigned char* pk = (const GAS unsigned char*)(kb + o) + voff;
      const GAS unsigned char* pv = (const GAS unsigned char*)(vb + o) + voff;
#pragma unroll 4
      for (int i = 0; i < 16; ++i) { CAPWAIT(CAP0); glds16<NT>(pk + (((i >> 2) + xg) & 3) * 131072 + (i & 3) * 1024, lds0 + L_BIG + sw * 4096 + (i >> 2) * 16384 + (i & 3) * 1024); }
#pragma unroll 4
      for (int i = 0; i < 16; ++i) { CAPWAIT(CAP0); glds16<NT>(pv + (((i >> 2) + xg) & 3) * 131072 + (i & 3) * 1024, lds0 + L_BIG + 65536 + sw * 4096 + (i >> 2) * 16384 + (i & 3) * 1024); }
    }
    {
      const size_t o = (size_t)(h * 128 + (side * 4 + b) * 16 + sw * 4) * DM;
#pragma unroll
      for (int i = 0; i < 8; ++i) {
        const GAS unsigned char* p = (const GAS unsigned char*)((i < 4 ? wob : woa) + o) + (i & 3) * 1024;
        CAPWAIT(i < 4 ? CAP0 + i : 7);
        asm volatile("global_load_dwordx4 %0, %2, off nt\n\tglobal_load_dword %1, %3, off sc1" : "+v"(junk), "+v"(junk1) : "v"(p + voff), "v"(p + (lane & 7) * 128) : "memory");
      }
    }
    lds_signal(misc + M_BDMA, lane);
    const u16* kp = ka + (size_t)(b * NH + h) * SK * DH;
    const u16* vp = va + (size_t)(b * NH + h) * SK * DH;
#define SOFF(i) ((((((i) + h) & (NS - 1)) * 2 + side) << 15) + sw * 2048)
#define LOAD(dst, t) dst = bld<NT>(rn, voff + ((t) & 1) * 1024, on + ((t) >> 1) * 8192); __builtin_amdgcn_sched_barrier(0)
#define QK(v) dot2(v[3], q[3], dot2(v[2], q[2], dot2(v[1], q[1], dot2(v[0], q[0], 0.f))))
#define SCORES(i)        \
      _Pragma("unroll") for (int j = 0; j < 4; ++j) s[j] = foldd<XOR7>(s[j], s[j + 4], lane & 4); \
      _Pragma("unroll") for (int j = 0; j < 2; ++j) s[j] = foldd<XOR2>(s[j], s[j + 2], lane & 2); \
      s[0] = foldd<XOR1>(s[0], s[1], lane & 1); \
      s[0] += dpp<XOR8>(s[0]);                                              \
      const unsigned sb = cvtpk(rbf(s[0]) * SCALE, 0.f);                    \
      mx = fmaxf(mx, bflo(sb)); \
      *(LAS u16*)(L + L_SC + (i) * 256 + sw * 64 + r * 16 + (c & 7) * 2) = (u16)sb
#define SAMPLE() now = __builtin_amdgcn_s_memrealtime(); anch = __hip_atomic_load(misc + M_ANCH, RLX_WG); __builtin_amdgcn_sched_barrier(0)
#define PACE(i)          \
      asm volatile("" : "+s"(now)); \
      const unsigned ph = (unsigned)now - (i) * TAU; \
      const bool eg = (i) >= 2 * NS - END_STEPS; \
      const int xw = max((eg ? XCENTER_END : XCENTER) + (((int)(ph - __builtin_amdgcn_readfirstlane(anch)) * (eg ? XGAIN_END : XGAIN)) >> 8), eg ? XMIN_END : XMIN); \
      __hip_atomic_store(misc + M_PH + sw, ph, RLX_WG)
    u32x4 d[8], q;
    u64 now; unsigned anch;
    float mx = -INFINITY;
    {
      const rsrc_t rn = mk_rsrc(kp, SK * 256);
      u32x4 e[8];
      float s[8];
      int on = SOFF(0);
#pragma unroll
      for (int t = 0; t < 8; ++t) { CAPWAIT(t < 4 ? CAP0 + 4 - t : CAP0); LOAD(e[t], t); }
      static_assert(CAP0 <= 8, "the 8 loads above must push the dropped loads out of the window");
      asm volatile("" :: "v"(junk), "v"(junk1));
      on = SOFF(1);
#pragma unroll
      for (int t = 0; t < 8; ++t) {
        if (t != 4) CAPWAIT(CAP0);
        LOAD(d[t], t);
        if (t == 4) { lds_wait_ge<2>(misc + M_QRDY, 1); q = *(LAS u32x4*)(L + L_QA + c * 16); asm volatile("" : "+v"(q)); }
        if (t >= 4) { s[2 * t - 8] = QK(e[2 * t - 8]); s[2 * t - 7] = QK(e[2 * t - 7]); asm volatile("" : "+v"(s[2 * t - 8]), "+v"(s[2 * t - 7])); }
      }
      SAMPLE();
      SCORES(0);
    }
#pragma nounroll
    for (int i = 1; i < NS; ++i) {
      PACE(i);
      const rsrc_t rn = mk_rsrc(i + 1 < NS ? kp : vp, SK * 256);
      const int on = SOFF(i + 1), xe = i + 1 < NS ? xw : xw + 16;
      float s[8];
#pragma unroll
      for (int t = 0; t < 8; ++t) { s[t] = QK(d[t]); asm volatile("" : "+v"(s[t])); WINWAIT(t < 6 ? xw : xe, t); LOAD(d[t], t); }
      SAMPLE();
      SCORES(i);
    }
    mx = xrow_max(row16_max(mx));
    if (lane == 0) miscf[M_WMAX + sw] = mx;
    lds_signal(misc + M_KDONE, lane);
#define PIN8(a) asm volatile("" : "+v"(a[0]), "+v"(a[1]), "+v"(a[2]), "+v"(a[3]), "+v"(a[4]), "+v"(a[5]), "+v"(a[6]), "+v"(a[7]))
    float acc[8];
#pragma unroll
    for (int j = 0; j < 8; ++j) acc[j] = 0.f;
    u32x4 scn = *(LAS u32x4*)(L + L_SC + sw * 64 + r * 16);
#pragma nounroll
    for (int i = 0; i < NS; ++i) {
      u32x4 sc = scn;
      asm volatile("" : "+v"(sc));
      PACE(NS + i);
      if (sw == 0) __hip_atomic_store(misc + M_STEP, (unsigned)i, RLX_WG);
      scn = *(LAS u32x4*)(L + L_SC + ((i + 1) & (NS - 1)) * 256 + sw * 64 + r * 16);
      const rsrc_t rn = mk_rsrc(vp, i + 1 < NS ? SK * 256 : 0);
      const int on = SOFF(i + 1), xv = i + 1 < NS ? xw : xw + 99;
#pragma unroll
      for (int t = 0; t < 8; t += 2) {
        const unsigned w = cvtpk(ex(bflo(sc[t >> 1]) - mx), ex(bfhi(sc[t >> 1]) - mx));
#pragma unroll
        for (int j = 0; j < 4; ++j) { acc[2 * j] = dot2(d[t][j], w & 0xffffu, acc[2 * j]); acc[2 * j + 1] = dot2(d[t][j], w << 16, acc[2 * j + 1]); }
        PIN8(acc); WINWAIT(xv, t); LOAD(d[t], t);
#pragma unroll
        for (int j = 0; j < 4; ++j) { acc[2 * j] = dot2(d[t + 1][j], w >> 16, acc[2 * j]); acc[2 * j + 1] = dot2(d[t + 1][j], w & 0xffff0000u, acc[2 * j + 1]); }
        PIN8(acc); WINWAIT(xv, t + 1); LOAD(d[t + 1], t + 1);
      }
      SAMPLE();
    }
    *(LAS f32x2*)(L + L_REC + sw * 512 + c * 32 + r * 8) = fold_rows(acc);
    asm volatile("" :: "s"(now), "v"(scn), "v"(anch) : "memory");
    LDS_BARRIER();
    return;
  }

  __builtin_amdgcn_s_setprio(2);
  const int hw = wave;
  const rsrc_t rws = mk_rsrc(ws, WS_BYTES);
  const u64 t0 = __builtin_amdgcn_s_memrealtime();
  LAS float* tmp = (LAS float*)(L + L_TMP);
  float gate = 0.f;
  u64 epoch = 0;
  const int band = xg >> 1, cg = (xg & 1) * 32 + idx;
  {
    const rsrc_t rx = mk_rsrc(x, NB * DM * 2), rwa = mk_rsrc(wqa, DM * 4096 * 2), rwb = mk_rsrc(wqb, DM * 4096 * 2);
    const int row0 = band * 128 + hw * 32, vo = (lane >> 3) * 4 * 8192 + (lane & 7) * 16;
    u32x4 wA[4], wB[4]; u32x2 xv[NB];
#pragma unroll
    for (int i = 0; i < 4; ++i) wA[i] = bld<NT>(rwa, vo, (row0 + i) * 8192 + cg * 128);
#pragma unroll
    for (int bb = 0; bb < NB; ++bb) xv[bb] = bld2<0>(rx, (lane >> 3) * 8, (bb * DM + row0) * 2);
    if (hw == 0) epoch = __hip_atomic_load((GAS u64*)(ws + WS_EPOCH), RLX_AGENT);
    __builtin_amdgcn_sched_barrier(0);
#pragma unroll
    for (int i = 0; i < 4; ++i) wB[i] = bld<NT>(rwb, vo, (row0 + i) * 8192 + cg * 128);
    __builtin_amdgcn_sched_barrier(0);
    LDS_BARRIER();
    asm volatile("" : "+v"(wA[0]));
    __builtin_amdgcn_sched_barrier(0);
    q_partial(wA, xv, tmp + hw * 256, lane);
    asm volatile("" : "+v"(epoch));
    epoch += 1;
    if (tid == 0) { misc[M_TAGA] = (unsigned)epoch; misc[M_TAGB] = (unsigned)(epoch >> 32) ^ ((unsigned)epoch * 0x9E3779B1u); }
    q_partial(wB, xv, tmp + 1024 + hw * 256, lane);
  }
  hsync(misc, 1, lane);
  const unsigned tagA = misc[M_TAGA], tagB = misc[M_TAGB];
  if (hw == 0) {
#pragma unroll
    for (int br = 0; br < 2; ++br) {
      LAS float* part = tmp + br * 1024 + lane * 4;
      const f32x4 s = (*(LAS f32x4*)part + *(LAS f32x4*)(part + 256)) + (*(LAS f32x4*)(part + 512) + *(LAS f32x4*)(part + 768));
      bst<SC1NT>(__builtin_bit_cast(u32x4, s), rws, (lane >> 4) * 65536 + (lane & 15) * 16, WS_QP + br * 262144 + band * 16384 + cg * 256);
    }
    DRAIN();
    if (lane < 4) bst<SC1>(u32x4{0u, tagA, 0u, tagB}, rws, lane * 16, WS_QF + (cg * 4 + band) * 64);
    const u32x4 xv = bld<0>(mk_rsrc(x, NB * DM * 2), lane * 16, b * DM * 2), gv = bld<0>(mk_rsrc(gw, DM * 2), lane * 16, 0);
    float s = 0.f;
#pragma unroll
    for (int j = 0; j < 4; ++j) { const unsigned p = cvtpk(bflo(xv[j]) * bflo(gv[j]), bfhi(xv[j]) * bfhi(gv[j])); s += bflo(p); s += bfhi(p); }
    s = rbf(wave_sum(s));
    gate = rbf(1.f / (1.f + __expf(-s)));
  }
  {
    wait_time<4>(t0, T_Q);
    u32x4 f[1];
    poll<1, SC1>(f, rws, (lane & 7) * 64, 0, WS_QF + (xg * 4 + hw) * 512, tagA, tagB);
    u32x2 pb[4];
    if (hw == (h & 3)) {
      u32x2 pa[4];
      q_fetch(pa, rws, 0, b, h, lane);
      q_fetch(pb, rws, 1, b, xg * 4 + hw, lane);
      *(LAS unsigned*)(L + L_QA + lane * 4) = q_sum(pa);
      lds_signal(misc + M_QRDY, lane);
    } else q_fetch(pb, rws, 1, b, xg * 4 + hw, lane);
    *(LAS unsigned*)(L + L_QB + hw * 256 + lane * 4) = q_sum(pb);
    lds_wait_ge<2>(misc + M_BDMA, 4);
  }
  __builtin_amdgcn_s_setprio(0);
  hsync(misc, 2, lane);
  {
    u32x4 q[4];
#pragma unroll
    for (int g = 0; g < 4; ++g) q[g] = *(LAS u32x4*)(L + L_QB + g * 256 + c * 16);
    float m[4], l[4], acc[4][8];
#pragma unroll
    for (int g = 0; g < 4; ++g) { m[g] = -INFINITY; l[g] = 0.f;
#pragma unroll
      for (int j = 0; j < 8; ++j) acc[g][j] = 0.f; }
#pragma nounroll
    for (int s = 0; s < 4; ++s) {
      u32x4 k[4], v[4];
#pragma unroll
      for (int t = 0; t < 4; ++t) { k[t] = *(LAS u32x4*)(L + L_BIG + (hw * 16 + s * 4 + t) * 1024 + lane * 16); v[t] = *(LAS u32x4*)(L + L_BIG + 65536 + (hw * 16 + s * 4 + t) * 1024 + lane * 16); }
      gqa_step(k, v, q, m, l, acc);
    }
#pragma unroll
    for (int g = 0; g < 4; ++g) {
      const float M = xrow_max(m[g]), w = ex(m[g] - M), Ls = xrow_sum(l[g] * w);
#pragma unroll
      for (int j = 0; j < 8; ++j) acc[g][j] *= w;
      LAS float* rec = tmp + (g * 4 + hw) * REC_F;
      *(LAS f32x2*)(rec + 4 + c * 8 + r * 2) = fold_rows(acc[g]);
      if (lane == 0) *(LAS f32x2*)rec = f32x2{M, Ls};
    }
  }
  hsync(misc, 3, lane);
  {
    LAS float* rec = tmp + hw * 4 * REC_F;
    f32x2 ml[4], av[4];
#pragma unroll
    for (int i = 0; i < 4; ++i) { ml[i] = *(LAS f32x2*)(rec + i * REC_F); av[i] = *(LAS f32x2*)(rec + i * REC_F + 4 + lane * 2); }
    const float M = fmaxf(fmaxf(ml[0][0], ml[1][0]), fmaxf(ml[2][0], ml[3][0]));
    float Ls = 0.f, o0 = 0.f, o1 = 0.f;
#pragma unroll
    for (int i = 0; i < 4; ++i) { const float wt = ex(ml[i][0] - M); Ls = fmaf(wt, ml[i][1], Ls); o0 = fmaf(wt, av[i][0], o0); o1 = fmaf(wt, av[i][1], o1); }
    *(LAS unsigned*)(L + L_PUB + hw * 256 + lane * 4) = cvtpk(o0 / Ls, o1 / Ls);
    if (lane == 0) *(LAS f32x2*)(L + L_PUB + 1024 + hw * 8) = f32x2{M, Ls};
  }
  hsync(misc, 4, lane);
  if (hw == 0) {
    const int pi = (b * 8 + xg) * 8 + sp;
    wait_time<4>(t0, T_PB);
    next_slot();
    bst<SC1NT>(*(LAS u32x4*)(L + L_PUB + lane * 16), rws, lane * 16, WS_PBP + pi * 1024);
    const u32x2 ml = *(LAS u32x2*)(L + L_PUB + 1024 + (lane & 3) * 8);
    DRAIN();
    next_slot();
    if (lane < 4) bst<SC1NT>(u32x4{ml[0], tagA, ml[1], tagB}, rws, lane * 16, WS_PBM + pi * 64);
  }
  if (hw == 3) cp_publish(0, misc, rws, bid, tagA, lane);
  wait_time<4>(t0, T_W);
  if (hw == 3) cp_consume(0, misc, rws, tagA, lane);
  {
    const GAS unsigned char* src = (const GAS unsigned char*)(woa + (size_t)(h * 128 + hw * 2) * DM) + lane * 16;
#pragma nounroll
    for (int i0 = 0; i0 < 32; i0 += 4) {
#pragma unroll
      for (int i = i0; i < i0 + 4; ++i) { const int o = (i >> 1) * 8192 + (i & 1) * 1024; CAPWAIT(2); glds16<SC1>(src + o, lds0 + L_BIG + hw * 2048 + o); }
      if (hw == 3) { if (i0 == 12) cp_publish(1, misc, rws, bid, tagA, lane); else if (i0 == 20) cp_consume(1, misc, rws, tagA, lane); }
      if (hw == 0 && i0 == 12) {
        u32x4 ml[1];
        poll<1, SC1NT>(ml, rws, (lane >> 3) * 64, 0, WS_PBM + (b * 8 + xg) * 512 + (h & 3) * 16, tagA, tagB);
        const u32x4 av = bld<SC1NT>(rws, (lane >> 3) * 1024 + (lane & 7) * 16, WS_PBP + (b * 8 + xg) * 8192 + (h & 3) * 256 + side * 128);
        const float pm = __uint_as_float(ml[0][0]), M = xrow_max(fmaxf(pm, dpp<XOR8>(pm)));
        const float wt = ex(pm - M) * __uint_as_float(ml[0][2]), Ls = xrow_sum(wt + dpp<XOR8>(wt));
        float o[8];
#pragma unroll
        for (int j = 0; j < 4; ++j) { o[2 * j] = wt * bflo(av[j]); o[2 * j + 1] = wt * bfhi(av[j]); }
        const f32x2 f = fold_rows(o);
        *(LAS unsigned*)(L + L_PUB + (lane & 7) * 16 + r * 4) = cvtpk((f[0] + dpp<XOR8>(f[0])) / Ls, (f[1] + dpp<XOR8>(f[1])) / Ls);
      }
    }
    DRAIN();
    hsync(misc, 5, lane);
    if (hw == 3) cp_publish(2, misc, rws, bid, tagA, lane);
  }
  unsigned w0[64], w1[64];
#pragma unroll
  for (int p = 0; p < 64; ++p) {
    const unsigned a = *(LAS unsigned*)(L + L_BIG + p * 2048 + tid * 4), bb = *(LAS unsigned*)(L + L_BIG + p * 2048 + 1024 + tid * 4);
    w0[p] = plo(bb, a); w1[p] = phi(bb, a);
    asm volatile("" : "+v"(w0[p]), "+v"(w1[p]));
  }
  hsync(misc, 6, lane);
  unsigned zpk;
  {
    const GAS unsigned char* src = (const GAS unsigned char*)(wob + (size_t)(h * 128 + side * 64 + hw * 2) * DM) + lane * 16;
    if (hw == 3) cp_consume(2, misc, rws, tagA, lane);
#pragma unroll 4
    for (int i = 0; i < 16; ++i) { const int o = (i >> 1) * 8192 + (i & 1) * 1024; CAPWAIT(2); glds16<SC1>(src + o, lds0 + L_BIG + hw * 2048 + o); }
    DRAIN();
    hsync(misc, 7, lane);
    float z0 = 0.f, z1 = 0.f;
#pragma unroll 4
    for (int p = 0; p < 32; ++p) {
      const unsigned a = *(LAS unsigned*)(L + L_BIG + p * 2048 + tid * 4), bb = *(LAS unsigned*)(L + L_BIG + p * 2048 + 1024 + tid * 4), op = *(LAS unsigned*)(L + L_PUB + p * 4);
      z0 = dot2(plo(bb, a), op, z0); z1 = dot2(phi(bb, a), op, z1);
    }
    zpk = cvtpk(z0, z1);
    asm volatile("" : "+v"(zpk));
  }
  {
    lds_wait_ge<32>(misc + M_KDONE, 4);
    const f32x4 wm = *(LAS f32x4*)(miscf + M_WMAX);
    const float M = fmaxf(fmaxf(wm[0], wm[1]), fmaxf(wm[2], wm[3]));
    float ls = 0.f;
#pragma unroll
    for (int i = 0; i < 2; ++i) {
      const u32x4 sc = *(LAS u32x4*)(L + L_SC + hw * 2048 + i * 1024 + lane * 16);
#pragma unroll
      for (int j = 0; j < 4; ++j) ls += ex(bflo(sc[j]) - M) + ex(bfhi(sc[j]) - M);
    }
    ls = wave_sum(ls);
    if (lane == 0) miscf[M_LSUM + hw] = ls;
    hsync(misc, 8, lane);
    if (hw == 0) {
      const f32x4 lw = *(LAS f32x4*)(miscf + M_LSUM);
      const float Ls = (lw[0] + lw[1]) + (lw[2] + lw[3]);
      if (lane < 4) bst<SC1NT>(u32x4{__float_as_uint(M), tagA, __float_as_uint(Ls), tagB}, rws, lane * 16, WS_ST + (b * 64 + jp) * 64);
      wait_time<32>(__builtin_amdgcn_s_memrealtime(), T_ST);
      u32x4 o[1];
      poll<1, SC1NT>(o, rws, 0, 0, WS_ST + (b * 64 + (jp ^ 1)) * 64, tagA, tagB);
      const float Mo = __uint_as_float(o[0][0]), MM = fmaxf(M, Mo);
      if (lane < 4) miscf[M_CNORM + lane] = ex(miscf[M_WMAX + lane] - MM) / (ex(M - MM) * Ls + ex(Mo - MM) * __uint_as_float(o[0][2]));
      if (bid == 0) {
        u32x4 all[4];
        wait_time<32>(__builtin_amdgcn_s_memrealtime(), T_ST);
        poll<4, SC1>(all, rws, lane * 64, 4096, WS_ST, tagA, tagB);
        if (lane == 0) __hip_atomic_store((GAS u64*)(ws + WS_EPOCH), epoch, RLX_AGENT);
      }
    } else if (hw == 3) {
      int k = 3, state = 0, rho = 0, rprev = RHO_MAX, have = 0, TP = 0;
      unsigned P = __builtin_amdgcn_readfirstlane(__hip_atomic_load(misc + M_ANCH, RLX_WG));
#pragma nounroll
      for (unsigned spins = 0; spins < SPIN_CAP && (unsigned)__builtin_amdgcn_readfirstlane(__hip_atomic_load(misc + M_STEP, RLX_WG)) < NS - 3; ++spins) {
        const int t = (int)(__builtin_amdgcn_s_memrealtime() - t0), tk = T_CP0 + (k - 3) * T_CP;
        if (state == 0 && t >= tk && k < NCP) { if (t < tk + T_CPLAG / 2) cp_publish(k, misc, rws, bid, tagA, lane); state = 1; }
        else if (state == 1 && t >= tk + T_CPLAG) {
          const unsigned Pe = P + (((tk - TP) * rho) >> 10), Pn = cp_mean(k, rws, Pe, tagA, lane);
          if (have) { const int rn = min(max((int)(Pn - P) * 1024 / T_CP, RHO_MIN), RHO_MAX); rho = min(rn, rprev); rprev = rn; }
          have = have || Pn != Pe; P = Pn; TP = tk; state = 0; ++k;
        }
        if (lane == 0) __hip_atomic_store(misc + M_ANCH, P + (((t - TP) * rho) >> 10), RLX_WG);
        __builtin_amdgcn_s_sleep(16);
      }
    }
  }
  int yoff = (tid >> 2) * 4096 + (tid & 3) * 16, goff = lane * 64;
  float gsel = lane < 32 ? gate : rbf(1.f - gate);
  asm volatile("" : "+v"(yoff), "+v"(goff), "+v"(gsel));
  if (hw == 1 && idx < 2) {
    const unsigned pf = *(const GAS unsigned*)(code_here() + (lane & 31) * 128);
    asm volatile("" :: "v"(pf));
  }
  LDS_BARRIER();
  const unsigned yanch = misc[M_ANCH];
  {
    const f32x4 cn = *(LAS f32x4*)(miscf + M_CNORM);
    f32x4 rl[4], rh[4];
#pragma unroll
    for (int w = 0; w < 4; ++w) { rl[w] = *(LAS f32x4*)(L + L_REC + w * 512 + c * 32); rh[w] = *(LAS f32x4*)(L + L_REC + w * 512 + c * 32 + 16); }
    __builtin_amdgcn_sched_barrier(0);
    f32x4 lo = cn[0] * rl[0], hi = cn[0] * rh[0];
#pragma unroll
    for (int w = 1; w < 4; ++w) { lo += cn[w] * rl[w]; hi += cn[w] * rh[w]; }
    unsigned ob[4] = {cvtpk(lo[0], lo[1]), cvtpk(lo[2], lo[3]), cvtpk(hi[0], hi[1]), cvtpk(hi[2], hi[3])};
    float y0 = 0.f, y1 = 0.f, y2 = 0.f, y3 = 0.f;
    asm volatile("s_nop 1" : "+v"(ob[0]), "+v"(ob[1]), "+v"(ob[2]), "+v"(ob[3]), "+v"(y0), "+v"(y1), "+v"(y2), "+v"(y3));
    Proj<0>::run(y0, y1, y2, y3, w0, w1, ob);
    asm volatile("s_nop 2" : "+v"(y0), "+v"(y1), "+v"(y2), "+v"(y3));
    const unsigned ypk = cvtpk(y0 + y2, y1 + y3);
    bst<SC1>(u32x4{plo(zpk, ypk), tagA, phi(zpk, ypk), tagB}, rws, yoff, WS_YG + b * 64 * 4096 + jp * 64);
  }
  if (hw == 0) {
    {
      const u64 tp = __builtin_amdgcn_s_memrealtime();
      const int dl = min(max((int)(__builtin_amdgcn_readfirstlane(yanch) + T_Y - (unsigned)tp), Y_MIN), Y_MAX);
#pragma nounroll
      while ((int)(__builtin_amdgcn_s_memrealtime() - tp) < dl) {}
    }
    u32x4 g[4];
    poll<4, SC1>(g, rws, goff, 16, WS_YG + (b * 64 + jp) * 4096, tagA, tagB);
    float u[8];
#pragma unroll
    for (int j = 0; j < 8; ++j) u[j] = fold32(bflo(g[j >> 1][(j & 1) * 2]), bfhi(g[j >> 1][(j & 1) * 2]));
#pragma unroll
    for (int j = 0; j < 4; ++j) u[j] = fold16(u[j], u[j + 4]);
#pragma unroll
    for (int j = 0; j < 2; ++j) u[j] = foldd<XOR8>(u[j], u[j + 2], lane & 8);
    u[0] = foldd<XOR7>(u[0], u[1], lane & 4);
    u[0] += dpp<XOR2>(u[0]); u[0] += dpp<XOR1>(u[0]);
    const float t = rbf(rbf(u[0]) * gsel);
    const unsigned res = cvtpk(fold32(t, t), 0.f);
    if (lane < 32 && (lane & 3) == 0) __builtin_amdgcn_raw_buffer_store_b16((u16)res, mk_rsrc(out, NB * DM * 2), (lane >> 2) * 2, (b * DM + jp * 8) * 2, SC1);
  }
}

extern "C" void kernel_launch(void* const* d_in, const int* in_sizes, int n_in, void* d_out, int out_size, void* d_ws, size_t ws_size, hipStream_t stream) {
  k_fused<<<256, 512, 0, stream>>>((const u16*)d_in[0], (const u16*)d_in[1], (const u16*)d_in[2], (const u16*)d_in[3], (const u16*)d_in[4], (const u16*)d_in[6], (const u16*)d_in[7],
                                   (const u16*)d_in[8], (const u16*)d_in[9], (const u16*)d_in[10], (u16*)d_out, (unsigned char*)d_ws);
}
```

```cpp
#include <hip/hip_runtime.h>
#include <hip/hip_bf16.h>
#include <cstdint>

typedef unsigned u32x4 __attribute__((ext_vector_type(4)));
typedef unsigned u32x2 __attribute__((ext_vector_type(2)));
typedef float f32x4 __attribute__((ext_vector_type(4)));
typedef float f32x2 __attribute__((ext_vector_type(2)));
typedef __attribute__((ext_vector_type(2))) __bf16 bf16x2;
typedef unsigned long long u64;
typedef unsigned short u16;
typedef __amdgpu_buffer_rsrc_t rsrc_t;
#define GAS __attribute__((address_space(1)))
#define LAS __attribute__((address_space(3)))
#define RLX_AGENT __ATOMIC_RELAXED, __HIP_MEMORY_SCOPE_AGENT
#define RLX_WG __ATOMIC_RELAXED, __HIP_MEMORY_SCOPE_WORKGROUP
#define DRAIN() asm volatile("s_waitcnt vmcnt(0)" ::: "memory")
#define LDS_BARRIER() do { asm volatile("s_waitcnt lgkmcnt(0)" ::: "memory"); __builtin_amdgcn_s_barrier(); asm volatile("" ::: "memory"); } while (0)
#define CAPWAIT(n) do { __builtin_amdgcn_sched_barrier(0); asm volatile("s_waitcnt vmcnt(%0)" :: "i"((n) - 1) : "memory"); } while (0)

constexpr int NB = 4, DM = 512, NH = 32, SK = 8192, DH = 128, WIN = 2048;
constexpr float SCALE = 0.088388347648318440f, L2E = 1.4426950408889634f;
constexpr int NT = 2, SC1 = 16, SC1NT = 18, RSRC_W3 = 0x00020000;
constexpr int NS = 32;
constexpr unsigned SPIN_CAP = 1u << 18;

constexpr int CAP0 = 5;
constexpr int TAU = 108;
constexpr int TK0 = 805;
constexpr int XCENTER = 11, XMIN = 6, XGAIN = 10;
constexpr int END_STEPS = 8, XCENTER_END = 9, XMIN_END = 2, XGAIN_END = 45;
constexpr int T_Q = 540, T_PB = 1680, T_W = 1850, T_ST = 500, SLOT = 64;
constexpr int T_CP0 = 4600, T_CP = 500, T_CPLAG = 200, RHO_MIN = -20, RHO_MAX = 60;
constexpr int T_Y = 63 * TAU + 190, Y_MIN = 40, Y_MAX = 72;

constexpr int WS_EPOCH = 0;
constexpr int WS_PH = 4096, NCP = 14;
constexpr int WS_QF = 32768;
constexpr int WS_QP = WS_QF + 16384;
constexpr int WS_PBM = WS_QP + 524288;
constexpr int WS_ST = WS_PBM + 16384;
constexpr int WS_PBP = WS_ST + 16384;
constexpr int WS_YG = WS_PBP + 262144;
constexpr int WS_BYTES = WS_YG + 256 * 4096;

constexpr int L_MISC = 0, L_QA = 256, L_QB = 512, L_REC = 1536, L_SC = 4096, L_TMP = 12288, L_PUB = L_TMP + 8704, L_BIG = 22528, L_DUMP = L_BIG + 131072, L_BYTES = L_DUMP + 1024;
constexpr int REC_F = 132;
enum { M_TAGA = 0, M_TAGB = 1, M_ANCH = 2, M_STEP = 3, M_QRDY = 4, M_HSYNC = 5, M_KDONE = 6, M_BDMA = 7, M_WMAX = 8, M_LSUM = 12, M_CNORM = 16, M_PH = 20 };

__device__ __forceinline__ float dot2(unsigned a, unsigned b, float c) {
  return __builtin_amdgcn_fdot2_f32_bf16(__builtin_bit_cast(bf16x2, a), __builtin_bit_cast(bf16x2, b), c, false);
}
__device__ __forceinline__ unsigned cvtpk(float lo, float hi) { return __builtin_bit_cast(unsigned, __builtin_convertvector(f32x2{lo, hi}, bf16x2)); }
__device__ __forceinline__ float bflo(unsigned u) { return __uint_as_float(u << 16); }
__device__ __forceinline__ float bfhi(unsigned u) { return __uint_as_float(u & 0xffff0000u); }
__device__ __forceinline__ float rbf(float x) { return bflo(cvtpk(x, 0.f)); }
__device__ __forceinline__ float ex(float x) { return __builtin_amdgcn_exp2f(x * L2E); }
__device__ __forceinline__ unsigned plo(unsigned hi, unsigned lo) { return __builtin_amdgcn_perm(hi, lo, 0x05040100u); }
__device__ __forceinline__ unsigned phi(unsigned hi, unsigned lo) { return __builtin_amdgcn_perm(hi, lo, 0x07060302u); }
template <int CTRL> __device__ __forceinline__ float dpp(float x) {
  return __builtin_bit_cast(float, __builtin_amdgcn_mov_dpp(__builtin_bit_cast(int, x), CTRL, 0xf, 0xf, true));
}
constexpr int XOR1 = 0xB1, XOR2 = 0x4E, XOR7 = 0x141, XOR8 = 0x128;
__device__ __forceinline__ float row16_sum(float x) { x += dpp<XOR1>(x); x += dpp<XOR2>(x); x += dpp<XOR7>(x); x += dpp<XOR8>(x); return x; }
__device__ __forceinline__ float row16_max(float x) { x = fmaxf(x, dpp<XOR1>(x)); x = fmaxf(x, dpp<XOR2>(x)); x = fmaxf(x, dpp<XOR7>(x)); return fmaxf(x, dpp<XOR8>(x)); }
template <int CTRL> __device__ __forceinline__ float foldd(float a, float b, bool hi) { return (hi ? b : a) + dpp<CTRL>(hi ? a : b); }
__device__ __forceinline__ float fold32(float x, float y) {
  auto t = __builtin_amdgcn_permlane32_swap(__float_as_uint(x), __float_as_uint(y), false, false);
  return __uint_as_float(t[0]) + __uint_as_float(t[1]);
}
__device__ __forceinline__ float fold16(float x, float y) {
  auto t = __builtin_amdgcn_permlane16_swap(__float_as_uint(x), __float_as_uint(y), false, false);
  return __uint_as_float(t[0]) + __uint_as_float(t[1]);
}
__device__ __forceinline__ float xrow_sum(float x) { x = fold16(x, x); return fold32(x, x); }
__device__ __forceinline__ float xrow_max(float x) {
  auto s = __builtin_amdgcn_permlane16_swap(__float_as_uint(x), __float_as_uint(x), false, false);
  x = fmaxf(__uint_as_float(s[0]), __uint_as_float(s[1]));
  auto t = __builtin_amdgcn_permlane32_swap(__float_as_uint(x), __float_as_uint(x), false, false);
  return fmaxf(__uint_as_float(t[0]), __uint_as_float(t[1]));
}
__device__ __forceinline__ float wave_sum(float x) { return xrow_sum(row16_sum(x)); }
__device__ __forceinline__ f32x2 fold_rows(const float (&a)[8]) {
  const float u0 = fold32(a[0], a[4]), u1 = fold32(a[1], a[5]), u2 = fold32(a[2], a[6]), u3 = fold32(a[3], a[7]);
  return f32x2{fold16(u0, u2), fold16(u1, u3)};
}

__device__ __forceinline__ rsrc_t mk_rsrc(const void* p, int bytes) { return __builtin_amdgcn_make_buffer_rsrc(const_cast<void*>(p), 0, bytes, RSRC_W3); }
template <int AUX> __device__ __forceinline__ u32x4 bld(rsrc_t r, int voff, int soff) { return __builtin_amdgcn_raw_buffer_load_b128(r, voff, soff, AUX); }
template <int AUX> __device__ __forceinline__ u32x2 bld2(rsrc_t r, int voff, int soff) { return __builtin_amdgcn_raw_buffer_load_b64(r, voff, soff, AUX); }
template <int AUX> __device__ __forceinline__ void bst(u32x4 v, rsrc_t r, int voff, int soff) { __builtin_amdgcn_raw_buffer_store_b128(v, r, voff, soff, AUX); }
template <int AUX> __device__ __forceinline__ void glds16(const GAS unsigned char* src, unsigned dst) {
  unsigned keep;
  if (AUX == NT) asm volatile("s_mov_b32 %0, m0\n\ts_mov_b32 m0, %2\n\ts_nop 0\n\tglobal_load_lds_dwordx4 %1, off nt\n\ts_mov_b32 m0, %0" : "=&s"(keep) : "v"(src), "s"(dst) : "memory");
  else asm volatile("s_mov_b32 %0, m0\n\ts_mov_b32 m0, %2\n\ts_nop 0\n\tglobal_load_lds_dwordx4 %1, off sc1\n\ts_mov_b32 m0, %0" : "=&s"(keep) : "v"(src), "s"(dst) : "memory");
}
__device__ __forceinline__ const GAS unsigned char* code_here() { u64 pc; asm volatile("s_getpc_b64 %0" : "=s"(pc)); return (const GAS unsigned char*)(pc & ~127ull); }

#define DITH(t) ((((t) & 1) << 2) | ((t) & 2) | (((t) & 4) >> 2))
#define WINWAIT(x, t) do { __builtin_amdgcn_sched_barrier(0); asm volatile("s_cmp_gt_i32 %0, %2\n\ts_cbranch_scc1 2f\n\ts_cmp_gt_i32 %0, %1\n\ts_cbranch_scc1 1f\n\ts_waitcnt vmcnt(3)\n1:\n\ts_waitcnt vmcnt(4)\n" \
    "2:\n\ts_cmp_gt_i32 %0, %3\n\ts_cbranch_scc1 3f\n\ts_waitcnt vmcnt(5)\n3:\n\ts_waitcnt vmcnt(6)" :: "s"(x), "i"(7 - DITH(t)), "i"(15 - DITH(t)), "i"(23 - DITH(t)) : "memory", "scc"); } while (0)

template <int SLEEP> __device__ __forceinline__ void wait_time(u64 t0, unsigned ticks) {
#pragma nounroll
  while (__builtin_amdgcn_s_memrealtime() - t0 < ticks) __builtin_amdgcn_s_sleep(SLEEP);
}
__device__ __forceinline__ void next_slot() {
  const unsigned n0 = (unsigned)__builtin_amdgcn_s_memrealtime(), left = SLOT - n0 % SLOT;
#pragma nounroll
  while ((unsigned)__builtin_amdgcn_s_memrealtime() - n0 < left) __builtin_amdgcn_s_sleep(1);
  asm volatile("" ::: "memory");
}
template <int SLEEP> __device__ __forceinline__ void lds_wait_ge(LAS unsigned* p, unsigned target) {
#pragma nounroll
  for (unsigned spins = 0; spins < 32 * SPIN_CAP && (unsigned)__builtin_amdgcn_readfirstlane(__hip_atomic_load(p, RLX_WG)) < target; ++spins) __builtin_amdgcn_s_sleep(SLEEP);
  asm volatile("" ::: "memory");
}
__device__ __forceinline__ void lds_signal(LAS unsigned* p, int lane) {
  asm volatile("s_waitcnt lgkmcnt(0)" ::: "memory");
  if (lane == 0) __builtin_amdgcn_atomic_inc32(p, 0xffffffffu, __ATOMIC_RELAXED, "workgroup");
}
__device__ __forceinline__ void hsync(LAS unsigned* misc, unsigned n, int lane) { lds_signal(misc + M_HSYNC, lane); lds_wait_ge<2>(misc + M_HSYNC, 4 * n); }
template <int N, int AUX> __device__ __forceinline__ void poll(u32x4 (&g)[N], rsrc_t rws, int voff, int stride, int soff, unsigned tagA, unsigned tagB) {
#pragma nounroll
  for (unsigned spins = 0; spins < SPIN_CAP; ++spins) {
    asm volatile("" ::: "memory");
    bool ok = true;
#pragma unroll
    for (int i = 0; i < N; ++i) g[i] = bld<AUX>(rws, voff + i * stride, soff);
#pragma unroll
    for (int i = 0; i < N; ++i) ok &= (g[i][1] == tagA) & (g[i][3] == tagB);
    if (__all(ok)) break;
    __builtin_amdgcn_s_sleep(2);
  }
  __builtin_amdgcn_fence(__ATOMIC_ACQUIRE, "wavefront");
  asm volatile("" ::: "memory");
}
__device__ __forceinline__ void cp_publish(int k, LAS unsigned* misc, rsrc_t rws, int bid, unsigned tagA, int lane) {
  if ((((bid >> 3) + k) & 15) == 0) {
    asm volatile("" ::: "memory");
    const u32x4 wp = *(LAS u32x4*)(misc + M_PH);
    const unsigned ph = wp[0] + (((int)(wp[1] - wp[0]) + (int)(wp[2] - wp[0]) + (int)(wp[3] - wp[0])) >> 2);
    if (lane < 8) __builtin_amdgcn_raw_buffer_store_b64(u32x2{ph, tagA}, rws, lane * 8, WS_PH + k * 2048 + ((bid & 7) + ((bid >> 7) << 3)) * 64, SC1);
  }
}
__device__ __forceinline__ unsigned cp_mean(int k, rsrc_t rws, unsigned ref, unsigned tagA, int lane) {
  asm volatile("" ::: "memory");
  const u32x2 g = bld2<SC1>(rws, (lane & 15) * 64, WS_PH + k * 2048);
  const bool ok = g[1] == tagA;
  const float d = wave_sum(ok ? (float)(int)(g[0] - ref) : 0.f), n = wave_sum(ok ? 1.f : 0.f);
  return __builtin_amdgcn_readfirstlane(n >= 32.f ? ref + (int)(d / n) : ref);
}
__device__ __forceinline__ void cp_consume(int k, LAS unsigned* misc, rsrc_t rws, unsigned tagA, int lane) {
  const unsigned a = cp_mean(k, rws, __hip_atomic_load(misc + M_ANCH, RLX_WG), tagA, lane);
  if (lane == 0) __hip_atomic_store(misc + M_ANCH, a, RLX_WG);
}

__device__ __forceinline__ void q_partial(const u32x4 (&w)[4], const u32x2 (&xv)[NB], LAS float* part, int lane) {
  float a[NB][8];
#pragma unroll
  for (int j = 0; j < 4; ++j) {
    const unsigned p0 = plo(w[1][j], w[0][j]), p1 = phi(w[1][j], w[0][j]), p2 = plo(w[3][j], w[2][j]), p3 = phi(w[3][j], w[2][j]);
#pragma unroll
    for (int bb = 0; bb < NB; ++bb) { a[bb][2 * j] = dot2(p2, xv[bb][1], dot2(p0, xv[bb][0], 0.f)); a[bb][2 * j + 1] = dot2(p3, xv[bb][1], dot2(p1, xv[bb][0], 0.f)); }
  }
  float f[8];
#pragma unroll
  for (int j = 0; j < 8; ++j) f[j] = fold16(fold32(a[0][j], a[2][j]), fold32(a[1][j], a[3][j]));
#pragma unroll
  for (int j = 0; j < 4; ++j) f[j] = foldd<XOR8>(f[j], f[j + 4], lane & 8);
  *(LAS f32x4*)(part + (lane >> 4) * 64 + (lane & 7) * 8 + ((lane >> 3) & 1) * 4) = f32x4{f[0], f[1], f[2], f[3]};
}
__device__ __forceinline__ void q_fetch(u32x2 (&p)[4], rsrc_t rws, int br, int b, int hq, int lane) {
#pragma unroll
  for (int i = 0; i < 4; ++i) p[i] = bld2<SC1NT>(rws, lane * 8 + i * 16384, WS_QP + br * 262144 + b * 65536 + hq * 512);
}
__device__ __forceinline__ unsigned q_sum(const u32x2 (&p)[4]) {
  const f32x2 s = (__builtin_bit_cast(f32x2, p[0]) + __builtin_bit_cast(f32x2, p[1])) + (__builtin_bit_cast(f32x2, p[2]) + __builtin_bit_cast(f32x2, p[3]));
  return cvtpk(s[0], s[1]);
}
__device__ __forceinline__ void gqa_step(const u32x4 (&k)[4], const u32x4 (&v)[4], const u32x4 (&q)[4], float (&m)[4], float (&l)[4], float (&acc)[4][8]) {
#pragma unroll
  for (int g = 0; g < 4; ++g) {
    float s[4];
#pragma unroll
    for (int t = 0; t < 4; ++t) s[t] = dot2(k[t][3], q[g][3], dot2(k[t][2], q[g][2], dot2(k[t][1], q[g][1], dot2(k[t][0], q[g][0], 0.f))));
#pragma unroll
    for (int t = 0; t < 4; t += 2) {
      const unsigned r1 = cvtpk(row16_sum(s[t]), row16_sum(s[t + 1]));
      const unsigned r2 = cvtpk(bflo(r1) * SCALE, bfhi(r1) * SCALE);
      s[t] = bflo(r2); s[t + 1] = bfhi(r2);
    }
    const float mn = fmaxf(fmaxf(m[g], fmaxf(s[0], s[1])), fmaxf(s[2], s[3]));
    const float al = ex(m[g] - mn);
    m[g] = mn;
    float ls = l[g] * al;
#pragma unroll
    for (int j = 0; j < 8; ++j) acc[g][j] *= al;
#pragma unroll
    for (int t = 0; t < 4; t += 2) {
      const float p0 = ex(s[t] - mn), p1 = ex(s[t + 1] - mn);
      ls += p0 + p1;
      const unsigned w = cvtpk(p0, p1);
#pragma unroll
      for (int j = 0; j < 4; ++j) {
        acc[g][2 * j] = dot2(v[t + 1][j], w >> 16, dot2(v[t][j], w & 0xffffu, acc[g][2 * j]));
        acc[g][2 * j + 1] = dot2(v[t + 1][j], w & 0xffff0000u, dot2(v[t][j], w << 16, acc[g][2 * j + 1]));
      }
    }
    l[g] = ls;
  }
}
template <int C> struct Proj {
  static __device__ __forceinline__ void run(float& y0, float& y1, float& y2, float& y3, const unsigned (&w0)[64], const unsigned (&w1)[64], const unsigned (&ob)[4]) {
#define DOTB(y, o, w) "v_dot2c_f32_bf16_dpp " y ", " o ", " w " row_newbcast:%16 row_mask:0xf bank_mask:0xf\n\t"
    asm volatile(DOTB("%0", "%4", "%8") DOTB("%1", "%4", "%12") DOTB("%2", "%5", "%9") DOTB("%3", "%5", "%13") DOTB("%0", "%6", "%10") DOTB("%1", "%6", "%14") DOTB("%2", "%7", "%11") DOTB("%3", "%7", "%15")
                 : "+v"(y0), "+v"(y1), "+v"(y2), "+v"(y3)
                 : "v"(ob[0]), "v"(ob[1]), "v"(ob[2]), "v"(ob[3]), "v"(w0[4 * C]), "v"(w0[4 * C + 1]), "v"(w0[4 * C + 2]), "v"(w0[4 * C + 3]),
                   "v"(w1[4 * C]), "v"(w1[4 * C + 1]), "v"(w1[4 * C + 2]), "v"(w1[4 * C + 3]), "i"(C));
#undef DOTB
    if constexpr (C + 1 < 16) Proj<C + 1>::run(y0, y1, y2, y3, w0, w1, ob);
  }
};

__global__ void __launch_bounds__(512, 2) k_fused(const u16* __restrict__ x, const u16* __restrict__ ka, const u16* __restrict__ va, const u16* __restrict__ wqa, const u16* __restrict__ woa,
                                                  const u16* __restrict__ kb, const u16* __restrict__ vb, const u16* __restrict__ wqb, const u16* __restrict__ wob,
                                                  const u16* __restrict__ gw, u16* __restrict__ out, unsigned char* __restrict__ ws) {
  __shared__ __attribute__((aligned(1024))) unsigned char smem[L_BYTES];
  LAS unsigned char* L = (LAS unsigned char*)smem;
  LAS unsigned* misc = (LAS unsigned*)(L + L_MISC);
  LAS float* miscf = (LAS float*)(L + L_MISC);
  const unsigned lds0 = (unsigned)(size_t)L;
  const int tid = threadIdx.x, lane = tid & 63, wave = __builtin_amdgcn_readfirstlane(tid >> 6), r = lane >> 4, c = lane & 15;
  const int bid = blockIdx.x, xg = bid & 7, idx = bid >> 3, b = idx & 3, sp = idx >> 2;
  const int side = sp & 1, h = xg * 4 + (idx >> 3), jp = h * 2 + side;
  asm volatile("" :: "s"(x), "s"(ka), "s"(va), "s"(wqa), "s"(woa), "s"(kb), "s"(vb), "s"(wqb), "s"(wob), "s"(gw), "s"(out), "s"(ws));

  if (wave >= 4) {
    __builtin_amdgcn_s_setprio(1);
    const int sw = wave - 4, voff = lane * 16;
    if (tid == 256) {
      const unsigned a0 = (unsigned)__builtin_amdgcn_s_memrealtime() + TK0;
      *(LAS u32x2*)(misc + M_ANCH) = u32x2{a0, 0u}; *(LAS u32x4*)(misc + M_QRDY) = u32x4{0u, 0u, 0u, 0u}; *(LAS u32x4*)(misc + M_PH) = u32x4{a0, a0, a0, a0};
    }
    if (sw == 3 && idx < 4) glds16<SC1>(code_here() + idx * 1024 + voff, lds0 + L_DUMP);
    LDS_BARRIER();
    {
      const size_t o = ((size_t)(b * 8 + xg) * SK + (SK - WIN) + sp * 64 + sw * 16) * DH;
      const GAS unsigned char* pk = (const GAS unsigned char*)(kb + o) + voff;
      const GAS unsigned char* pv = (const GAS unsigned char*)(vb + o) + voff;
#pragma unroll 4
      for (int i = 0; i < 16; ++i) { CAPWAIT(CAP0); glds16<NT>(pk + (((i >> 2) + xg) & 3) * 131072 + (i & 3) * 1024, lds0 + L_BIG + sw * 4096 + (i >> 2) * 16384 + (i & 3) * 1024); }
#pragma unroll 4
      for (int i = 0; i < 16; ++i) { CAPWAIT(CAP0); glds16<NT>(pv + (((i >> 2) + xg) & 3) * 131072 + (i & 3) * 1024, lds0 + L_BIG + 65536 + sw * 4096 + (i >> 2) * 16384 + (i & 3) * 1024); }
    }
    {
      const size_t o = (size_t)(h * 128 + (side * 4 + b) * 16 + sw * 4) * DM;
#pragma unroll
      for (int i = 0; i < 8; ++i) { CAPWAIT(CAP0); glds16<SC1>((const GAS unsigned char*)((i < 4 ? wob : woa) + o) + (i & 3) * 1024 + voff, lds0 + L_DUMP); }
    }
    static_assert(CAP0 <= 8, "the 8 loads above must push the GQA rows out of the window");
    lds_signal(misc + M_BDMA, lane);
    const u16* kp = ka + (size_t)(b * NH + h) * SK * DH;
    const u16* vp = va + (size_t)(b * NH + h) * SK * DH;
#define SOFF(i) ((((((i) + h) & (NS - 1)) * 2 + side) << 15) + sw * 2048)
#define LOAD(dst, t) dst = bld<NT>(rn, voff + ((t) & 1) * 1024, on + ((t) >> 1) * 8192); __builtin_amdgcn_sched_barrier(0)
#define QK(v) dot2(v[3], q[3], dot2(v[2], q[2], dot2(v[1], q[1], dot2(v[0], q[0], 0.f))))
#define SCORES(i)        \
      _Pragma("unroll") for (int j = 0; j < 4; ++j) s[j] = foldd<XOR7>(s[j], s[j + 4], lane & 4); \
      _Pragma("unroll") for (int j = 0; j < 2; ++j) s[j] = foldd<XOR2>(s[j], s[j + 2], lane & 2); \
      s[0] = foldd<XOR1>(s[0], s[1], lane & 1); \
      s[0] += dpp<XOR8>(s[0]);                                              \
      const unsigned sb = cvtpk(rbf(s[0]) * SCALE, 0.f);                    \
      mx = fmaxf(mx, bflo(sb)); \
      *(LAS u16*)(L + L_SC + (i) * 256 + sw * 64 + r * 16 + (c & 7) * 2) = (u16)sb
#define SAMPLE() now = __builtin_amdgcn_s_memrealtime(); anch = __hip_atomic_load(misc + M_ANCH, RLX_WG); __builtin_amdgcn_sched_barrier(0)
#define PACE(i)          \
      asm volatile("" : "+s"(now)); \
      const unsigned ph = (unsigned)now - (i) * TAU; \
      const bool eg = (i) >= 2 * NS - END_STEPS; \
      const int xw = max((eg ? XCENTER_END : XCENTER) + (((int)(ph - __builtin_amdgcn_readfirstlane(anch)) * (eg ? XGAIN_END : XGAIN)) >> 8), eg ? XMIN_END : XMIN); \
      __hip_atomic_store(misc + M_PH + sw, ph, RLX_WG)
    u32x4 d[8], q;
    u64 now; unsigned anch;
    float mx = -INFINITY;
    {
      const rsrc_t rn = mk_rsrc(kp, SK * 256);
      u32x4 e[8];
      float s[8];
      int on = SOFF(0);
#pragma unroll
      for (int t = 0; t < 8; ++t) { CAPWAIT(CAP0); LOAD(e[t], t); }
      on = SOFF(1);
#pragma unroll
      for (int t = 0; t < 8; ++t) {
        if (t != 4) CAPWAIT(CAP0);
        LOAD(d[t], t);
        if (t == 4) { lds_wait_ge<2>(misc + M_QRDY, 1); q = *(LAS u32x4*)(L + L_QA + c * 16); asm volatile("" : "+v"(q)); }
        if (t >= 4) { s[2 * t - 8] = QK(e[2 * t - 8]); s[2 * t - 7] = QK(e[2 * t - 7]); asm volatile("" : "+v"(s[2 * t - 8]), "+v"(s[2 * t - 7])); }
      }
      SAMPLE();
      SCORES(0);
    }
#pragma nounroll
    for (int i = 1; i < NS; ++i) {
      PACE(i);
      const rsrc_t rn = mk_rsrc(i + 1 < NS ? kp : vp, SK * 256);
      const int on = SOFF(i + 1), xe = i + 1 < NS ? xw : xw + 16;
      float s[8];
#pragma unroll
      for (int t = 0; t < 8; ++t) { s[t] = QK(d[t]); asm volatile("" : "+v"(s[t])); WINWAIT(t < 6 ? xw : xe, t); LOAD(d[t], t); }
      SAMPLE();
      SCORES(i);
    }
    mx = xrow_max(row16_max(mx));
    if (lane == 0) miscf[M_WMAX + sw] = mx;
    lds_signal(misc + M_KDONE, lane);
#define PIN8(a) asm volatile("" : "+v"(a[0]), "+v"(a[1]), "+v"(a[2]), "+v"(a[3]), "+v"(a[4]), "+v"(a[5]), "+v"(a[6]), "+v"(a[7]))
    float acc[8];
#pragma unroll
    for (int j = 0; j < 8; ++j) acc[j] = 0.f;
    u32x4 scn = *(LAS u32x4*)(L + L_SC + sw * 64 + r * 16);
#pragma nounroll
    for (int i = 0; i < NS; ++i) {
      u32x4 sc = scn;
      asm volatile("" : "+v"(sc));
      PACE(NS + i);
      if (sw == 0) __hip_atomic_store(misc + M_STEP, (unsigned)i, RLX_WG);
      scn = *(LAS u32x4*)(L + L_SC + ((i + 1) & (NS - 1)) * 256 + sw * 64 + r * 16);
      const rsrc_t rn = mk_rsrc(vp, i + 1 < NS ? SK * 256 : 0);
      const int on = SOFF(i + 1), xv = i + 1 < NS ? xw : xw + 99;
#pragma unroll
      for (int t = 0; t < 8; t += 2) {
        const unsigned w = cvtpk(ex(bflo(sc[t >> 1]) - mx), ex(bfhi(sc[t >> 1]) - mx));
#pragma unroll
        for (int j = 0; j < 4; ++j) { acc[2 * j] = dot2(d[t][j], w & 0xffffu, acc[2 * j]); acc[2 * j + 1] = dot2(d[t][j], w << 16, acc[2 * j + 1]); }
        PIN8(acc); WINWAIT(xv, t); LOAD(d[t], t);
#pragma unroll
        for (int j = 0; j < 4; ++j) { acc[2 * j] = dot2(d[t + 1][j], w >> 16, acc[2 * j]); acc[2 * j + 1] = dot2(d[t + 1][j], w & 0xffff0000u, acc[2 * j + 1]); }
        PIN8(acc); WINWAIT(xv, t + 1); LOAD(d[t + 1], t + 1);
      }
      SAMPLE();
    }
    *(LAS f32x2*)(L + L_REC + sw * 512 + c * 32 + r * 8) = fold_rows(acc);
    asm volatile("" :: "s"(now), "v"(scn), "v"(anch) : "memory");
    LDS_BARRIER();
    return;
  }

  __builtin_amdgcn_s_setprio(2);
  const int hw = wave;
  const rsrc_t rws = mk_rsrc(ws, WS_BYTES);
  const u64 t0 = __builtin_amdgcn_s_memrealtime();
  LAS float* tmp = (LAS float*)(L + L_TMP);
  float gate = 0.f;
  u64 epoch = 0;
  const int band = xg >> 1, cg = (xg & 1) * 32 + idx;
  {
    const rsrc_t rx = mk_rsrc(x, NB * DM * 2), rwa = mk_rsrc(wqa, DM * 4096 * 2), rwb = mk_rsrc(wqb, DM * 4096 * 2);
    const int row0 = band * 128 + hw * 32, vo = (lane >> 3) * 4 * 8192 + (lane & 7) * 16;
    u32x4 wA[4], wB[4]; u32x2 xv[NB];
#pragma unroll
    for (int i = 0; i < 4; ++i) wA[i] = bld<NT>(rwa, vo, (row0 + i) * 8192 + cg * 128);
#pragma unroll
    for (int bb = 0; bb < NB; ++bb) xv[bb] = bld2<0>(rx, (lane >> 3) * 8, (bb * DM + row0) * 2);
    if (hw == 0) epoch = __hip_atomic_load((GAS u64*)(ws + WS_EPOCH), RLX_AGENT);
    __builtin_amdgcn_sched_barrier(0);
#pragma unroll
    for (int i = 0; i < 4; ++i) wB[i] = bld<NT>(rwb, vo, (row0 + i) * 8192 + cg * 128);
    __builtin_amdgcn_sched_barrier(0);
    LDS_BARRIER();
    asm volatile("" : "+v"(wA[0]));
    __builtin_amdgcn_sched_barrier(0);
    q_partial(wA, xv, tmp + hw * 256, lane);
    asm volatile("" : "+v"(epoch));
    epoch += 1;
    if (tid == 0) { misc[M_TAGA] = (unsigned)epoch; misc[M_TAGB] = (unsigned)(epoch >> 32) ^ ((unsigned)epoch * 0x9E3779B1u); }
    q_partial(wB, xv, tmp + 1024 + hw * 256, lane);
  }
  hsync(misc, 1, lane);
  const unsigned tagA = misc[M_TAGA], tagB = misc[M_TAGB];
  if (hw == 0) {
#pragma unroll
    for (int br = 0; br < 2; ++br) {
      LAS float* part = tmp + br * 1024 + lane * 4;
      const f32x4 s = (*(LAS f32x4*)part + *(LAS f32x4*)(part + 256)) + (*(LAS f32x4*)(part + 512) + *(LAS f32x4*)(part + 768));
      bst<SC1NT>(__builtin_bit_cast(u32x4, s), rws, (lane >> 4) * 65536 + (lane & 15) * 16, WS_QP + br * 262144 + band * 16384 + cg * 256);
    }
    DRAIN();
    if (lane < 4) bst<SC1>(u32x4{0u, tagA, 0u, tagB}, rws, lane * 16, WS_QF + (cg * 4 + band) * 64);
    const u32x4 xv = bld<0>(mk_rsrc(x, NB * DM * 2), lane * 16, b * DM * 2), gv = bld<0>(mk_rsrc(gw, DM * 2), lane * 16, 0);
    float s = 0.f;
#pragma unroll
    for (int j = 0; j < 4; ++j) { const unsigned p = cvtpk(bflo(xv[j]) * bflo(gv[j]), bfhi(xv[j]) * bfhi(gv[j])); s += bflo(p); s += bfhi(p); }
    s = rbf(wave_sum(s));
    gate = rbf(1.f / (1.f + __expf(-s)));
  }
  {
    wait_time<4>(t0, T_Q);
    u32x4 f[1];
    poll<1, SC1>(f, rws, (lane & 7) * 64, 0, WS_QF + (xg * 4 + hw) * 512, tagA, tagB);
    u32x2 pb[4];
    if (hw == (h & 3)) {
      u32x2 pa[4];
      q_fetch(pa, rws, 0, b, h, lane);
      q_fetch(pb, rws, 1, b, xg * 4 + hw, lane);
      *(LAS unsigned*)(L + L_QA + lane * 4) = q_sum(pa);
      lds_signal(misc + M_QRDY, lane);
    } else q_fetch(pb, rws, 1, b, xg * 4 + hw, lane);
    *(LAS unsigned*)(L + L_QB + hw * 256 + lane * 4) = q_sum(pb);
    lds_wait_ge<2>(misc + M_BDMA, 4);
  }
  __builtin_amdgcn_s_setprio(0);
  hsync(misc, 2, lane);
  {
    u32x4 q[4];
#pragma unroll
    for (int g = 0; g < 4; ++g) q[g] = *(LAS u32x4*)(L + L_QB + g * 256 + c * 16);
    float m[4], l[4], acc[4][8];
#pragma unroll
    for (int g = 0; g < 4; ++g) { m[g] = -INFINITY; l[g] = 0.f;
#pragma unroll
      for (int j = 0; j < 8; ++j) acc[g][j] = 0.f; }
#pragma nounroll
    for (int s = 0; s < 4; ++s) {
      u32x4 k[4], v[4];
#pragma unroll
      for (int t = 0; t < 4; ++t) { k[t] = *(LAS u32x4*)(L + L_BIG + (hw * 16 + s * 4 + t) * 1024 + lane * 16); v[t] = *(LAS u32x4*)(L + L_BIG + 65536 + (hw * 16 + s * 4 + t) * 1024 + lane * 16); }
      gqa_step(k, v, q, m, l, acc);
    }
#pragma unroll
    for (int g = 0; g < 4; ++g) {
      const float M = xrow_max(m[g]), w = ex(m[g] - M), Ls = xrow_sum(l[g] * w);
#pragma unroll
      for (int j = 0; j < 8; ++j) acc[g][j] *= w;
      LAS float* rec = tmp + (g * 4 + hw) * REC_F;
      *(LAS f32x2*)(rec + 4 + c * 8 + r * 2) = fold_rows(acc[g]);
      if (lane == 0) *(LAS f32x2*)rec = f32x2{M, Ls};
    }
  }
  hsync(misc, 3, lane);
  {
    LAS float* rec = tmp + hw * 4 * REC_F;
    f32x2 ml[4], av[4];
#pragma unroll
    for (int i = 0; i < 4; ++i) { ml[i] = *(LAS f32x2*)(rec + i * REC_F); av[i] = *(LAS f32x2*)(rec + i * REC_F + 4 + lane * 2); }
    const float M = fmaxf(fmaxf(ml[0][0], ml[1][0]), fmaxf(ml[2][0], ml[3][0]));
    float Ls = 0.f, o0 = 0.f, o1 = 0.f;
#pragma unroll
    for (int i = 0; i < 4; ++i) { const float wt = ex(ml[i][0] - M); Ls = fmaf(wt, ml[i][1], Ls); o0 = fmaf(wt, av[i][0], o0); o1 = fmaf(wt, av[i][1], o1); }
    *(LAS unsigned*)(L + L_PUB + hw * 256 + lane * 4) = cvtpk(o0 / Ls, o1 / Ls);
    if (lane == 0) *(LAS f32x2*)(L + L_PUB + 1024 + hw * 8) = f32x2{M, Ls};
  }
  hsync(misc, 4, lane);
  if (hw == 0) {
    const int pi = (b * 8 + xg) * 8 + sp;
    wait_time<4>(t0, T_PB);
    next_slot();
    bst<SC1NT>(*(LAS u32x4*)(L + L_PUB + lane * 16), rws, lane * 16, WS_PBP + pi * 1024);
    const u32x2 ml = *(LAS u32x2*)(L + L_PUB + 1024 + (lane & 3) * 8);
    DRAIN();
    next_slot();
    if (lane < 4) bst<SC1NT>(u32x4{ml[0], tagA, ml[1], tagB}, rws, lane * 16, WS_PBM + pi * 64);
  }
  if (hw == 3) cp_publish(0, misc, rws, bid, tagA, lane);
  wait_time<4>(t0, T_W);
  if (hw == 3) cp_consume(0, misc, rws, tagA, lane);
  {
    const GAS unsigned char* src = (const GAS unsigned char*)(woa + (size_t)(h * 128 + hw * 2) * DM) + lane * 16;
#pragma nounroll
    for (int i0 = 0; i0 < 32; i0 += 4) {
#pragma unroll
      for (int i = i0; i < i0 + 4; ++i) { const int o = (i >> 1) * 8192 + (i & 1) * 1024; CAPWAIT(2); glds16<SC1>(src + o, lds0 + L_BIG + hw * 2048 + o); }
      if (hw == 3) { if (i0 == 12) cp_publish(1, misc, rws, bid, tagA, lane); else if (i0 == 20) cp_consume(1, misc, rws, tagA, lane); }
      if (hw == 0 && i0 == 12) {
        u32x4 ml[1];
        poll<1, SC1NT>(ml, rws, (lane >> 3) * 64, 0, WS_PBM + (b * 8 + xg) * 512 + (h & 3) * 16, tagA, tagB);
        const u32x4 av = bld<SC1NT>(rws, (lane >> 3) * 1024 + (lane & 7) * 16, WS_PBP + (b * 8 + xg) * 8192 + (h & 3) * 256 + side * 128);
        const float pm = __uint_as_float(ml[0][0]), M = xrow_max(fmaxf(pm, dpp<XOR8>(pm)));
        const float wt = ex(pm - M) * __uint_as_float(ml[0][2]), Ls = xrow_sum(wt + dpp<XOR8>(wt));
        float o[8];
#pragma unroll
        for (int j = 0; j < 4; ++j) { o[2 * j] = wt * bflo(av[j]); o[2 * j + 1] = wt * bfhi(av[j]); }
        const f32x2 f = fold_rows(o);
        *(LAS unsigned*)(L + L_PUB + (lane & 7) * 16 + r * 4) = cvtpk((f[0] + dpp<XOR8>(f[0])) / Ls, (f[1] + dpp<XOR8>(f[1])) / Ls);
      }
    }
    DRAIN();
    hsync(misc, 5, lane);
    if (hw == 3) cp_publish(2, misc, rws, bid, tagA, lane);
  }
  unsigned w0[64], w1[64];
#pragma unroll
  for (int p = 0; p < 64; ++p) {
    const unsigned a = *(LAS unsigned*)(L + L_BIG + p * 2048 + tid * 4), bb = *(LAS unsigned*)(L + L_BIG + p * 2048 + 1024 + tid * 4);
    w0[p] = plo(bb, a); w1[p] = phi(bb, a);
    asm volatile("" : "+v"(w0[p]), "+v"(w1[p]));
  }
  hsync(misc, 6, lane);
  unsigned zpk;
  {
    const GAS unsigned char* src = (const GAS unsigned char*)(wob + (size_t)(h * 128 + side * 64 + hw * 2) * DM) + lane * 16;
    if (hw == 3) cp_consume(2, misc, rws, tagA, lane);
#pragma unroll 4
    for (int i = 0; i < 16; ++i) { const int o = (i >> 1) * 8192 + (i & 1) * 1024; CAPWAIT(2); glds16<SC1>(src + o, lds0 + L_BIG + hw * 2048 + o); }
    DRAIN();
    hsync(misc, 7, lane);
    float z0 = 0.f, z1 = 0.f;
#pragma unroll 4
    for (int p = 0; p < 32; ++p) {
      const unsigned a = *(LAS unsigned*)(L + L_BIG + p * 2048 + tid * 4), bb = *(LAS unsigned*)(L + L_BIG + p * 2048 + 1024 + tid * 4), op = *(LAS unsigned*)(L + L_PUB + p * 4);
      z0 = dot2(plo(bb, a), op, z0); z1 = dot2(phi(bb, a), op, z1);
    }
    zpk = cvtpk(z0, z1);
    asm volatile("" : "+v"(zpk));
  }
  {
    lds_wait_ge<32>(misc + M_KDONE, 4);
    const f32x4 wm = *(LAS f32x4*)(miscf + M_WMAX);
    const float M = fmaxf(fmaxf(wm[0], wm[1]), fmaxf(wm[2], wm[3]));
    float ls = 0.f;
#pragma unroll
    for (int i = 0; i < 2; ++i) {
      const u32x4 sc = *(LAS u32x4*)(L + L_SC + hw * 2048 + i * 1024 + lane * 16);
#pragma unroll
      for (int j = 0; j < 4; ++j) ls += ex(bflo(sc[j]) - M) + ex(bfhi(sc[j]) - M);
    }
    ls = wave_sum(ls);
    if (lane == 0) miscf[M_LSUM + hw] = ls;
    hsync(misc, 8, lane);
    if (hw == 0) {
      const f32x4 lw = *(LAS f32x4*)(miscf + M_LSUM);
      const float Ls = (lw[0] + lw[1]) + (lw[2] + lw[3]);
      if (lane < 4) bst<SC1NT>(u32x4{__float_as_uint(M), tagA, __float_as_uint(Ls), tagB}, rws, lane * 16, WS_ST + (b * 64 + jp) * 64);
      wait_time<32>(__builtin_amdgcn_s_memrealtime(), T_ST);
      u32x4 o[1];
      poll<1, SC1NT>(o, rws, 0, 0, WS_ST + (b * 64 + (jp ^ 1)) * 64, tagA, tagB);
      const float Mo = __uint_as_float(o[0][0]), MM = fmaxf(M, Mo);
      if (lane < 4) miscf[M_CNORM + lane] = ex(miscf[M_WMAX + lane] - MM) / (ex(M - MM) * Ls + ex(Mo - MM) * __uint_as_float(o[0][2]));
      if (bid == 0) {
        u32x4 all[4];
        wait_time<32>(__builtin_amdgcn_s_memrealtime(), T_ST);
        poll<4, SC1>(all, rws, lane * 64, 4096, WS_ST, tagA, tagB);
        if (lane == 0) __hip_atomic_store((GAS u64*)(ws + WS_EPOCH), epoch, RLX_AGENT);
      }
    } else if (hw == 3) {
      int k = 3, state = 0, rho = 0, rprev = RHO_MAX, have = 0, TP = 0;
      unsigned P = __builtin_amdgcn_readfirstlane(__hip_atomic_load(misc + M_ANCH, RLX_WG));
#pragma nounroll
      for (unsigned spins = 0; spins < SPIN_CAP && (unsigned)__builtin_amdgcn_readfirstlane(__hip_atomic_load(misc + M_STEP, RLX_WG)) < NS - 3; ++spins) {
        const int t = (int)(__builtin_amdgcn_s_memrealtime() - t0), tk = T_CP0 + (k - 3) * T_CP;
        if (state == 0 && t >= tk && k < NCP) { if (t < tk + T_CPLAG / 2) cp_publish(k, misc, rws, bid, tagA, lane); state = 1; }
        else if (state == 1 && t >= tk + T_CPLAG) {
          const unsigned Pe = P + (((tk - TP) * rho) >> 10), Pn = cp_mean(k, rws, Pe, tagA, lane);
          if (have) { const int rn = min(max((int)(Pn - P) * 1024 / T_CP, RHO_MIN), RHO_MAX); rho = min(rn, rprev); rprev = rn; }
          have = have || Pn != Pe; P = Pn; TP = tk; state = 0; ++k;
        }
        if (lane == 0) __hip_atomic_store(misc + M_ANCH, P + (((t - TP) * rho) >> 10), RLX_WG);
        __builtin_amdgcn_s_sleep(16);
      }
    }
  }
  int yoff = (tid >> 2) * 4096 + (tid & 3) * 16, goff = lane * 64;
  float gsel = lane < 32 ? gate : rbf(1.f - gate);
  asm volatile("" : "+v"(yoff), "+v"(goff), "+v"(gsel));
  if (hw == 1 && idx < 2) {
    const unsigned pf = *(const GAS unsigned*)(code_here() + (lane & 31) * 128);
    asm volatile("" :: "v"(pf));
  }
  LDS_BARRIER();
  const unsigned yanch = misc[M_ANCH];
  {
    const f32x4 cn = *(LAS f32x4*)(miscf + M_CNORM);
    f32x4 rl[4], rh[4];
#pragma unroll
    for (int w = 0; w < 4; ++w) { rl[w] = *(LAS f32x4*)(L + L_REC + w * 512 + c * 32); rh[w] = *(LAS f32x4*)(L + L_REC + w * 512 + c * 32 + 16); }
    __builtin_amdgcn_sched_barrier(0);
    f32x4 lo = cn[0] * rl[0], hi = cn[0] * rh[0];
#pragma unroll
    for (int w = 1; w < 4; ++w) { lo += cn[w] * rl[w]; hi += cn[w] * rh[w]; }
    unsigned ob[4] = {cvtpk(lo[0], lo[1]), cvtpk(lo[2], lo[3]), cvtpk(hi[0], hi[1]), cvtpk(hi[2], hi[3])};
    float y0 = 0.f, y1 = 0.f, y2 = 0.f, y3 = 0.f;
    asm volatile("s_nop 1" : "+v"(ob[0]), "+v"(ob[1]), "+v"(ob[2]), "+v"(ob[3]), "+v"(y0), "+v"(y1), "+v"(y2), "+v"(y3));
    Proj<0>::run(y0, y1, y2, y3, w0, w1, ob);
    asm volatile("s_nop 2" : "+v"(y0), "+v"(y1), "+v"(y2), "+v"(y3));
    const unsigned ypk = cvtpk(y0 + y2, y1 + y3);
    bst<SC1>(u32x4{plo(zpk, ypk), tagA, phi(zpk, ypk), tagB}, rws, yoff, WS_YG + b * 64 * 4096 + jp * 64);
  }
  if (hw == 0) {
    {
      const u64 tp = __builtin_amdgcn_s_memrealtime();
      const int dl = min(max((int)(__builtin_amdgcn_readfirstlane(yanch) + T_Y - (unsigned)tp), Y_MIN), Y_MAX);
#pragma nounroll
      while ((int)(__builtin_amdgcn_s_memrealtime() - tp) < dl) {}
    }
    u32x4 g[4];
    poll<4, SC1>(g, rws, goff, 16, WS_YG + (b * 64 + jp) * 4096, tagA, tagB);
    float u[8];
#pragma unroll
    for (int j = 0; j < 8; ++j) u[j] = fold32(bflo(g[j >> 1][(j & 1) * 2]), bfhi(g[j >> 1][(j & 1) * 2]));
#pragma unroll
    for (int j = 0; j < 4; ++j) u[j] = fold16(u[j], u[j + 4]);
#pragma unroll
    for (int j = 0; j < 2; ++j) u[j] = foldd<XOR8>(u[j], u[j + 2], lane & 8);
    u[0] = foldd<XOR7>(u[0], u[1], lane & 4);
    u[0] += dpp<XOR2>(u[0]); u[0] += dpp<XOR1>(u[0]);
    const float t = rbf(rbf(u[0]) * gsel);
    const unsigned res = cvtpk(fold32(t, t), 0.f);
    if (lane < 32 && (lane & 3) == 0) __builtin_amdgcn_raw_buffer_store_b16((u16)res, mk_rsrc(out, NB * DM * 2), (lane >> 2) * 2, (b * DM + jp * 8) * 2, SC1);
  }
}

extern "C" void kernel_launch(void* const* d_in, const int* in_sizes, int n_in, void* d_out, int out_size, void* d_ws, size_t ws_size, hipStream_t stream) {
  k_fused<<<256, 512, 0, stream>>>((const u16*)d_in[0], (const u16*)d_in[1], (const u16*)d_in[2], (const u16*)d_in[3], (const u16*)d_in[4], (const u16*)d_in[6], (const u16*)d_in[7],
                                   (const u16*)d_in[8], (const u16*)d_in[9], (const u16*)d_in[10], (u16*)d_out, (unsigned char*)d_ws);
}
```

```cpp
#include <hip/hip_runtime.h>
#include <hip/hip_bf16.h>
#include <cstdint>

typedef unsigned u32x4 __attribute__((ext_vector_type(4)));
typedef unsigned u32x2 __attribute__((ext_vector_type(2)));
typedef float f32x4 __attribute__((ext_vector_type(4)));
typedef float f32x2 __attribute__((ext_vector_type(2)));
typedef __attribute__((ext_vector_type(2))) __bf16 bf16x2;
typedef unsigned long long u64;
typedef unsigned short u16;
typedef __amdgpu_buffer_rsrc_t rsrc_t;
#define GAS __attribute__((address_space(1)))
#define LAS __attribute__((address_space(3)))
#define RLX_AGENT __ATOMIC_RELAXED, __HIP_MEMORY_SCOPE_AGENT
#define RLX_WG __ATOMIC_RELAXED, __HIP_MEMORY_SCOPE_WORKGROUP
#define DRAIN() asm volatile("s_waitcnt vmcnt(0)" ::: "memory")
#define LDS_BARRIER() do { asm volatile("s_waitcnt lgkmcnt(0)" ::: "memory"); __builtin_amdgcn_s_barrier(); asm volatile("" ::: "memory"); } while (0)
#define CAPWAIT(n) do { __builtin_amdgcn_sched_barrier(0); asm volatile("s_waitcnt vmcnt(%0)" :: "i"((n) - 1) : "memory"); } while (0)

constexpr int NB = 4, DM = 512, NH = 32, SK = 8192, DH = 128, WIN = 2048;
constexpr float SCALE = 0.088388347648318440f, L2E = 1.4426950408889634f;
constexpr int NT = 2, SC1 = 16, SC1NT = 18, RSRC_W3 = 0x00020000;
constexpr int NS = 32;
constexpr unsigned SPIN_CAP = 1u << 18;

constexpr int CAP0 = 5;
constexpr int TAU = 108;
constexpr int TK0 = 813;
constexpr int XCENTER = 11, XMIN = 6, XGAIN = 10;
constexpr int END_STEPS = 8, XCENTER_END = 9, XMIN_END = 2, XGAIN_END = 45;
constexpr int T_Q = 540, T_PB = 1680, T_W = 1850, T_ST = 500, SLOT = 64;
constexpr int T_CP0 = 4600, T_CP = 500, T_CPLAG = 200, RHO_MIN = -20, RHO_MAX = 60;
constexpr int T_Y = 63 * TAU + 190, Y_MIN = 40, Y_MAX = 72;

constexpr int WS_EPOCH = 0;
constexpr int WS_PH = 4096, NCP = 14;
constexpr int WS_QF = 32768;
constexpr int WS_QP = WS_QF + 16384;
constexpr int WS_PBM = WS_QP + 524288;
constexpr int WS_ST = WS_PBM + 16384;
constexpr int WS_PBP = WS_ST + 16384;
constexpr int WS_YG = WS_PBP + 262144;
constexpr int WS_BYTES = WS_YG + 256 * 4096;

constexpr int L_MISC = 0, L_QA = 256, L_QB = 512, L_REC = 1536, L_SC = 4096, L_TMP = 12288, L_PUB = L_TMP + 8704, L_BIG = 22528, L_DUMP = L_BIG + 131072, L_BYTES = L_DUMP + 1024;
constexpr int REC_F = 132;
enum { M_TAGA = 0, M_TAGB = 1, M_ANCH = 2, M_STEP = 3, M_QRDY = 4, M_HSYNC = 5, M_KDONE = 6, M_BDMA = 7, M_WMAX = 8, M_LSUM = 12, M_CNORM = 16, M_PH = 20, M_GATE = 24 };

__device__ __forceinline__ float dot2(unsigned a, unsigned b, float c) {
  return __builtin_amdgcn_fdot2_f32_bf16(__builtin_bit_cast(bf16x2, a), __builtin_bit_cast(bf16x2, b), c, false);
}
__device__ __forceinline__ unsigned cvtpk(float lo, float hi) { return __builtin_bit_cast(unsigned, __builtin_convertvector(f32x2{lo, hi}, bf16x2)); }
__device__ __forceinline__ float bflo(unsigned u) { return __uint_as_float(u << 16); }
__device__ __forceinline__ float bfhi(unsigned u) { return __uint_as_float(u & 0xffff0000u); }
__device__ __forceinline__ float rbf(float x) { return bflo(cvtpk(x, 0.f)); }
__device__ __forceinline__ float ex(float x) { return __builtin_amdgcn_exp2f(x * L2E); }
__device__ __forceinline__ unsigned plo(unsigned hi, unsigned lo) { return __builtin_amdgcn_perm(hi, lo, 0x05040100u); }
__device__ __forceinline__ unsigned phi(unsigned hi, unsigned lo) { return __builtin_amdgcn_perm(hi, lo, 0x07060302u); }
template <int CTRL> __device__ __forceinline__ float dpp(float x) {
  return __builtin_bit_cast(float, __builtin_amdgcn_mov_dpp(__builtin_bit_cast(int, x), CTRL, 0xf, 0xf, true));
}
constexpr int XOR1 = 0xB1, XOR2 = 0x4E, XOR7 = 0x141, XOR8 = 0x128;
__device__ __forceinline__ float row16_sum(float x) { x += dpp<XOR1>(x); x += dpp<XOR2>(x); x += dpp<XOR7>(x); x += dpp<XOR8>(x); return x; }
__device__ __forceinline__ float row16_max(float x) { x = fmaxf(x, dpp<XOR1>(x)); x = fmaxf(x, dpp<XOR2>(x)); x = fmaxf(x, dpp<XOR7>(x)); return fmaxf(x, dpp<XOR8>(x)); }
template <int CTRL> __device__ __forceinline__ float foldd(float a, float b, bool hi) { return (hi ? b : a) + dpp<CTRL>(hi ? a : b); }
__device__ __forceinline__ float fold32(float x, float y) {
  auto t = __builtin_amdgcn_permlane32_swap(__float_as_uint(x), __float_as_uint(y), false, false);
  return __uint_as_float(t[0]) + __uint_as_float(t[1]);
}
__device__ __forceinline__ float fold16(float x, float y) {
  auto t = __builtin_amdgcn_permlane16_swap(__float_as_uint(x), __float_as_uint(y), false, false);
  return __uint_as_float(t[0]) + __uint_as_float(t[1]);
}
__device__ __forceinline__ float xrow_sum(float x) { x = fold16(x, x); return fold32(x, x); }
__device__ __forceinline__ float xrow_max(float x) {
  auto s = __builtin_amdgcn_permlane16_swap(__float_as_uint(x), __float_as_uint(x), false, false);
  x = fmaxf(__uint_as_float(s[0]), __uint_as_float(s[1]));
  auto t = __builtin_amdgcn_permlane32_swap(__float_as_uint(x), __float_as_uint(x), false, false);
  return fmaxf(__uint_as_float(t[0]), __uint_as_float(t[1]));
}
__device__ __forceinline__ float wave_sum(float x) { return xrow_sum(row16_sum(x)); }
__device__ __forceinline__ f32x2 fold_rows(const float (&a)[8]) {
  const float u0 = fold32(a[0], a[4]), u1 = fold32(a[1], a[5]), u2 = fold32(a[2], a[6]), u3 = fold32(a[3], a[7]);
  return f32x2{fold16(u0, u2), fold16(u1, u3)};
}

__device__ __forceinline__ rsrc_t mk_rsrc(const void* p, int bytes) { return __builtin_amdgcn_make_buffer_rsrc(const_cast<void*>(p), 0, bytes, RSRC_W3); }
template <int AUX> __device__ __forceinline__ u32x4 bld(rsrc_t r, int voff, int soff) { return __builtin_amdgcn_raw_buffer_load_b128(r, voff, soff, AUX); }
template <int AUX> __device__ __forceinline__ u32x2 bld2(rsrc_t r, int voff, int soff) { return __builtin_amdgcn_raw_buffer_load_b64(r, voff, soff, AUX); }
template <int AUX> __device__ __forceinline__ void bst(u32x4 v, rsrc_t r, int voff, int soff) { __builtin_amdgcn_raw_buffer_store_b128(v, r, voff, soff, AUX); }
template <int AUX> __device__ __forceinline__ void glds16(const GAS unsigned char* src, unsigned dst) {
  unsigned keep;
  if (AUX == NT) asm volatile("s_mov_b32 %0, m0\n\ts_mov_b32 m0, %2\n\ts_nop 0\n\tglobal_load_lds_dwordx4 %1, off nt\n\ts_mov_b32 m0, %0" : "=&s"(keep) : "v"(src), "s"(dst) : "memory");
  else asm volatile("s_mov_b32 %0, m0\n\ts_mov_b32 m0, %2\n\ts_nop 0\n\tglobal_load_lds_dwordx4 %1, off sc1\n\ts_mov_b32 m0, %0" : "=&s"(keep) : "v"(src), "s"(dst) : "memory");
}
__device__ __forceinline__ void l2_pin(const GAS unsigned char* p, unsigned dump, int lane) {
  unsigned keep;
  asm volatile("s_mov_b32 %0, m0\n\ts_mov_b32 m0, %3\n\ts_nop 0\n\tglobal_load_lds_dwordx4 %1, off nt\n\tglobal_load_lds_dword %2, off sc1\n\ts_mov_b32 m0, %0"
               : "=&s"(keep) : "v"(p + lane * 16), "v"(p + (lane & 7) * 128), "s"(dump) : "memory");
}
__device__ __forceinline__ const GAS unsigned char* code_here() { u64 pc; asm volatile("s_getpc_b64 %0" : "=s"(pc)); return (const GAS unsigned char*)(pc & ~127ull); }

#define DITH(t) ((((t) & 1) << 2) | ((t) & 2) | (((t) & 4) >> 2))
#define WINWAIT(x, t) do { __builtin_amdgcn_sched_barrier(0); asm volatile("s_cmp_gt_i32 %0, %2\n\ts_cbranch_scc1 2f\n\ts_cmp_gt_i32 %0, %1\n\ts_cbranch_scc1 1f\n\ts_waitcnt vmcnt(3)\n1:\n\ts_waitcnt vmcnt(4)\n" \
    "2:\n\ts_cmp_gt_i32 %0, %3\n\ts_cbranch_scc1 3f\n\ts_waitcnt vmcnt(5)\n3:\n\ts_waitcnt vmcnt(6)" :: "s"(x), "i"(7 - DITH(t)), "i"(15 - DITH(t)), "i"(23 - DITH(t)) : "memory", "scc"); } while (0)

template <int SLEEP> __device__ __forceinline__ void wait_time(u64 t0, unsigned ticks) {
#pragma nounroll
  while (__builtin_amdgcn_s_memrealtime() - t0 < ticks) __builtin_amdgcn_s_sleep(SLEEP);
}
__device__ __forceinline__ void next_slot() {
  const unsigned n0 = (unsigned)__builtin_amdgcn_s_memrealtime(), left = SLOT - n0 % SLOT;
#pragma nounroll
  while ((unsigned)__builtin_amdgcn_s_memrealtime() - n0 < left) __builtin_amdgcn_s_sleep(1);
  asm volatile("" ::: "memory");
}
template <int SLEEP> __device__ __forceinline__ void lds_wait_ge(LAS unsigned* p, unsigned target) {
#pragma nounroll
  for (unsigned spins = 0; spins < 32 * SPIN_CAP && (unsigned)__builtin_amdgcn_readfirstlane(__hip_atomic_load(p, RLX_WG)) < target; ++spins) __builtin_amdgcn_s_sleep(SLEEP);
  asm volatile("" ::: "memory");
}
__device__ __forceinline__ void lds_signal(LAS unsigned* p, int lane) {
  asm volatile("s_waitcnt lgkmcnt(0)" ::: "memory");
  if (lane == 0) __builtin_amdgcn_atomic_inc32(p, 0xffffffffu, __ATOMIC_RELAXED, "workgroup");
}
__device__ __forceinline__ void hsync(LAS unsigned* misc, unsigned n, int lane) { lds_signal(misc + M_HSYNC, lane); lds_wait_ge<2>(misc + M_HSYNC, 4 * n); }
template <int N, int AUX> __device__ __forceinline__ void poll(u32x4 (&g)[N], rsrc_t rws, int voff, int stride, int soff, unsigned tagA, unsigned tagB) {
#pragma nounroll
  for (unsigned spins = 0; spins < SPIN_CAP; ++spins) {
    asm volatile("" ::: "memory");
    bool ok = true;
#pragma unroll
    for (int i = 0; i < N; ++i) g[i] = bld<AUX>(rws, voff + i * stride, soff);
#pragma unroll
    for (int i = 0; i < N; ++i) ok &= (g[i][1] == tagA) & (g[i][3] == tagB);
    if (__all(ok)) break;
    __builtin_amdgcn_s_sleep(2);
  }
  __builtin_amdgcn_fence(__ATOMIC_ACQUIRE, "wavefront");
  asm volatile("" ::: "memory");
}
__device__ __forceinline__ void cp_publish(int k, LAS unsigned* misc, rsrc_t rws, int bid, unsigned tagA, int lane) {
  if ((((bid >> 3) + k) & 15) == 0) {
    asm volatile("" ::: "memory");
    const u32x4 wp = *(LAS u32x4*)(misc + M_PH);
    const unsigned ph = wp[0] + (((int)(wp[1] - wp[0]) + (int)(wp[2] - wp[0]) + (int)(wp[3] - wp[0])) >> 2);
    if (lane < 8) __builtin_amdgcn_raw_buffer_store_b64(u32x2{ph, tagA}, rws, lane * 8, WS_PH + k * 2048 + ((bid & 7) + ((bid >> 7) << 3)) * 64, SC1);
  }
}
__device__ __forceinline__ unsigned cp_mean(int k, rsrc_t rws, unsigned ref, unsigned tagA, int lane) {
  asm volatile("" ::: "memory");
  const u32x2 g = bld2<SC1>(rws, (lane & 15) * 64, WS_PH + k * 2048);
  const bool ok = g[1] == tagA;
  const float d = wave_sum(ok ? (float)(int)(g[0] - ref) : 0.f), n = wave_sum(ok ? 1.f : 0.f);
  return __builtin_amdgcn_readfirstlane(n >= 32.f ? ref + (int)(d / n) : ref);
}
__device__ __forceinline__ void cp_consume(int k, LAS unsigned* misc, rsrc_t rws, unsigned tagA, int lane) {
  const unsigned a = cp_mean(k, rws, __hip_atomic_load(misc + M_ANCH, RLX_WG), tagA, lane);
  if (lane == 0) __hip_atomic_store(misc + M_ANCH, a, RLX_WG);
}

__device__ __forceinline__ void q_partial(const u32x4 (&w)[4], const u32x2 (&xv)[NB], LAS float* part, int lane) {
  float a[NB][8];
#pragma unroll
  for (int j = 0; j < 4; ++j) {
    const unsigned p0 = plo(w[1][j], w[0][j]), p1 = phi(w[1][j], w[0][j]), p2 = plo(w[3][j], w[2][j]), p3 = phi(w[3][j], w[2][j]);
#pragma unroll
    for (int bb = 0; bb < NB; ++bb) { a[bb][2 * j] = dot2(p2, xv[bb][1], dot2(p0, xv[bb][0], 0.f)); a[bb][2 * j + 1] = dot2(p3, xv[bb][1], dot2(p1, xv[bb][0], 0.f)); }
  }
  float f[8];
#pragma unroll
  for (int j = 0; j < 8; ++j) f[j] = fold16(fold32(a[0][j], a[2][j]), fold32(a[1][j], a[3][j]));
#pragma unroll
  for (int j = 0; j < 4; ++j) f[j] = foldd<XOR8>(f[j], f[j + 4], lane & 8);
  *(LAS f32x4*)(part + (lane >> 4) * 64 + (lane & 7) * 8 + ((lane >> 3) & 1) * 4) = f32x4{f[0], f[1], f[2], f[3]};
}
__device__ __forceinline__ void q_fetch(u32x2 (&p)[4], rsrc_t rws, int br, int b, int hq, int lane) {
#pragma unroll
  for (int i = 0; i < 4; ++i) p[i] = bld2<SC1NT>(rws, lane * 8 + i * 16384, WS_QP + br * 262144 + b * 65536 + hq * 512);
}
__device__ __forceinline__ unsigned q_sum(const u32x2 (&p)[4]) {
  const f32x2 s = (__builtin_bit_cast(f32x2, p[0]) + __builtin_bit_cast(f32x2, p[1])) + (__builtin_bit_cast(f32x2, p[2]) + __builtin_bit_cast(f32x2, p[3]));
  return cvtpk(s[0], s[1]);
}
__device__ __forceinline__ void gqa_step(const u32x4 (&k)[4], const u32x4 (&v)[4], const u32x4 (&q)[4], float (&m)[4], float (&l)[4], float (&acc)[4][8]) {
#pragma unroll
  for (int g = 0; g < 4; ++g) {
    float s[4];
#pragma unroll
    for (int t = 0; t < 4; ++t) s[t] = dot2(k[t][3], q[g][3], dot2(k[t][2], q[g][2], dot2(k[t][1], q[g][1], dot2(k[t][0], q[g][0], 0.f))));
#pragma unroll
    for (int t = 0; t < 4; t += 2) {
      const unsigned r1 = cvtpk(row16_sum(s[t]), row16_sum(s[t + 1]));
      const unsigned r2 = cvtpk(bflo(r1) * SCALE, bfhi(r1) * SCALE);
      s[t] = bflo(r2); s[t + 1] = bfhi(r2);
    }
    const float mn = fmaxf(fmaxf(m[g], fmaxf(s[0], s[1])), fmaxf(s[2], s[3]));
    const float al = ex(m[g] - mn);
    m[g] = mn;
    float ls = l[g] * al;
#pragma unroll
    for (int j = 0; j < 8; ++j) acc[g][j] *= al;
#pragma unroll
    for (int t = 0; t < 4; t += 2) {
      const float p0 = ex(s[t] - mn), p1 = ex(s[t + 1] - mn);
      ls += p0 + p1;
      const unsigned w = cvtpk(p0, p1);
#pragma unroll
      for (int j = 0; j < 4; ++j) {
        acc[g][2 * j] = dot2(v[t + 1][j], w >> 16, dot2(v[t][j], w & 0xffffu, acc[g][2 * j]));
        acc[g][2 * j + 1] = dot2(v[t + 1][j], w & 0xffff0000u, dot2(v[t][j], w << 16, acc[g][2 * j + 1]));
      }
    }
    l[g] = ls;
  }
}
template <int C> struct Proj {
  static __device__ __forceinline__ void run(float& y0, float& y1, float& y2, float& y3, const unsigned (&w0)[64], const unsigned (&w1)[64], const unsigned (&ob)[4]) {
#define DOTB(y, o, w) "v_dot2c_f32_bf16_dpp " y ", " o ", " w " row_newbcast:%16 row_mask:0xf bank_mask:0xf\n\t"
    asm volatile(DOTB("%0", "%4", "%8") DOTB("%1", "%4", "%12") DOTB("%2", "%5", "%9") DOTB("%3", "%5", "%13") DOTB("%0", "%6", "%10") DOTB("%1", "%6", "%14") DOTB("%2", "%7", "%11") DOTB("%3", "%7", "%15")
                 : "+v"(y0), "+v"(y1), "+v"(y2), "+v"(y3)
                 : "v"(ob[0]), "v"(ob[1]), "v"(ob[2]), "v"(ob[3]), "v"(w0[4 * C]), "v"(w0[4 * C + 1]), "v"(w0[4 * C + 2]), "v"(w0[4 * C + 3]),
                   "v"(w1[4 * C]), "v"(w1[4 * C + 1]), "v"(w1[4 * C + 2]), "v"(w1[4 * C + 3]), "i"(C));
#undef DOTB
    if constexpr (C + 1 < 16) Proj<C + 1>::run(y0, y1, y2, y3, w0, w1, ob);
  }
};

__global__ void __launch_bounds__(512, 2) k_fused(const u16* __restrict__ x, const u16* __restrict__ ka, const u16* __restrict__ va, const u16* __restrict__ wqa, const u16* __restrict__ woa,
                                                  const u16* __restrict__ kb, const u16* __restrict__ vb, const u16* __restrict__ wqb, const u16* __restrict__ wob,
                                                  const u16* __restrict__ gw, u16* __restrict__ out, unsigned char* __restrict__ ws) {
  __shared__ __attribute__((aligned(1024))) unsigned char smem[L_BYTES];
  LAS unsigned char* L = (LAS unsigned char*)smem;
  LAS unsigned* misc = (LAS unsigned*)(L + L_MISC);
  LAS float* miscf = (LAS float*)(L + L_MISC);
  const unsigned lds0 = (unsigned)(size_t)L;
  const int tid = threadIdx.x, lane = tid & 63, wave = __builtin_amdgcn_readfirstlane(tid >> 6), r = lane >> 4, c = lane & 15;
  const int bid = blockIdx.x, xg = bid & 7, idx = bid >> 3, b = idx & 3, sp = idx >> 2;
  const int side = sp & 1, h = xg * 4 + (idx >> 3), jp = h * 2 + side;
  asm volatile("" :: "s"(x), "s"(ka), "s"(va), "s"(wqa), "s"(woa), "s"(kb), "s"(vb), "s"(wqb), "s"(wob), "s"(gw), "s"(out), "s"(ws));

  if (wave >= 4) {
    __builtin_amdgcn_s_setprio(1);
    const int sw = wave - 4, voff = lane * 16;
    if (tid == 256) {
      const unsigned a0 = (unsigned)__builtin_amdgcn_s_memrealtime() + TK0;
      *(LAS u32x2*)(misc + M_ANCH) = u32x2{a0, 0u}; *(LAS u32x4*)(misc + M_QRDY) = u32x4{0u, 0u, 0u, 0u}; *(LAS u32x4*)(misc + M_PH) = u32x4{a0, a0, a0, a0};
    }
    if (sw == 3 && idx < 5) l2_pin(code_here() + idx * 1024, lds0 + L_DUMP, lane);
    LDS_BARRIER();
    {
      const size_t o = ((size_t)(b * 8 + xg) * SK + (SK - WIN) + sp * 64 + sw * 16) * DH;
      const GAS unsigned char* pk = (const GAS unsigned char*)(kb + o) + voff;
      const GAS unsigned char* pv = (const GAS unsigned char*)(vb + o) + voff;
#pragma unroll 4
      for (int i = 0; i < 16; ++i) { CAPWAIT(CAP0); glds16<NT>(pk + (((i >> 2) + xg) & 3) * 131072 + (i & 3) * 1024, lds0 + L_BIG + sw * 4096 + (i >> 2) * 16384 + (i & 3) * 1024); }
#pragma unroll 4
      for (int i = 0; i < 16; ++i) { CAPWAIT(CAP0); glds16<NT>(pv + (((i >> 2) + xg) & 3) * 131072 + (i & 3) * 1024, lds0 + L_BIG + 65536 + sw * 4096 + (i >> 2) * 16384 + (i & 3) * 1024); }
    }
    {
      const size_t o = (size_t)(h * 128 + (side * 4 + b) * 16 + sw * 4) * DM;
#pragma unroll
      for (int i = 0; i < 8; ++i) { CAPWAIT(i < 4 ? CAP0 + i : 7); l2_pin((const GAS unsigned char*)((i < 4 ? wob : woa) + o) + (i & 3) * 1024, lds0 + L_DUMP, lane); }
    }
    lds_signal(misc + M_BDMA, lane);
    const u16* kp = ka + (size_t)(b * NH + h) * SK * DH;
    const u16* vp = va + (size_t)(b * NH + h) * SK * DH;
#define SOFF(i) ((((((i) + h) & (NS - 1)) * 2 + side) << 15) + sw * 2048)
#define LOAD(dst, t) dst = bld<NT>(rn, voff + ((t) & 1) * 1024, on + ((t) >> 1) * 8192); __builtin_amdgcn_sched_barrier(0)
#define QK(v) dot2(v[3], q[3], dot2(v[2], q[2], dot2(v[1], q[1], dot2(v[0], q[0], 0.f))))
#define SCORES(i)        \
      _Pragma("unroll") for (int j = 0; j < 4; ++j) s[j] = foldd<XOR7>(s[j], s[j + 4], lane & 4); \
      _Pragma("unroll") for (int j = 0; j < 2; ++j) s[j] = foldd<XOR2>(s[j], s[j + 2], lane & 2); \
      s[0] = foldd<XOR1>(s[0], s[1], lane & 1); \
      s[0] += dpp<XOR8>(s[0]);                                              \
      const unsigned sb = cvtpk(rbf(s[0]) * SCALE, 0.f);                    \
      mx = fmaxf(mx, bflo(sb)); \
      *(LAS u16*)(L + L_SC + (i) * 256 + sw * 64 + r * 16 + (c & 7) * 2) = (u16)sb
#define SAMPLE() now = __builtin_amdgcn_s_memrealtime(); anch = __hip_atomic_load(misc + M_ANCH, RLX_WG); __builtin_amdgcn_sched_barrier(0)
#define PACE(i)          \
      asm volatile("" : "+s"(now)); \
      const unsigned ph = (unsigned)now - (i) * TAU; \
      const bool eg = (i) >= 2 * NS - END_STEPS; \
      const int xw = max((eg ? XCENTER_END : XCENTER) + (((int)(ph - __builtin_amdgcn_readfirstlane(anch)) * (eg ? XGAIN_END : XGAIN)) >> 8), eg ? XMIN_END : XMIN); \
      __hip_atomic_store(misc + M_PH + sw, ph, RLX_WG)
    u32x4 d[8], q;
    u64 now; unsigned anch;
    float mx = -INFINITY;
    {
      const rsrc_t rn = mk_rsrc(kp, SK * 256);
      u32x4 e[8];
      float s[8];
      int on = SOFF(0);
#pragma unroll
      for (int t = 0; t < 8; ++t) { CAPWAIT(t < 4 ? CAP0 + 4 - t : CAP0); LOAD(e[t], t); }
      on = SOFF(1);
#pragma unroll
      for (int t = 0; t < 8; ++t) {
        if (t != 4) CAPWAIT(CAP0);
        LOAD(d[t], t);
        if (t == 4) { lds_wait_ge<2>(misc + M_QRDY, 1); q = *(LAS u32x4*)(L + L_QA + c * 16); asm volatile("" : "+v"(q)); }
        if (t >= 4) { s[2 * t - 8] = QK(e[2 * t - 8]); s[2 * t - 7] = QK(e[2 * t - 7]); asm volatile("" : "+v"(s[2 * t - 8]), "+v"(s[2 * t - 7])); }
      }
      SAMPLE();
      SCORES(0);
    }
#pragma nounroll
    for (int i = 1; i < NS; ++i) {
      PACE(i);
      const rsrc_t rn = mk_rsrc(i + 1 < NS ? kp : vp, SK * 256);
      const int on = SOFF(i + 1), xe = i + 1 < NS ? xw : xw + 16;
      float s[8];
#pragma unroll
      for (int t = 0; t < 8; ++t) { s[t] = QK(d[t]); asm volatile("" : "+v"(s[t])); WINWAIT(t < 6 ? xw : xe, t); LOAD(d[t], t); }
      SAMPLE();
      SCORES(i);
    }
    mx = xrow_max(row16_max(mx));
    if (lane == 0) miscf[M_WMAX + sw] = mx;
    lds_signal(misc + M_KDONE, lane);
#define PIN8(a) asm volatile("" : "+v"(a[0]), "+v"(a[1]), "+v"(a[2]), "+v"(a[3]), "+v"(a[4]), "+v"(a[5]), "+v"(a[6]), "+v"(a[7]))
    float acc[8];
#pragma unroll
    for (int j = 0; j < 8; ++j) acc[j] = 0.f;
    u32x4 scn = *(LAS u32x4*)(L + L_SC + sw * 64 + r * 16);
#pragma nounroll
    for (int i = 0; i < NS; ++i) {
      u32x4 sc = scn;
      asm volatile("" : "+v"(sc));
      PACE(NS + i);
      if (sw == 0) __hip_atomic_store(misc + M_STEP, (unsigned)i, RLX_WG);
      scn = *(LAS u32x4*)(L + L_SC + ((i + 1) & (NS - 1)) * 256 + sw * 64 + r * 16);
      const rsrc_t rn = mk_rsrc(vp, i + 1 < NS ? SK * 256 : 0);
      const int on = SOFF(i + 1), xv = i + 1 < NS ? xw : xw + 99;
#pragma unroll
      for (int t = 0; t < 8; t += 2) {
        const unsigned w = cvtpk(ex(bflo(sc[t >> 1]) - mx), ex(bfhi(sc[t >> 1]) - mx));
#pragma unroll
        for (int j = 0; j < 4; ++j) { acc[2 * j] = dot2(d[t][j], w & 0xffffu, acc[2 * j]); acc[2 * j + 1] = dot2(d[t][j], w << 16, acc[2 * j + 1]); }
        PIN8(acc); WINWAIT(xv, t); LOAD(d[t], t);
#pragma unroll
        for (int j = 0; j < 4; ++j) { acc[2 * j] = dot2(d[t + 1][j], w >> 16, acc[2 * j]); acc[2 * j + 1] = dot2(d[t + 1][j], w & 0xffff0000u, acc[2 * j + 1]); }
        PIN8(acc); WINWAIT(xv, t + 1); LOAD(d[t + 1], t + 1);
      }
      SAMPLE();
    }
    *(LAS f32x2*)(L + L_REC + sw * 512 + c * 32 + r * 8) = fold_rows(acc);
    asm volatile("" :: "s"(now), "v"(scn), "v"(anch) : "memory");
    LDS_BARRIER();
    return;
  }

  __builtin_amdgcn_s_setprio(2);
  const int hw = wave;
  const rsrc_t rws = mk_rsrc(ws, WS_BYTES);
  const u64 t0 = __builtin_amdgcn_s_memrealtime();
  LAS float* tmp = (LAS float*)(L + L_TMP);
  u64 epoch = 0;
  const int band = xg >> 1, cg = (xg & 1) * 32 + idx;
  {
    const rsrc_t rx = mk_rsrc(x, NB * DM * 2), rwa = mk_rsrc(wqa, DM * 4096 * 2), rwb = mk_rsrc(wqb, DM * 4096 * 2);
    const int row0 = band * 128 + hw * 32, vo = (lane >> 3) * 4 * 8192 + (lane & 7) * 16;
    u32x4 wA[4], wB[4]; u32x2 xv[NB];
#pragma unroll
    for (int i = 0; i < 4; ++i) wA[i] = bld<NT>(rwa, vo, (row0 + i) * 8192 + cg * 128);
#pragma unroll
    for (int bb = 0; bb < NB; ++bb) xv[bb] = bld2<0>(rx, (lane >> 3) * 8, (bb * DM + row0) * 2);
    if (hw == 0) epoch = __hip_atomic_load((GAS u64*)(ws + WS_EPOCH), RLX_AGENT);
    __builtin_amdgcn_sched_barrier(0);
#pragma unroll
    for (int i = 0; i < 4; ++i) wB[i] = bld<NT>(rwb, vo, (row0 + i) * 8192 + cg * 128);
    __builtin_amdgcn_sched_barrier(0);
    LDS_BARRIER();
    asm volatile("" : "+v"(wA[0]));
    __builtin_amdgcn_sched_barrier(0);
    q_partial(wA, xv, tmp + hw * 256, lane);
    asm volatile("" : "+v"(epoch));
    epoch += 1;
    if (tid == 0) { misc[M_TAGA] = (unsigned)epoch; misc[M_TAGB] = (unsigned)(epoch >> 32) ^ ((unsigned)epoch * 0x9E3779B1u); }
    q_partial(wB, xv, tmp + 1024 + hw * 256, lane);
  }
  hsync(misc, 1, lane);
  const unsigned tagA = misc[M_TAGA], tagB = misc[M_TAGB];
  if (hw == 0) {
#pragma unroll
    for (int br = 0; br < 2; ++br) {
      LAS float* part = tmp + br * 1024 + lane * 4;
      const f32x4 s = (*(LAS f32x4*)part + *(LAS f32x4*)(part + 256)) + (*(LAS f32x4*)(part + 512) + *(LAS f32x4*)(part + 768));
      bst<SC1NT>(__builtin_bit_cast(u32x4, s), rws, (lane >> 4) * 65536 + (lane & 15) * 16, WS_QP + br * 262144 + band * 16384 + cg * 256);
    }
    DRAIN();
    if (lane < 4) bst<SC1>(u32x4{0u, tagA, 0u, tagB}, rws, lane * 16, WS_QF + (cg * 4 + band) * 64);
    const u32x4 xv = bld<0>(mk_rsrc(x, NB * DM * 2), lane * 16, b * DM * 2), gv = bld<0>(mk_rsrc(gw, DM * 2), lane * 16, 0);
    float s = 0.f;
#pragma unroll
    for (int j = 0; j < 4; ++j) { const unsigned p = cvtpk(bflo(xv[j]) * bflo(gv[j]), bfhi(xv[j]) * bfhi(gv[j])); s += bflo(p); s += bfhi(p); }
    s = rbf(wave_sum(s));
    if (lane == 0) miscf[M_GATE] = rbf(1.f / (1.f + __expf(-s)));
  }
  {
    wait_time<4>(t0, T_Q);
    u32x4 f[1];
    poll<1, SC1>(f, rws, (lane & 7) * 64, 0, WS_QF + (xg * 4 + hw) * 512, tagA, tagB);
    u32x2 pb[4];
    if (hw == (h & 3)) {
      u32x2 pa[4];
      q_fetch(pa, rws, 0, b, h, lane);
      q_fetch(pb, rws, 1, b, xg * 4 + hw, lane);
      *(LAS unsigned*)(L + L_QA + lane * 4) = q_sum(pa);
      lds_signal(misc + M_QRDY, lane);
    } else q_fetch(pb, rws, 1, b, xg * 4 + hw, lane);
    *(LAS unsigned*)(L + L_QB + hw * 256 + lane * 4) = q_sum(pb);
    lds_wait_ge<2>(misc + M_BDMA, 4);
  }
  __builtin_amdgcn_s_setprio(0);
  hsync(misc, 2, lane);
  {
    u32x4 q[4];
#pragma unroll
    for (int g = 0; g < 4; ++g) q[g] = *(LAS u32x4*)(L + L_QB + g * 256 + c * 16);
    float m[4], l[4], acc[4][8];
#pragma unroll
    for (int g = 0; g < 4; ++g) { m[g] = -INFINITY; l[g] = 0.f;
#pragma unroll
      for (int j = 0; j < 8; ++j) acc[g][j] = 0.f; }
#pragma nounroll
    for (int s = 0; s < 4; ++s) {
      u32x4 k[4], v[4];
#pragma unroll
      for (int t = 0; t < 4; ++t) { k[t] = *(LAS u32x4*)(L + L_BIG + (hw * 16 + s * 4 + t) * 1024 + lane * 16); v[t] = *(LAS u32x4*)(L + L_BIG + 65536 + (hw * 16 + s * 4 + t) * 1024 + lane * 16); }
      gqa_step(k, v, q, m, l, acc);
    }
#pragma unroll
    for (int g = 0; g < 4; ++g) {
      const float M = xrow_max(m[g]), w = ex(m[g] - M), Ls = xrow_sum(l[g] * w);
#pragma unroll
      for (int j = 0; j < 8; ++j) acc[g][j] *= w;
      LAS float* rec = tmp + (g * 4 + hw) * REC_F;
      *(LAS f32x2*)(rec + 4 + c * 8 + r * 2) = fold_rows(acc[g]);
      if (lane == 0) *(LAS f32x2*)rec = f32x2{M, Ls};
    }
  }
  hsync(misc, 3, lane);
  {
    LAS float* rec = tmp + hw * 4 * REC_F;
    f32x2 ml[4], av[4];
#pragma unroll
    for (int i = 0; i < 4; ++i) { ml[i] = *(LAS f32x2*)(rec + i * REC_F); av[i] = *(LAS f32x2*)(rec + i * REC_F + 4 + lane * 2); }
    const float M = fmaxf(fmaxf(ml[0][0], ml[1][0]), fmaxf(ml[2][0], ml[3][0]));
    float Ls = 0.f, o0 = 0.f, o1 = 0.f;
#pragma unroll
    for (int i = 0; i < 4; ++i) { const float wt = ex(ml[i][0] - M); Ls = fmaf(wt, ml[i][1], Ls); o0 = fmaf(wt, av[i][0], o0); o1 = fmaf(wt, av[i][1], o1); }
    *(LAS unsigned*)(L + L_PUB + hw * 256 + lane * 4) = cvtpk(o0 / Ls, o1 / Ls);
    if (lane == 0) *(LAS f32x2*)(L + L_PUB + 1024 + hw * 8) = f32x2{M, Ls};
  }
  hsync(misc, 4, lane);
  if (hw == 0) {
    const int pi = (b * 8 + xg) * 8 + sp;
    wait_time<4>(t0, T_PB);
    next_slot();
    bst<SC1NT>(*(LAS u32x4*)(L + L_PUB + lane * 16), rws, lane * 16, WS_PBP + pi * 1024);
    const u32x2 ml = *(LAS u32x2*)(L + L_PUB + 1024 + (lane & 3) * 8);
    DRAIN();
    next_slot();
    if (lane < 4) bst<SC1NT>(u32x4{ml[0], tagA, ml[1], tagB}, rws, lane * 16, WS_PBM + pi * 64);
  }
  if (hw == 3) cp_publish(0, misc, rws, bid, tagA, lane);
  wait_time<4>(t0, T_W);
  if (hw == 3) cp_consume(0, misc, rws, tagA, lane);
  {
    const GAS unsigned char* src = (const GAS unsigned char*)(woa + (size_t)(h * 128 + hw * 2) * DM) + lane * 16;
#pragma nounroll
    for (int i0 = 0; i0 < 32; i0 += 4) {
#pragma unroll
      for (int i = i0; i < i0 + 4; ++i) { const int o = (i >> 1) * 8192 + (i & 1) * 1024; CAPWAIT(2); glds16<SC1>(src + o, lds0 + L_BIG + hw * 2048 + o); }
      if (hw == 3) { if (i0 == 12) cp_publish(1, misc, rws, bid, tagA, lane); else if (i0 == 20) cp_consume(1, misc, rws, tagA, lane); }
      if (hw == 0 && i0 == 12) {
        u32x4 ml[1];
        poll<1, SC1NT>(ml, rws, (lane >> 3) * 64, 0, WS_PBM + (b * 8 + xg) * 512 + (h & 3) * 16, tagA, tagB);
        const u32x4 av = bld<SC1NT>(rws, (lane >> 3) * 1024 + (lane & 7) * 16, WS_PBP + (b * 8 + xg) * 8192 + (h & 3) * 256 + side * 128);
        const float pm = __uint_as_float(ml[0][0]), M = xrow_max(fmaxf(pm, dpp<XOR8>(pm)));
        const float wt = ex(pm - M) * __uint_as_float(ml[0][2]), Ls = xrow_sum(wt + dpp<XOR8>(wt));
        float o[8];
#pragma unroll
        for (int j = 0; j < 4; ++j) { o[2 * j] = wt * bflo(av[j]); o[2 * j + 1] = wt * bfhi(av[j]); }
        const f32x2 f = fold_rows(o);
        *(LAS unsigned*)(L + L_PUB + (lane & 7) * 16 + r * 4) = cvtpk((f[0] + dpp<XOR8>(f[0])) / Ls, (f[1] + dpp<XOR8>(f[1])) / Ls);
      }
    }
    DRAIN();
    hsync(misc, 5, lane);
    if (hw == 3) cp_publish(2, misc, rws, bid, tagA, lane);
  }
  unsigned w0[64], w1[64];
#pragma unroll
  for (int p = 0; p < 64; ++p) {
    const unsigned a = *(LAS unsigned*)(L + L_BIG + p * 2048 + tid * 4), bb = *(LAS unsigned*)(L + L_BIG + p * 2048 + 1024 + tid * 4);
    w0[p] = plo(bb, a); w1[p] = phi(bb, a);
    asm volatile("" : "+v"(w0[p]), "+v"(w1[p]));
  }
  hsync(misc, 6, lane);
  unsigned zpk;
  {
    const GAS unsigned char* src = (const GAS unsigned char*)(wob + (size_t)(h * 128 + side * 64 + hw * 2) * DM) + lane * 16;
    if (hw == 3) cp_consume(2, misc, rws, tagA, lane);
#pragma unroll 4
    for (int i = 0; i < 16; ++i) { const int o = (i >> 1) * 8192 + (i & 1) * 1024; CAPWAIT(2); glds16<SC1>(src + o, lds0 + L_BIG + hw * 2048 + o); }
    DRAIN();
    hsync(misc, 7, lane);
    float z0 = 0.f, z1 = 0.f;
#pragma unroll 4
    for (int p = 0; p < 32; ++p) {
      const unsigned a = *(LAS unsigned*)(L + L_BIG + p * 2048 + tid * 4), bb = *(LAS unsigned*)(L + L_BIG + p * 2048 + 1024 + tid * 4), op = *(LAS unsigned*)(L + L_PUB + p * 4);
      z0 = dot2(plo(bb, a), op, z0); z1 = dot2(phi(bb, a), op, z1);
    }
    zpk = cvtpk(z0, z1);
    asm volatile("" : "+v"(zpk));
  }
  {
    lds_wait_ge<32>(misc + M_KDONE, 4);
    const f32x4 wm = *(LAS f32x4*)(miscf + M_WMAX);
    const float M = fmaxf(fmaxf(wm[0], wm[1]), fmaxf(wm[2], wm[3]));
    float ls = 0.f;
#pragma unroll
    for (int i = 0; i < 2; ++i) {
      const u32x4 sc = *(LAS u32x4*)(L + L_SC + hw * 2048 + i * 1024 + lane * 16);
#pragma unroll
      for (int j = 0; j < 4; ++j) ls += ex(bflo(sc[j]) - M) + ex(bfhi(sc[j]) - M);
    }
    ls = wave_sum(ls);
    if (lane == 0) miscf[M_LSUM + hw] = ls;
    hsync(misc, 8, lane);
    if (hw == 0) {
      const f32x4 lw = *(LAS f32x4*)(miscf + M_LSUM);
      const float Ls = (lw[0] + lw[1]) + (lw[2] + lw[3]);
      if (lane < 4) bst<SC1NT>(u32x4{__float_as_uint(M), tagA, __float_as_uint(Ls), tagB}, rws, lane * 16, WS_ST + (b * 64 + jp) * 64);
      wait_time<32>(__builtin_amdgcn_s_memrealtime(), T_ST);
      u32x4 o[1];
      poll<1, SC1NT>(o, rws, 0, 0, WS_ST + (b * 64 + (jp ^ 1)) * 64, tagA, tagB);
      const float Mo = __uint_as_float(o[0][0]), MM = fmaxf(M, Mo);
      if (lane < 4) miscf[M_CNORM + lane] = ex(miscf[M_WMAX + lane] - MM) / (ex(M - MM) * Ls + ex(Mo - MM) * __uint_as_float(o[0][2]));
      if (bid == 0) {
        u32x4 all[4];
        wait_time<32>(__builtin_amdgcn_s_memrealtime(), T_ST);
        poll<4, SC1>(all, rws, lane * 64, 4096, WS_ST, tagA, tagB);
        if (lane == 0) __hip_atomic_store((GAS u64*)(ws + WS_EPOCH), epoch, RLX_AGENT);
      }
    } else if (hw == 3) {
      int k = 3, state = 0, rho = 0, rprev = RHO_MAX, have = 0, TP = 0;
      unsigned P = __builtin_amdgcn_readfirstlane(__hip_atomic_load(misc + M_ANCH, RLX_WG));
#pragma nounroll
      for (unsigned spins = 0; spins < SPIN_CAP && (unsigned)__builtin_amdgcn_readfirstlane(__hip_atomic_load(misc + M_STEP, RLX_WG)) < NS - 3; ++spins) {
        const int t = (int)(__builtin_amdgcn_s_memrealtime() - t0), tk = T_CP0 + (k - 3) * T_CP;
        if (state == 0 && t >= tk && k < NCP) { if (t < tk + T_CPLAG / 2) cp_publish(k, misc, rws, bid, tagA, lane); state = 1; }
        else if (state == 1 && t >= tk + T_CPLAG) {
          const unsigned Pe = P + (((tk - TP) * rho) >> 10), Pn = cp_mean(k, rws, Pe, tagA, lane);
          if (have) { const int rn = min(max((int)(Pn - P) * 1024 / T_CP, RHO_MIN), RHO_MAX); rho = min(rn, rprev); rprev = rn; }
          have = have || Pn != Pe; P = Pn; TP = tk; state = 0; ++k;
        }
        if (lane == 0) __hip_atomic_store(misc + M_ANCH, P + (((t - TP) * rho) >> 10), RLX_WG);
        __builtin_amdgcn_s_sleep(16);
      }
    }
  }
  int yoff = (tid >> 2) * 4096 + (tid & 3) * 16, goff = lane * 64 + hw * 16;
  const float gate = miscf[M_GATE];
  float gsel = lane < 32 ? gate : rbf(1.f - gate);
  asm volatile("" : "+v"(yoff), "+v"(goff), "+v"(gsel));
  if (hw == 1 && idx < 3) { l2_pin(code_here() + idx * 1024, lds0 + L_DUMP, lane); DRAIN(); }
  if (hw == 2) {
    const unsigned t = __builtin_amdgcn_raw_buffer_load_b32(mk_rsrc(out, NB * DM * 2), 0, (b * DM + jp * 8) * 2, 0);
    asm volatile("" :: "v"(t));
  }
  LDS_BARRIER();
  const unsigned yanch = misc[M_ANCH];
  {
    const f32x4 cn = *(LAS f32x4*)(miscf + M_CNORM);
    f32x4 rl[4], rh[4];
#pragma unroll
    for (int w = 0; w < 4; ++w) { rl[w] = *(LAS f32x4*)(L + L_REC + w * 512 + c * 32); rh[w] = *(LAS f32x4*)(L + L_REC + w * 512 + c * 32 + 16); }
    __builtin_amdgcn_sched_barrier(0);
    f32x4 lo = cn[0] * rl[0], hi = cn[0] * rh[0];
#pragma unroll
    for (int w = 1; w < 4; ++w) { lo += cn[w] * rl[w]; hi += cn[w] * rh[w]; }
    unsigned ob[4] = {cvtpk(lo[0], lo[1]), cvtpk(lo[2], lo[3]), cvtpk(hi[0], hi[1]), cvtpk(hi[2], hi[3])};
    float y0 = 0.f, y1 = 0.f, y2 = 0.f, y3 = 0.f;
    asm volatile("s_nop 1" : "+v"(ob[0]), "+v"(ob[1]), "+v"(ob[2]), "+v"(ob[3]), "+v"(y0), "+v"(y1), "+v"(y2), "+v"(y3));
    Proj<0>::run(y0, y1, y2, y3, w0, w1, ob);
    asm volatile("s_nop 2" : "+v"(y0), "+v"(y1), "+v"(y2), "+v"(y3));
    const unsigned ypk = cvtpk(y0 + y2, y1 + y3);
    bst<SC1>(u32x4{plo(zpk, ypk), tagA, phi(zpk, ypk), tagB}, rws, yoff, WS_YG + b * 64 * 4096 + jp * 64);
  }
  {
    {
      const u64 tp = __builtin_amdgcn_s_memrealtime();
      const int dl = min(max((int)(__builtin_amdgcn_readfirstlane(yanch) + T_Y - (unsigned)tp), Y_MIN), Y_MAX);
#pragma nounroll
      while ((int)(__builtin_amdgcn_s_memrealtime() - tp) < dl) {}
    }
    u32x4 g[1];
    poll<1, SC1>(g, rws, goff, 0, WS_YG + (b * 64 + jp) * 4096, tagA, tagB);
    const float u = row16_sum(fold16(fold32(bflo(g[0][0]), bfhi(g[0][0])), fold32(bflo(g[0][2]), bfhi(g[0][2]))));
    const float t = rbf(rbf(u) * gsel);
    const unsigned res = cvtpk(fold32(t, t), 0.f);
    if (lane == 0 || lane == 16) __builtin_amdgcn_raw_buffer_store_b16((u16)res, mk_rsrc(out, NB * DM * 2), r * 2, (b * DM + jp * 8 + hw * 2) * 2, 0);
  }
}

extern "C" void kernel_launch(void* const* d_in, const int* in_sizes, int n_in, void* d_out, int out_size, void* d_ws, size_t ws_size, hipStream_t stream) {
  k_fused<<<256, 512, 0, stream>>>((const u16*)d_in[0], (const u16*)d_in[1], (const u16*)d_in[2], (const u16*)d_in[3], (const u16*)d_in[4], (const u16*)d_in[6], (const u16*)d_in[7],
                                   (const u16*)d_in[8], (const u16*)d_in[9], (const u16*)d_in[10], (u16*)d_out, (unsigned char*)d_ws);
}
```

```cpp
#include <hip/hip_runtime.h>
#include <hip/hip_bf16.h>
#include <cstdint>

typedef unsigned u32x4 __attribute__((ext_vector_type(4)));
typedef unsigned u32x2 __attribute__((ext_vector_type(2)));
typedef float f32x4 __attribute__((ext_vector_type(4)));
typedef float f32x2 __attribute__((ext_vector_type(2)));
typedef __attribute__((ext_vector_type(2))) __bf16 bf16x2;
typedef unsigned long long u64;
typedef unsigned short u16;
typedef __amdgpu_buffer_rsrc_t rsrc_t;
#define GAS __attribute__((address_space(1)))
#define LAS __attribute__((address_space(3)))
#define RLX_AGENT __ATOMIC_RELAXED, __HIP_MEMORY_SCOPE_AGENT
#define RLX_WG __ATOMIC_RELAXED, __HIP_MEMORY_SCOPE_WORKGROUP
#define DRAIN() asm volatile("s_waitcnt vmcnt(0)" ::: "memory")
#define LDS_BARRIER() do { asm volatile("s_waitcnt lgkmcnt(0)" ::: "memory"); __builtin_amdgcn_s_barrier(); asm volatile("" ::: "memory"); } while (0)
#define CAPWAIT(n) do { __builtin_amdgcn_sched_barrier(0); asm volatile("s_waitcnt vmcnt(%0)" :: "i"((n) - 1) : "memory"); } while (0)

constexpr int NB = 4, DM = 512, NH = 32, SK = 8192, DH = 128, WIN = 2048;
constexpr float SCALE = 0.088388347648318440f, L2E = 1.4426950408889634f;
constexpr int NT = 2, SC1 = 16, SC1NT = 18, RSRC_W3 = 0x00020000;
constexpr int NS = 32;
constexpr unsigned SPIN_CAP = 1u << 18;

constexpr int CAP0 = 5;
constexpr int TAU = 108;
constexpr int TK0 = 800;
constexpr int XCENTER = 11, XMIN = 6, XGAIN = 10;
constexpr int END_STEPS = 8, XCENTER_END = 9, XMIN_END = 2, XGAIN_END = 45;
constexpr int T_Q = 540, T_PB = 1790, T_W = 2000, T_ST = 500, SLOT = 64;
constexpr int T_CP0 = 4700, T_CP = 500, T_CPLAG = 200, RHO_MIN = -20, RHO_MAX = 60;
constexpr int T_Y = 63 * TAU + 190, Y_MIN = 40, Y_MAX = 72;

constexpr int WS_EPOCH = 0;
constexpr int WS_PH = 4096, NCP = 14;
constexpr int WS_QF = 32768;
constexpr int WS_QP = WS_QF + 16384;
constexpr int WS_PBM = WS_QP + 524288;
constexpr int WS_ST = WS_PBM + 16384;
constexpr int WS_PBP = WS_ST + 16384;
constexpr int WS_YG = WS_PBP + 262144;
constexpr int WS_BYTES = WS_YG + 256 * 4096;

constexpr int L_MISC = 0, L_QA = 256, L_QB = 512, L_REC = 1536, L_SC = 4096, L_TMP = 12288, L_PUB = L_TMP + 8704, L_BIG = 22528, L_DUMP = L_BIG + 131072, L_BYTES = L_DUMP + 1024;
constexpr int REC_F = 132;
enum { M_TAGA = 0, M_TAGB = 1, M_ANCH = 2, M_STEP = 3, M_QRDY = 4, M_HSYNC = 5, M_KDONE = 6, M_BDMA = 7, M_WMAX = 8, M_LSUM = 12, M_CNORM = 16, M_PH = 20, M_GATE = 24 };

__device__ __forceinline__ float dot2(unsigned a, unsigned b, float c) {
  return __builtin_amdgcn_fdot2_f32_bf16(__builtin_bit_cast(bf16x2, a), __builtin_bit_cast(bf16x2, b), c, false);
}
__device__ __forceinline__ unsigned cvtpk(float lo, float hi) { return __builtin_bit_cast(unsigned, __builtin_convertvector(f32x2{lo, hi}, bf16x2)); }
__device__ __forceinline__ float bflo(unsigned u) { return __uint_as_float(u << 16); }
__device__ __forceinline__ float bfhi(unsigned u) { return __uint_as_float(u & 0xffff0000u); }
__device__ __forceinline__ float rbf(float x) { return bflo(cvtpk(x, 0.f)); }
__device__ __forceinline__ float ex(float x) { return __builtin_amdgcn_exp2f(x * L2E); }
__device__ __forceinline__ unsigned plo(unsigned hi, unsigned lo) { return __builtin_amdgcn_perm(hi, lo, 0x05040100u); }
__device__ __forceinline__ unsigned phi(unsigned hi, unsigned lo) { return __builtin_amdgcn_perm(hi, lo, 0x07060302u); }
template <int CTRL> __device__ __forceinline__ float dpp(float x) {
  return __builtin_bit_cast(float, __builtin_amdgcn_mov_dpp(__builtin_bit_cast(int, x), CTRL, 0xf, 0xf, true));
}
constexpr int XOR1 = 0xB1, XOR2 = 0x4E, XOR7 = 0x141, XOR8 = 0x128;
__device__ __forceinline__ float row16_sum(float x) { x += dpp<XOR1>(x); x += dpp<XOR2>(x); x += dpp<XOR7>(x); x += dpp<XOR8>(x); return x; }
__device__ __forceinline__ float row16_max(float x) { x = fmaxf(x, dpp<XOR1>(x)); x = fmaxf(x, dpp<XOR2>(x)); x = fmaxf(x, dpp<XOR7>(x)); return fmaxf(x, dpp<XOR8>(x)); }
template <int CTRL> __device__ __forceinline__ float foldd(float a, float b, bool hi) { return (hi ? b : a) + dpp<CTRL>(hi ? a : b); }
__device__ __forceinline__ float fold32(float x, float y) {
  auto t = __builtin_amdgcn_permlane32_swap(__float_as_uint(x), __float_as_uint(y), false, false);
  return __uint_as_float(t[0]) + __uint_as_float(t[1]);
}
__device__ __forceinline__ float fold16(float x, float y) {
  auto t = __builtin_amdgcn_permlane16_swap(__float_as_uint(x), __float_as_uint(y), false, false);
  return __uint_as_float(t[0]) + __uint_as_float(t[1]);
}
__device__ __forceinline__ float xrow_sum(float x) { x = fold16(x, x); return fold32(x, x); }
__device__ __forceinline__ float xrow_max(float x) {
  auto s = __builtin_amdgcn_permlane16_swap(__float_as_uint(x), __float_as_uint(x), false, false);
  x = fmaxf(__uint_as_float(s[0]), __uint_as_float(s[1]));
  auto t = __builtin_amdgcn_permlane32_swap(__float_as_uint(x), __float_as_uint(x), false, false);
  return fmaxf(__uint_as_float(t[0]), __uint_as_float(t[1]));
}
__device__ __forceinline__ float wave_sum(float x) { return xrow_sum(row16_sum(x)); }
__device__ __forceinline__ f32x2 fold_rows(const float (&a)[8]) {
  const float u0 = fold32(a[0], a[4]), u1 = fold32(a[1], a[5]), u2 = fold32(a[2], a[6]), u3 = fold32(a[3], a[7]);
  return f32x2{fold16(u0, u2), fold16(u1, u3)};
}

__device__ __forceinline__ rsrc_t mk_rsrc(const void* p, int bytes) { return __builtin_amdgcn_make_buffer_rsrc(const_cast<void*>(p), 0, bytes, RSRC_W3); }
template <int AUX> __device__ __forceinline__ u32x4 bld(rsrc_t r, int voff, int soff) { return __builtin_amdgcn_raw_buffer_load_b128(r, voff, soff, AUX); }
template <int AUX> __device__ __forceinline__ u32x2 bld2(rsrc_t r, int voff, int soff) { return __builtin_amdgcn_raw_buffer_load_b64(r, voff, soff, AUX); }
template <int AUX> __device__ __forceinline__ void bst(u32x4 v, rsrc_t r, int voff, int soff) { __builtin_amdgcn_raw_buffer_store_b128(v, r, voff, soff, AUX); }
template <int AUX> __device__ __forceinline__ void glds16(const GAS unsigned char* src, unsigned dst) {
  unsigned keep;
  if (AUX == NT) asm volatile("s_mov_b32 %0, m0\n\ts_mov_b32 m0, %2\n\ts_nop 0\n\tglobal_load_lds_dwordx4 %1, off nt\n\ts_mov_b32 m0, %0" : "=&s"(keep) : "v"(src), "s"(dst) : "memory");
  else asm volatile("s_mov_b32 %0, m0\n\ts_mov_b32 m0, %2\n\ts_nop 0\n\tglobal_load_lds_dwordx4 %1, off sc1\n\ts_mov_b32 m0, %0" : "=&s"(keep) : "v"(src), "s"(dst) : "memory");
}
__device__ __forceinline__ void l2_pin(const GAS unsigned char* p, unsigned dump, int lane) {
  unsigned keep;
  asm volatile("s_mov_b32 %0, m0\n\ts_mov_b32 m0, %3\n\ts_nop 0\n\tglobal_load_lds_dwordx4 %1, off nt\n\tglobal_load_lds_dword %2, off sc1\n\ts_mov_b32 m0, %0"
               : "=&s"(keep) : "v"(p + lane * 16), "v"(p + (lane & 7) * 128), "s"(dump) : "memory");
}
__device__ __forceinline__ const GAS unsigned char* code_here() { u64 pc; asm volatile("s_getpc_b64 %0" : "=s"(pc)); return (const GAS unsigned char*)(pc & ~127ull); }

#define DITH(t) ((((t) & 1) << 2) | ((t) & 2) | (((t) & 4) >> 2))
#define WINWAIT(x, t) do { __builtin_amdgcn_sched_barrier(0); asm volatile("s_cmp_gt_i32 %0, %2\n\ts_cbranch_scc1 2f\n\ts_cmp_gt_i32 %0, %1\n\ts_cbranch_scc1 1f\n\ts_waitcnt vmcnt(3)\n1:\n\ts_waitcnt vmcnt(4)\n" \
    "2:\n\ts_cmp_gt_i32 %0, %3\n\ts_cbranch_scc1 3f\n\ts_waitcnt vmcnt(5)\n3:\n\ts_waitcnt vmcnt(6)" :: "s"(x), "i"(7 - DITH(t)), "i"(15 - DITH(t)), "i"(23 - DITH(t)) : "memory", "scc"); } while (0)

template <int SLEEP> __device__ __forceinline__ void wait_time(u64 t0, unsigned ticks) {
#pragma nounroll
  while (__builtin_amdgcn_s_memrealtime() - t0 < ticks) __builtin_amdgcn_s_sleep(SLEEP);
}
__device__ __forceinline__ void next_slot() {
  const unsigned n0 = (unsigned)__builtin_amdgcn_s_memrealtime(), left = SLOT - n0 % SLOT;
#pragma nounroll
  while ((unsigned)__builtin_amdgcn_s_memrealtime() - n0 < left) __builtin_amdgcn_s_sleep(1);
  asm volatile("" ::: "memory");
}
template <int SLEEP> __device__ __forceinline__ void lds_wait_ge(LAS unsigned* p, unsigned target) {
#pragma nounroll
  for (unsigned spins = 0; spins < 32 * SPIN_CAP && (unsigned)__builtin_amdgcn_readfirstlane(__hip_atomic_load(p, RLX_WG)) < target; ++spins) __builtin_amdgcn_s_sleep(SLEEP);
  asm volatile("" ::: "memory");
}
__device__ __forceinline__ void lds_signal(LAS unsigned* p, int lane) {
  asm volatile("s_waitcnt lgkmcnt(0)" ::: "memory");
  if (lane == 0) __builtin_amdgcn_atomic_inc32(p, 0xffffffffu, __ATOMIC_RELAXED, "workgroup");
}
__device__ __forceinline__ void hsync(LAS unsigned* misc, unsigned n, int lane) { lds_signal(misc + M_HSYNC, lane); lds_wait_ge<2>(misc + M_HSYNC, 4 * n); }
template <int N, int AUX> __device__ __forceinline__ void poll(u32x4 (&g)[N], rsrc_t rws, int voff, int stride, int soff, unsigned tagA, unsigned tagB) {
#pragma nounroll
  for (unsigned spins = 0; spins < SPIN_CAP; ++spins) {
    asm volatile("" ::: "memory");
    bool ok = true;
#pragma unroll
    for (int i = 0; i < N; ++i) g[i] = bld<AUX>(rws, voff + i * stride, soff);
#pragma unroll
    for (int i = 0; i < N; ++i) ok &= (g[i][1] == tagA) & (g[i][3] == tagB);
    if (__all(ok)) break;
    __builtin_amdgcn_s_sleep(2);
  }
  __builtin_amdgcn_fence(__ATOMIC_ACQUIRE, "wavefront");
  asm volatile("" ::: "memory");
}
__device__ __forceinline__ void cp_publish(int k, LAS unsigned* misc, rsrc_t rws, int bid, unsigned tagA, int lane) {
  if ((((bid >> 3) + k) & 15) == 0) {
    asm volatile("" ::: "memory");
    const u32x4 wp = *(LAS u32x4*)(misc + M_PH);
    const unsigned ph = wp[0] + (((int)(wp[1] - wp[0]) + (int)(wp[2] - wp[0]) + (int)(wp[3] - wp[0])) >> 2);
    if (lane < 8) __builtin_amdgcn_raw_buffer_store_b64(u32x2{ph, tagA}, rws, lane * 8, WS_PH + k * 2048 + ((bid & 7) + ((bid >> 7) << 3)) * 64, SC1);
  }
}
__device__ __forceinline__ unsigned cp_mean(int k, rsrc_t rws, unsigned ref, unsigned tagA, int lane) {
  asm volatile("" ::: "memory");
  const u32x2 g = bld2<SC1>(rws, (lane & 15) * 64, WS_PH + k * 2048);
  const bool ok = g[1] == tagA;
  const float d = wave_sum(ok ? (float)(int)(g[0] - ref) : 0.f), n = wave_sum(ok ? 1.f : 0.f);
  return __builtin_amdgcn_readfirstlane(n >= 32.f ? ref + (int)(d / n) : ref);
}
__device__ __forceinline__ void cp_consume(int k, LAS unsigned* misc, rsrc_t rws, unsigned tagA, int lane) {
  const unsigned a = cp_mean(k, rws, __hip_atomic_load(misc + M_ANCH, RLX_WG), tagA, lane);
  if (lane == 0) __hip_atomic_store(misc + M_ANCH, a, RLX_WG);
}

__device__ __forceinline__ void q_partial(const u32x4 (&w)[4], const u32x2 (&xv)[NB], LAS float* part, int lane) {
  float a[NB][8];
#pragma unroll
  for (int j = 0; j < 4; ++j) {
    const unsigned p0 = plo(w[1][j], w[0][j]), p1 = phi(w[1][j], w[0][j]), p2 = plo(w[3][j], w[2][j]), p3 = phi(w[3][j], w[2][j]);
#pragma unroll
    for (int bb = 0; bb < NB; ++bb) { a[bb][2 * j] = dot2(p2, xv[bb][1], dot2(p0, xv[bb][0], 0.f)); a[bb][2 * j + 1] = dot2(p3, xv[bb][1], dot2(p1, xv[bb][0], 0.f)); }
  }
  float f[8];
#pragma unroll
  for (int j = 0; j < 8; ++j) f[j] = fold16(fold32(a[0][j], a[2][j]), fold32(a[1][j], a[3][j]));
#pragma unroll
  for (int j = 0; j < 4; ++j) f[j] = foldd<XOR8>(f[j], f[j + 4], lane & 8);
  *(LAS f32x4*)(part + (lane >> 4) * 64 + (lane & 7) * 8 + ((lane >> 3) & 1) * 4) = f32x4{f[0], f[1], f[2], f[3]};
}
__device__ __forceinline__ void q_fetch(u32x2 (&p)[4], rsrc_t rws, int br, int b, int hq, int lane) {
#pragma unroll
  for (int i = 0; i < 4; ++i) p[i] = bld2<SC1NT>(rws, lane * 8 + i * 16384, WS_QP + br * 262144 + b * 65536 + hq * 512);
}
__device__ __forceinline__ unsigned q_sum(const u32x2 (&p)[4]) {
  const f32x2 s = (__builtin_bit_cast(f32x2, p[0]) + __builtin_bit_cast(f32x2, p[1])) + (__builtin_bit_cast(f32x2, p[2]) + __builtin_bit_cast(f32x2, p[3]));
  return cvtpk(s[0], s[1]);
}
__device__ __forceinline__ void gqa_step(const u32x4 (&k)[4], const u32x4 (&v)[4], const u32x4 (&q)[4], float (&m)[4], float (&l)[4], float (&acc)[4][8]) {
#pragma unroll
  for (int g = 0; g < 4; ++g) {
    float s[4];
#pragma unroll
    for (int t = 0; t < 4; ++t) s[t] = dot2(k[t][3], q[g][3], dot2(k[t][2], q[g][2], dot2(k[t][1], q[g][1], dot2(k[t][0], q[g][0], 0.f))));
#pragma unroll
    for (int t = 0; t < 4; t += 2) {
      const unsigned r1 = cvtpk(row16_sum(s[t]), row16_sum(s[t + 1]));
      const unsigned r2 = cvtpk(bflo(r1) * SCALE, bfhi(r1) * SCALE);
      s[t] = bflo(r2); s[t + 1] = bfhi(r2);
    }
    const float mn = fmaxf(fmaxf(m[g], fmaxf(s[0], s[1])), fmaxf(s[2], s[3]));
    const float al = ex(m[g] - mn);
    m[g] = mn;
    float ls = l[g] * al;
#pragma unroll
    for (int j = 0; j < 8; ++j) acc[g][j] *= al;
#pragma unroll
    for (int t = 0; t < 4; t += 2) {
      const float p0 = ex(s[t] - mn), p1 = ex(s[t + 1] - mn);
      ls += p0 + p1;
      const unsigned w = cvtpk(p0, p1);
#pragma unroll
      for (int j = 0; j < 4; ++j) {
        acc[g][2 * j] = dot2(v[t + 1][j], w >> 16, dot2(v[t][j], w & 0xffffu, acc[g][2 * j]));
        acc[g][2 * j + 1] = dot2(v[t + 1][j], w & 0xffff0000u, dot2(v[t][j], w << 16, acc[g][2 * j + 1]));
      }
    }
    l[g] = ls;
  }
}
template <int C> struct Proj {
  static __device__ __forceinline__ void run(float& y0, float& y1, float& y2, float& y3, const unsigned (&w0)[64], const unsigned (&w1)[64], const unsigned (&ob)[4]) {
#define DOTB(y, o, w) "v_dot2c_f32_bf16_dpp " y ", " o ", " w " row_newbcast:%16 row_mask:0xf bank_mask:0xf\n\t"
    asm volatile(DOTB("%0", "%4", "%8") DOTB("%1", "%4", "%12") DOTB("%2", "%5", "%9") DOTB("%3", "%5", "%13") DOTB("%0", "%6", "%10") DOTB("%1", "%6", "%14") DOTB("%2", "%7", "%11") DOTB("%3", "%7", "%15")
                 : "+v"(y0), "+v"(y1), "+v"(y2), "+v"(y3)
                 : "v"(ob[0]), "v"(ob[1]), "v"(ob[2]), "v"(ob[3]), "v"(w0[4 * C]), "v"(w0[4 * C + 1]), "v"(w0[4 * C + 2]), "v"(w0[4 * C + 3]),
                   "v"(w1[4 * C]), "v"(w1[4 * C + 1]), "v"(w1[4 * C + 2]), "v"(w1[4 * C + 3]), "i"(C));
#undef DOTB
    if constexpr (C + 1 < 16) Proj<C + 1>::run(y0, y1, y2, y3, w0, w1, ob);
  }
};

__global__ void __launch_bounds__(512, 2) k_fused(const u16* __restrict__ x, const u16* __restrict__ ka, const u16* __restrict__ va, const u16* __restrict__ wqa, const u16* __restrict__ woa,
                                                  const u16* __restrict__ kb, const u16* __restrict__ vb, const u16* __restrict__ wqb, const u16* __restrict__ wob,
                                                  const u16* __restrict__ gw, u16* __restrict__ out, unsigned char* __restrict__ ws) {
  __shared__ __attribute__((aligned(1024))) unsigned char smem[L_BYTES];
  LAS unsigned char* L = (LAS unsigned char*)smem;
  LAS unsigned* misc = (LAS unsigned*)(L + L_MISC);
  LAS float* miscf = (LAS float*)(L + L_MISC);
  const unsigned lds0 = (unsigned)(size_t)L;
  const int tid = threadIdx.x, lane = tid & 63, wave = __builtin_amdgcn_readfirstlane(tid >> 6), r = lane >> 4, c = lane & 15;
  const int bid = blockIdx.x, xg = bid & 7, idx = bid >> 3, b = idx & 3, sp = idx >> 2;
  const int side = sp & 1, h = xg * 4 + (idx >> 3), jp = h * 2 + side;
  asm volatile("" :: "s"(x), "s"(ka), "s"(va), "s"(wqa), "s"(woa), "s"(kb), "s"(vb), "s"(wqb), "s"(wob), "s"(gw), "s"(out), "s"(ws));

  if (wave >= 4) {
    __builtin_amdgcn_s_setprio(1);
    const int sw = wave - 4, voff = lane * 16;
    if (tid == 256) {
      const unsigned a0 = (unsigned)__builtin_amdgcn_s_memrealtime() + TK0;
      *(LAS u32x2*)(misc + M_ANCH) = u32x2{a0, 0u}; *(LAS u32x4*)(misc + M_QRDY) = u32x4{0u, 0u, 0u, 0u}; *(LAS u32x4*)(misc + M_PH) = u32x4{a0, a0, a0, a0};
    }
    if (sw == 3 && idx < 5) l2_pin(code_here() + idx * 1024, lds0 + L_DUMP, lane);
    LDS_BARRIER();
    {
      const size_t o = ((size_t)(b * 8 + xg) * SK + (SK - WIN) + sp * 64 + sw * 16) * DH;
      const GAS unsigned char* pk = (const GAS unsigned char*)(kb + o) + voff;
      const GAS unsigned char* pv = (const GAS unsigned char*)(vb + o) + voff;
#pragma unroll 4
      for (int i = 0; i < 16; ++i) { CAPWAIT(CAP0); glds16<NT>(pk + (((i >> 2) + xg) & 3) * 131072 + (i & 3) * 1024, lds0 + L_BIG + sw * 4096 + (i >> 2) * 16384 + (i & 3) * 1024); }
#pragma unroll 4
      for (int i = 0; i < 16; ++i) { CAPWAIT(CAP0); glds16<NT>(pv + (((i >> 2) + xg) & 3) * 131072 + (i & 3) * 1024, lds0 + L_BIG + 65536 + sw * 4096 + (i >> 2) * 16384 + (i & 3) * 1024); }
    }
    {
      const size_t o = (size_t)(h * 128 + (side * 4 + b) * 16 + sw * 4) * DM;
#pragma unroll
      for (int i = 0; i < 8; ++i) { CAPWAIT(CAP0); glds16<SC1>((const GAS unsigned char*)((i < 4 ? wob : woa) + o) + (i & 3) * 1024 + voff, lds0 + L_DUMP); }
    }
    lds_signal(misc + M_BDMA, lane);
    const u16* kp = ka + (size_t)(b * NH + h) * SK * DH;
    const u16* vp = va + (size_t)(b * NH + h) * SK * DH;
#define SOFF(i) ((((((i) + h) & (NS - 1)) * 2 + side) << 15) + sw * 2048)
#define LOAD(dst, t) dst = bld<NT>(rn, voff + ((t) & 1) * 1024, on + ((t) >> 1) * 8192); __builtin_amdgcn_sched_barrier(0)
#define QK(v) dot2(v[3], q[3], dot2(v[2], q[2], dot2(v[1], q[1], dot2(v[0], q[0], 0.f))))
#define SCORES(i)        \
      _Pragma("unroll") for (int j = 0; j < 4; ++j) s[j] = foldd<XOR7>(s[j], s[j + 4], lane & 4); \
      _Pragma("unroll") for (int j = 0; j < 2; ++j) s[j] = foldd<XOR2>(s[j], s[j + 2], lane & 2); \
      s[0] = foldd<XOR1>(s[0], s[1], lane & 1); \
      s[0] += dpp<XOR8>(s[0]);                                              \
      const unsigned sb = cvtpk(rbf(s[0]) * SCALE, 0.f);                    \
      mx = fmaxf(mx, bflo(sb)); \
      *(LAS u16*)(L + L_SC + (i) * 256 + sw * 64 + r * 16 + (c & 7) * 2) = (u16)sb
#define SAMPLE() now = __builtin_amdgcn_s_memrealtime(); anch = __hip_atomic_load(misc + M_ANCH, RLX_WG); __builtin_amdgcn_sched_barrier(0)
#define PACE(i)          \
      asm volatile("" : "+s"(now)); \
      const unsigned ph = (unsigned)now - (i) * TAU; \
      const bool eg = (i) >= 2 * NS - END_STEPS; \
      const int xw = max((eg ? XCENTER_END : XCENTER) + (((int)(ph - __builtin_amdgcn_readfirstlane(anch)) * (eg ? XGAIN_END : XGAIN)) >> 8), eg ? XMIN_END : XMIN); \
      __hip_atomic_store(misc + M_PH + sw, ph, RLX_WG)
    u32x4 d[8], q;
    u64 now; unsigned anch;
    float mx = -INFINITY;
    {
      const rsrc_t rn = mk_rsrc(kp, SK * 256);
      u32x4 e[8];
      float s[8];
      int on = SOFF(0);
#pragma unroll
      for (int t = 0; t < 8; ++t) { CAPWAIT(CAP0); LOAD(e[t], t); }
      on = SOFF(1);
#pragma unroll
      for (int t = 0; t < 8; ++t) {
        if (t != 4) CAPWAIT(CAP0);
        LOAD(d[t], t);
        if (t == 4) { lds_wait_ge<2>(misc + M_QRDY, 1); q = *(LAS u32x4*)(L + L_QA + c * 16); asm volatile("" : "+v"(q)); }
        if (t >= 4) { s[2 * t - 8] = QK(e[2 * t - 8]); s[2 * t - 7] = QK(e[2 * t - 7]); asm volatile("" : "+v"(s[2 * t - 8]), "+v"(s[2 * t - 7])); }
      }
      SAMPLE();
      SCORES(0);
    }
#pragma nounroll
    for (int i = 1; i < NS; ++i) {
      PACE(i);
      const rsrc_t rn = mk_rsrc(i + 1 < NS ? kp : vp, SK * 256);
      const int on = SOFF(i + 1), xe = i + 1 < NS ? xw : xw + 16;
      float s[8];
#pragma unroll
      for (int t = 0; t < 8; ++t) { s[t] = QK(d[t]); asm volatile("" : "+v"(s[t])); WINWAIT(t < 6 ? xw : xe, t); LOAD(d[t], t); }
      SAMPLE();
      SCORES(i);
    }
    mx = xrow_max(row16_max(mx));
    if (lane == 0) miscf[M_WMAX + sw] = mx;
    lds_signal(misc + M_KDONE, lane);
#define PIN8(a) asm volatile("" : "+v"(a[0]), "+v"(a[1]), "+v"(a[2]), "+v"(a[3]), "+v"(a[4]), "+v"(a[5]), "+v"(a[6]), "+v"(a[7]))
    float acc[8];
#pragma unroll
    for (int j = 0; j < 8; ++j) acc[j] = 0.f;
    u32x4 scn = *(LAS u32x4*)(L + L_SC + sw * 64 + r * 16);
#pragma nounroll
    for (int i = 0; i < NS; ++i) {
      u32x4 sc = scn;
      asm volatile("" : "+v"(sc));
      PACE(NS + i);
      if (sw == 0) __hip_atomic_store(misc + M_STEP, (unsigned)i, RLX_WG);
      scn = *(LAS u32x4*)(L + L_SC + ((i + 1) & (NS - 1)) * 256 + sw * 64 + r * 16);
      const rsrc_t rn = mk_rsrc(vp, i + 1 < NS ? SK * 256 : 0);
      const int on = SOFF(i + 1), xv = i + 1 < NS ? xw : xw + 99;
#pragma unroll
      for (int t = 0; t < 8; t += 2) {
        const unsigned w = cvtpk(ex(bflo(sc[t >> 1]) - mx), ex(bfhi(sc[t >> 1]) - mx));
#pragma unroll
        for (int j = 0; j < 4; ++j) { acc[2 * j] = dot2(d[t][j], w & 0xffffu, acc[2 * j]); acc[2 * j + 1] = dot2(d[t][j], w << 16, acc[2 * j + 1]); }
        PIN8(acc); WINWAIT(xv, t); LOAD(d[t], t);
#pragma unroll
        for (int j = 0; j < 4; ++j) { acc[2 * j] = dot2(d[t + 1][j], w >> 16, acc[2 * j]); acc[2 * j + 1] = dot2(d[t + 1][j], w & 0xffff0000u, acc[2 * j + 1]); }
        PIN8(acc); WINWAIT(xv, t + 1); LOAD(d[t + 1], t + 1);
      }
      SAMPLE();
    }
    *(LAS f32x2*)(L + L_REC + sw * 512 + c * 32 + r * 8) = fold_rows(acc);
    asm volatile("" :: "s"(now), "v"(scn), "v"(anch) : "memory");
    LDS_BARRIER();
    return;
  }

  __builtin_amdgcn_s_setprio(2);
  const int hw = wave;
  const rsrc_t rws = mk_rsrc(ws, WS_BYTES);
  const u64 t0 = __builtin_amdgcn_s_memrealtime();
  LAS float* tmp = (LAS float*)(L + L_TMP);
  u64 epoch = 0;
  const int band = xg >> 1, cg = (xg & 1) * 32 + idx;
  {
    const rsrc_t rx = mk_rsrc(x, NB * DM * 2), rwa = mk_rsrc(wqa, DM * 4096 * 2), rwb = mk_rsrc(wqb, DM * 4096 * 2);
    const int row0 = band * 128 + hw * 32, vo = (lane >> 3) * 4 * 8192 + (lane & 7) * 16;
    u32x4 wA[4], wB[4]; u32x2 xv[NB];
#pragma unroll
    for (int i = 0; i < 4; ++i) wA[i] = bld<NT>(rwa, vo, (row0 + i) * 8192 + cg * 128);
#pragma unroll
    for (int bb = 0; bb < NB; ++bb) xv[bb] = bld2<0>(rx, (lane >> 3) * 8, (bb * DM + row0) * 2);
    if (hw == 0) epoch = __hip_atomic_load((GAS u64*)(ws + WS_EPOCH), RLX_AGENT);
    __builtin_amdgcn_sched_barrier(0);
#pragma unroll
    for (int i = 0; i < 4; ++i) wB[i] = bld<NT>(rwb, vo, (row0 + i) * 8192 + cg * 128);
    __builtin_amdgcn_sched_barrier(0);
    LDS_BARRIER();
    asm volatile("" : "+v"(wA[0]));
    __builtin_amdgcn_sched_barrier(0);
    q_partial(wA, xv, tmp + hw * 256, lane);
    asm volatile("" : "+v"(epoch));
    epoch += 1;
    if (tid == 0) { misc[M_TAGA] = (unsigned)epoch; misc[M_TAGB] = (unsigned)(epoch >> 32) ^ ((unsigned)epoch * 0x9E3779B1u); }
    q_partial(wB, xv, tmp + 1024 + hw * 256, lane);
  }
  hsync(misc, 1, lane);
  const unsigned tagA = misc[M_TAGA], tagB = misc[M_TAGB];
  if (hw == 0) {
#pragma unroll
    for (int br = 0; br < 2; ++br) {
      LAS float* part = tmp + br * 1024 + lane * 4;
      const f32x4 s = (*(LAS f32x4*)part + *(LAS f32x4*)(part + 256)) + (*(LAS f32x4*)(part + 512) + *(LAS f32x4*)(part + 768));
      bst<SC1NT>(__builtin_bit_cast(u32x4, s), rws, (lane >> 4) * 65536 + (lane & 15) * 16, WS_QP + br * 262144 + band * 16384 + cg * 256);
    }
    DRAIN();
    if (lane < 4) bst<SC1>(u32x4{0u, tagA, 0u, tagB}, rws, lane * 16, WS_QF + (cg * 4 + band) * 64);
    const u32x4 xv = bld<0>(mk_rsrc(x, NB * DM * 2), lane * 16, b * DM * 2), gv = bld<0>(mk_rsrc(gw, DM * 2), lane * 16, 0);
    float s = 0.f;
#pragma unroll
    for (int j = 0; j < 4; ++j) { const unsigned p = cvtpk(bflo(xv[j]) * bflo(gv[j]), bfhi(xv[j]) * bfhi(gv[j])); s += bflo(p); s += bfhi(p); }
    s = rbf(wave_sum(s));
    if (lane == 0) miscf[M_GATE] = rbf(1.f / (1.f + __expf(-s)));
  }
  {
    wait_time<4>(t0, T_Q);
    u32x4 f[1];
    poll<1, SC1>(f, rws, (lane & 7) * 64, 0, WS_QF + (xg * 4 + hw) * 512, tagA, tagB);
    u32x2 pb[4];
    if (hw == (h & 3)) {
      u32x2 pa[4];
      q_fetch(pa, rws, 0, b, h, lane);
      q_fetch(pb, rws, 1, b, xg * 4 + hw, lane);
      *(LAS unsigned*)(L + L_QA + lane * 4) = q_sum(pa);
      lds_signal(misc + M_QRDY, lane);
    } else q_fetch(pb, rws, 1, b, xg * 4 + hw, lane);
    *(LAS unsigned*)(L + L_QB + hw * 256 + lane * 4) = q_sum(pb);
    lds_wait_ge<2>(misc + M_BDMA, 4);
  }
  __builtin_amdgcn_s_setprio(0);
  hsync(misc, 2, lane);
  {
    u32x4 q[4];
#pragma unroll
    for (int g = 0; g < 4; ++g) q[g] = *(LAS u32x4*)(L + L_QB + g * 256 + c * 16);
    float m[4], l[4], acc[4][8];
#pragma unroll
    for (int g = 0; g < 4; ++g) { m[g] = -INFINITY; l[g] = 0.f;
#pragma unroll
      for (int j = 0; j < 8; ++j) acc[g][j] = 0.f; }
#pragma nounroll
    for (int s = 0; s < 4; ++s) {
      u32x4 k[4], v[4];
#pragma unroll
      for (int t = 0; t < 4; ++t) { k[t] = *(LAS u32x4*)(L + L_BIG + (hw * 16 + s * 4 + t) * 1024 + lane * 16); v[t] = *(LAS u32x4*)(L + L_BIG + 65536 + (hw * 16 + s * 4 + t) * 1024 + lane * 16); }
      gqa_step(k, v, q, m, l, acc);
    }
#pragma unroll
    for (int g = 0; g < 4; ++g) {
      const float M = xrow_max(m[g]), w = ex(m[g] - M), Ls = xrow_sum(l[g] * w);
#pragma unroll
      for (int j = 0; j < 8; ++j) acc[g][j] *= w;
      LAS float* rec = tmp + (g * 4 + hw) * REC_F;
      *(LAS f32x2*)(rec + 4 + c * 8 + r * 2) = fold_rows(acc[g]);
      if (lane == 0) *(LAS f32x2*)rec = f32x2{M, Ls};
    }
  }
  hsync(misc, 3, lane);
  {
    LAS float* rec = tmp + hw * 4 * REC_F;
    f32x2 ml[4], av[4];
#pragma unroll
    for (int i = 0; i < 4; ++i) { ml[i] = *(LAS f32x2*)(rec + i * REC_F); av[i] = *(LAS f32x2*)(rec + i * REC_F + 4 + lane * 2); }
    const float M = fmaxf(fmaxf(ml[0][0], ml[1][0]), fmaxf(ml[2][0], ml[3][0]));
    float Ls = 0.f, o0 = 0.f, o1 = 0.f;
#pragma unroll
    for (int i = 0; i < 4; ++i) { const float wt = ex(ml[i][0] - M); Ls = fmaf(wt, ml[i][1], Ls); o0 = fmaf(wt, av[i][0], o0); o1 = fmaf(wt, av[i][1], o1); }
    *(LAS unsigned*)(L + L_PUB + hw * 256 + lane * 4) = cvtpk(o0 / Ls, o1 / Ls);
    if (lane == 0) *(LAS f32x2*)(L + L_PUB + 1024 + hw * 8) = f32x2{M, Ls};
  }
  hsync(misc, 4, lane);
  if (hw == 0) {
    const int pi = (b * 8 + xg) * 8 + sp;
    wait_time<4>(t0, T_PB);
    next_slot();
    bst<SC1NT>(*(LAS u32x4*)(L + L_PUB + lane * 16), rws, lane * 16, WS_PBP + pi * 1024);
    const u32x2 ml = *(LAS u32x2*)(L + L_PUB + 1024 + (lane & 3) * 8);
    DRAIN();
    next_slot();
    if (lane < 4) bst<SC1NT>(u32x4{ml[0], tagA, ml[1], tagB}, rws, lane * 16, WS_PBM + pi * 64);
  }
  if (hw == 3) cp_publish(0, misc, rws, bid, tagA, lane);
  wait_time<4>(t0, T_W);
  if (hw == 3) cp_consume(0, misc, rws, tagA, lane);
  {
    const GAS unsigned char* src = (const GAS unsigned char*)(woa + (size_t)(h * 128 + hw * 2) * DM) + lane * 16;
#pragma nounroll
    for (int i0 = 0; i0 < 32; i0 += 4) {
#pragma unroll
      for (int i = i0; i < i0 + 4; ++i) { const int o = (i >> 1) * 8192 + (i & 1) * 1024; CAPWAIT(2); glds16<SC1>(src + o, lds0 + L_BIG + hw * 2048 + o); }
      if (hw == 3) { if (i0 == 12) cp_publish(1, misc, rws, bid, tagA, lane); else if (i0 == 20) cp_consume(1, misc, rws, tagA, lane); }
      if (hw == 0 && i0 == 12) {
        u32x4 ml[1];
        poll<1, SC1NT>(ml, rws, (lane >> 3) * 64, 0, WS_PBM + (b * 8 + xg) * 512 + (h & 3) * 16, tagA, tagB);
        const u32x4 av = bld<SC1NT>(rws, (lane >> 3) * 1024 + (lane & 7) * 16, WS_PBP + (b * 8 + xg) * 8192 + (h & 3) * 256 + side * 128);
        const float pm = __uint_as_float(ml[0][0]), M = xrow_max(fmaxf(pm, dpp<XOR8>(pm)));
        const float wt = ex(pm - M) * __uint_as_float(ml[0][2]), Ls = xrow_sum(wt + dpp<XOR8>(wt));
        float o[8];
#pragma unroll
        for (int j = 0; j < 4; ++j) { o[2 * j] = wt * bflo(av[j]); o[2 * j + 1] = wt * bfhi(av[j]); }
        const f32x2 f = fold_rows(o);
        *(LAS unsigned*)(L + L_PUB + (lane & 7) * 16 + r * 4) = cvtpk((f[0] + dpp<XOR8>(f[0])) / Ls, (f[1] + dpp<XOR8>(f[1])) / Ls);
      }
    }
    DRAIN();
    hsync(misc, 5, lane);
    if (hw == 3) cp_publish(2, misc, rws, bid, tagA, lane);
  }
  unsigned w0[64], w1[64];
#pragma unroll
  for (int p = 0; p < 64; ++p) {
    const unsigned a = *(LAS unsigned*)(L + L_BIG + p * 2048 + tid * 4), bb = *(LAS unsigned*)(L + L_BIG + p * 2048 + 1024 + tid * 4);
    w0[p] = plo(bb, a); w1[p] = phi(bb, a);
    asm volatile("" : "+v"(w0[p]), "+v"(w1[p]));
  }
  hsync(misc, 6, lane);
  unsigned zpk;
  {
    const GAS unsigned char* src = (const GAS unsigned char*)(wob + (size_t)(h * 128 + side * 64 + hw * 2) * DM) + lane * 16;
    if (hw == 3) cp_consume(2, misc, rws, tagA, lane);
#pragma unroll 4
    for (int i = 0; i < 16; ++i) { const int o = (i >> 1) * 8192 + (i & 1) * 1024; CAPWAIT(2); glds16<SC1>(src + o, lds0 + L_BIG + hw * 2048 + o); }
    DRAIN();
    hsync(misc, 7, lane);
    float z0 = 0.f, z1 = 0.f;
#pragma unroll 4
    for (int p = 0; p < 32; ++p) {
      const unsigned a = *(LAS unsigned*)(L + L_BIG + p * 2048 + tid * 4), bb = *(LAS unsigned*)(L + L_BIG + p * 2048 + 1024 + tid * 4), op = *(LAS unsigned*)(L + L_PUB + p * 4);
      z0 = dot2(plo(bb, a), op, z0); z1 = dot2(phi(bb, a), op, z1);
    }
    zpk = cvtpk(z0, z1);
    asm volatile("" : "+v"(zpk));
  }
  {
    lds_wait_ge<32>(misc + M_KDONE, 4);
    const f32x4 wm = *(LAS f32x4*)(miscf + M_WMAX);
    const float M = fmaxf(fmaxf(wm[0], wm[1]), fmaxf(wm[2], wm[3]));
    float ls = 0.f;
#pragma unroll
    for (int i = 0; i < 2; ++i) {
      const u32x4 sc = *(LAS u32x4*)(L + L_SC + hw * 2048 + i * 1024 + lane * 16);
#pragma unroll
      for (int j = 0; j < 4; ++j) ls += ex(bflo(sc[j]) - M) + ex(bfhi(sc[j]) - M);
    }
    ls = wave_sum(ls);
    if (lane == 0) miscf[M_LSUM + hw] = ls;
    hsync(misc, 8, lane);
    if (hw == 0) {
      const f32x4 lw = *(LAS f32x4*)(miscf + M_LSUM);
      const float Ls = (lw[0] + lw[1]) + (lw[2] + lw[3]);
      if (lane < 4) bst<SC1NT>(u32x4{__float_as_uint(M), tagA, __float_as_uint(Ls), tagB}, rws, lane * 16, WS_ST + (b * 64 + jp) * 64);
      wait_time<32>(__builtin_amdgcn_s_memrealtime(), T_ST);
      u32x4 o[1];
      poll<1, SC1NT>(o, rws, 0, 0, WS_ST + (b * 64 + (jp ^ 1)) * 64, tagA, tagB);
      const float Mo = __uint_as_float(o[0][0]), MM = fmaxf(M, Mo);
      if (lane < 4) miscf[M_CNORM + lane] = ex(miscf[M_WMAX + lane] - MM) / (ex(M - MM) * Ls + ex(Mo - MM) * __uint_as_float(o[0][2]));
      if (bid == 0) {
        u32x4 all[4];
        wait_time<32>(__builtin_amdgcn_s_memrealtime(), T_ST);
        poll<4, SC1>(all, rws, lane * 64, 4096, WS_ST, tagA, tagB);
        if (lane == 0) __hip_atomic_store((GAS u64*)(ws + WS_EPOCH), epoch, RLX_AGENT);
      }
    } else if (hw == 3) {
      int k = 3, state = 0, rho = 0, rprev = RHO_MAX, have = 0, TP = 0;
      unsigned P = __builtin_amdgcn_readfirstlane(__hip_atomic_load(misc + M_ANCH, RLX_WG));
#pragma nounroll
      for (unsigned spins = 0; spins < SPIN_CAP && (unsigned)__builtin_amdgcn_readfirstlane(__hip_atomic_load(misc + M_STEP, RLX_WG)) < NS - 3; ++spins) {
        const int t = (int)(__builtin_amdgcn_s_memrealtime() - t0), tk = T_CP0 + (k - 3) * T_CP;
        if (state == 0 && t >= tk && k < NCP) { if (t < tk + T_CPLAG / 2) cp_publish(k, misc, rws, bid, tagA, lane); state = 1; }
        else if (state == 1 && t >= tk + T_CPLAG) {
          const unsigned Pe = P + (((tk - TP) * rho) >> 10), Pn = cp_mean(k, rws, Pe, tagA, lane);
          if (have) { const int rn = min(max((int)(Pn - P) * 1024 / T_CP, RHO_MIN), RHO_MAX); rho = min(rn, rprev); rprev = rn; }
          have = have || Pn != Pe; P = Pn; TP = tk; state = 0; ++k;
        }
        if (lane == 0) __hip_atomic_store(misc + M_ANCH, P + (((t - TP) * rho) >> 10), RLX_WG);
        __builtin_amdgcn_s_sleep(16);
      }
    }
  }
  int yoff = (tid >> 2) * 4096 + (tid & 3) * 16, goff = lane * 64 + hw * 16;
  const float gate = miscf[M_GATE];
  float gsel = lane < 32 ? gate : rbf(1.f - gate);
  asm volatile("" : "+v"(yoff), "+v"(goff), "+v"(gsel));
  if (hw == 1 && idx < 3) { l2_pin(code_here() + idx * 1024, lds0 + L_DUMP, lane); DRAIN(); }
  if (hw == 2) {
    if (lane == 0) __builtin_amdgcn_raw_buffer_store_b32(0u, mk_rsrc(out, NB * DM * 2), 0, (b * DM + jp * 8 + hw * 2) * 2, 0);
    DRAIN();
  }
  LDS_BARRIER();
  const unsigned yanch = misc[M_ANCH];
  {
    const f32x4 cn = *(LAS f32x4*)(miscf + M_CNORM);
    f32x4 rl[4], rh[4];
#pragma unroll
    for (int w = 0; w < 4; ++w) { rl[w] = *(LAS f32x4*)(L + L_REC + w * 512 + c * 32); rh[w] = *(LAS f32x4*)(L + L_REC + w * 512 + c * 32 + 16); }
    __builtin_amdgcn_sched_barrier(0);
    f32x4 lo = cn[0] * rl[0], hi = cn[0] * rh[0];
#pragma unroll
    for (int w = 1; w < 4; ++w) { lo += cn[w] * rl[w]; hi += cn[w] * rh[w]; }
    unsigned ob[4] = {cvtpk(lo[0], lo[1]), cvtpk(lo[2], lo[3]), cvtpk(hi[0], hi[1]), cvtpk(hi[2], hi[3])};
    float y0 = 0.f, y1 = 0.f, y2 = 0.f, y3 = 0.f;
    asm volatile("s_nop 1" : "+v"(ob[0]), "+v"(ob[1]), "+v"(ob[2]), "+v"(ob[3]), "+v"(y0), "+v"(y1), "+v"(y2), "+v"(y3));
    Proj<0>::run(y0, y1, y2, y3, w0, w1, ob);
    asm volatile("s_nop 2" : "+v"(y0), "+v"(y1), "+v"(y2), "+v"(y3));
    const unsigned ypk = cvtpk(y0 + y2, y1 + y3);
    bst<SC1>(u32x4{plo(zpk, ypk), tagA, phi(zpk, ypk), tagB}, rws, yoff, WS_YG + b * 64 * 4096 + jp * 64);
  }
  {
    {
      const u64 tp = __builtin_amdgcn_s_memrealtime();
      const int dl = min(max((int)(__builtin_amdgcn_readfirstlane(yanch) + T_Y - (unsigned)tp), Y_MIN), Y_MAX);
#pragma nounroll
      while ((int)(__builtin_amdgcn_s_memrealtime() - tp) < dl) {}
    }
    u32x4 g[1];
    poll<1, SC1>(g, rws, goff, 0, WS_YG + (b * 64 + jp) * 4096, tagA, tagB);
    const float u = row16_sum(fold16(fold32(bflo(g[0][0]), bfhi(g[0][0])), fold32(bflo(g[0][2]), bfhi(g[0][2]))));
    const float t = rbf(rbf(u) * gsel);
    const unsigned res = cvtpk(fold32(t, t), 0.f);
    if (lane == 0 || lane == 16) __builtin_amdgcn_raw_buffer_store_b16((u16)res, mk_rsrc(out, NB * DM * 2), r * 2, (b * DM + jp * 8 + hw * 2) * 2, 0);
  }
}

extern "C" void kernel_launch(void* const* d_in, const int* in_sizes, int n_in, void* d_out, int out_size, void* d_ws, size_t ws_size, hipStream_t stream) {
  k_fused<<<256, 512, 0, stream>>>((const u16*)d_in[0], (const u16*)d_in[1], (const u16*)d_in[2], (const u16*)d_in[3], (const u16*)d_in[4], (const u16*)d_in[6], (const u16*)d_in[7],
                                   (const u16*)d_in[8], (const u16*)d_in[9], (const u16*)d_in[10], (u16*)d_out, (unsigned char*)d_ws);
}
```

```cpp
#include <hip/hip_runtime.h>
#include <hip/hip_bf16.h>
#include <cstdint>

typedef unsigned u32x4 __attribute__((ext_vector_type(4)));
typedef unsigned u32x2 __attribute__((ext_vector_type(2)));
typedef float f32x4 __attribute__((ext_vector_type(4)));
typedef float f32x2 __attribute__((ext_vector_type(2)));
typedef __attribute__((ext_vector_type(2))) __bf16 bf16x2;
typedef unsigned long long u64;
typedef unsigned short u16;
typedef __amdgpu_buffer_rsrc_t rsrc_t;
#define GAS __attribute__((address_space(1)))
#define LAS __attribute__((address_space(3)))
#define RLX_AGENT __ATOMIC_RELAXED, __HIP_MEMORY_SCOPE_AGENT
#define RLX_WG __ATOMIC_RELAXED, __HIP_MEMORY_SCOPE_WORKGROUP
#define DRAIN() asm volatile("s_waitcnt vmcnt(0)" ::: "memory")
#define LDS_BARRIER() do { asm volatile("s_waitcnt lgkmcnt(0)" ::: "memory"); __builtin_amdgcn_s_barrier(); asm volatile("" ::: "memory"); } while (0)
#define CAPWAIT(n) do { __builtin_amdgcn_sched_barrier(0); asm volatile("s_waitcnt vmcnt(%0)" :: "i"((n) - 1) : "memory"); } while (0)

constexpr int NB = 4, DM = 512, NH = 32, SK = 8192, DH = 128, WIN = 2048;
constexpr float SCALE = 0.088388347648318440f, L2E = 1.4426950408889634f;
constexpr int NT = 2, SC1 = 16, SC1NT = 18, RSRC_W3 = 0x00020000;
constexpr int NS = 32;
constexpr unsigned SPIN_CAP = 1u << 18;

constexpr int CAP0 = 5;
constexpr int TAU = 108;
constexpr int TK0 = 813;
constexpr int XCENTER = 11, XMIN = 6, XGAIN = 10;
constexpr int END_STEPS = 8, XCENTER_END = 9, XMIN_END = 2, XGAIN_END = 45;
constexpr int T_Q = 540, T_PB = 1790, T_W = 2000, T_ST = 500, SLOT = 64;
constexpr int T_CP0 = 4700, T_CP = 500, T_CPLAG = 200, RHO_MIN = -20, RHO_MAX = 60;
constexpr int T_Y = 63 * TAU + 190, Y_MIN = 40, Y_MAX = 72;

constexpr int WS_EPOCH = 0;
constexpr int WS_PH = 4096, NCP = 14;
constexpr int WS_QF = 32768;
constexpr int WS_QP = WS_QF + 16384;
constexpr int WS_PBM = WS_QP + 524288;
constexpr int WS_ST = WS_PBM + 16384;
constexpr int WS_PBP = WS_ST + 16384;
constexpr int WS_YG = WS_PBP + 262144;
constexpr int WS_BYTES = WS_YG + 256 * 4096;

constexpr int L_MISC = 0, L_QA = 256, L_QB = 512, L_REC = 1536, L_SC = 4096, L_TMP = 12288, L_PUB = L_TMP + 8704, L_BIG = 22528, L_DUMP = L_BIG + 131072, L_BYTES = L_DUMP + 1024;
constexpr int REC_F = 132;
enum { M_TAGA = 0, M_TAGB = 1, M_ANCH = 2, M_STEP = 3, M_QRDY = 4, M_HSYNC = 5, M_KDONE = 6, M_BDMA = 7, M_WMAX = 8, M_LSUM = 12, M_CNORM = 16, M_PH = 20, M_GATE = 24 };

__device__ __forceinline__ float dot2(unsigned a, unsigned b, float c) {
  return __builtin_amdgcn_fdot2_f32_bf16(__builtin_bit_cast(bf16x2, a), __builtin_bit_cast(bf16x2, b), c, false);
}
__device__ __forceinline__ unsigned cvtpk(float lo, float hi) { return __builtin_bit_cast(unsigned, __builtin_convertvector(f32x2{lo, hi}, bf16x2)); }
__device__ __forceinline__ float bflo(unsigned u) { return __uint_as_float(u << 16); }
__device__ __forceinline__ float bfhi(unsigned u) { return __uint_as_float(u & 0xffff0000u); }
__device__ __forceinline__ float rbf(float x) { return bflo(cvtpk(x, 0.f)); }
__device__ __forceinline__ float ex(float x) { return __builtin_amdgcn_exp2f(x * L2E); }
__device__ __forceinline__ unsigned plo(unsigned hi, unsigned lo) { return __builtin_amdgcn_perm(hi, lo, 0x05040100u); }
__device__ __forceinline__ unsigned phi(unsigned hi, unsigned lo) { return __builtin_amdgcn_perm(hi, lo, 0x07060302u); }
template <int CTRL> __device__ __forceinline__ float dpp(float x) {
  return __builtin_bit_cast(float, __builtin_amdgcn_mov_dpp(__builtin_bit_cast(int, x), CTRL, 0xf, 0xf, true));
}
constexpr int XOR1 = 0xB1, XOR2 = 0x4E, XOR7 = 0x141, XOR8 = 0x128;
__device__ __forceinline__ float row16_sum(float x) { x += dpp<XOR1>(x); x += dpp<XOR2>(x); x += dpp<XOR7>(x); x += dpp<XOR8>(x); return x; }
__device__ __forceinline__ float row16_max(float x) { x = fmaxf(x, dpp<XOR1>(x)); x = fmaxf(x, dpp<XOR2>(x)); x = fmaxf(x, dpp<XOR7>(x)); return fmaxf(x, dpp<XOR8>(x)); }
template <int CTRL> __device__ __forceinline__ float foldd(float a, float b, bool hi) { return (hi ? b : a) + dpp<CTRL>(hi ? a : b); }
__device__ __forceinline__ float fold32(float x, float y) {
  auto t = __builtin_amdgcn_permlane32_swap(__float_as_uint(x), __float_as_uint(y), false, false);
  return __uint_as_float(t[0]) + __uint_as_float(t[1]);
}
__device__ __forceinline__ float fold16(float x, float y) {
  auto t = __builtin_amdgcn_permlane16_swap(__float_as_uint(x), __float_as_uint(y), false, false);
  return __uint_as_float(t[0]) + __uint_as_float(t[1]);
}
__device__ __forceinline__ float xrow_sum(float x) { x = fold16(x, x); return fold32(x, x); }
__device__ __forceinline__ float xrow_max(float x) {
  auto s = __builtin_amdgcn_permlane16_swap(__float_as_uint(x), __float_as_uint(x), false, false);
  x = fmaxf(__uint_as_float(s[0]), __uint_as_float(s[1]));
  auto t = __builtin_amdgcn_permlane32_swap(__float_as_uint(x), __float_as_uint(x), false, false);
  return fmaxf(__uint_as_float(t[0]), __uint_as_float(t[1]));
}
__device__ __forceinline__ float wave_sum(float x) { return xrow_sum(row16_sum(x)); }
__device__ __forceinline__ f32x2 fold_rows(const float (&a)[8]) {
  const float u0 = fold32(a[0], a[4]), u1 = fold32(a[1], a[5]), u2 = fold32(a[2], a[6]), u3 = fold32(a[3], a[7]);
  return f32x2{fold16(u0, u2), fold16(u1, u3)};
}

__device__ __forceinline__ rsrc_t mk_rsrc(const void* p, int bytes) { return __builtin_amdgcn_make_buffer_rsrc(const_cast<void*>(p), 0, bytes, RSRC_W3); }
template <int AUX> __device__ __forceinline__ u32x4 bld(rsrc_t r, int voff, int soff) { return __builtin_amdgcn_raw_buffer_load_b128(r, voff, soff, AUX); }
template <int AUX> __device__ __forceinline__ u32x2 bld2(rsrc_t r, int voff, int soff) { return __builtin_amdgcn_raw_buffer_load_b64(r, voff, soff, AUX); }
template <int AUX> __device__ __forceinline__ void bst(u32x4 v, rsrc_t r, int voff, int soff) { __builtin_amdgcn_raw_buffer_store_b128(v, r, voff, soff, AUX); }
template <int AUX> __device__ __forceinline__ void glds16(const GAS unsigned char* src, unsigned dst) {
  unsigned keep;
  if (AUX == NT) asm volatile("s_mov_b32 %0, m0\n\ts_mov_b32 m0, %2\n\ts_nop 0\n\tglobal_load_lds_dwordx4 %1, off nt\n\ts_mov_b32 m0, %0" : "=&s"(keep) : "v"(src), "s"(dst) : "memory");
  else asm volatile("s_mov_b32 %0, m0\n\ts_mov_b32 m0, %2\n\ts_nop 0\n\tglobal_load_lds_dwordx4 %1, off sc1\n\ts_mov_b32 m0, %0" : "=&s"(keep) : "v"(src), "s"(dst) : "memory");
}
__device__ __forceinline__ void l2_pin(const GAS unsigned char* p, unsigned dump, int lane) {
  unsigned keep;
  asm volatile("s_mov_b32 %0, m0\n\ts_mov_b32 m0, %3\n\ts_nop 0\n\tglobal_load_lds_dwordx4 %1, off nt\n\tglobal_load_lds_dword %2, off sc1\n\ts_mov_b32 m0, %0"
               : "=&s"(keep) : "v"(p + lane * 16), "v"(p + (lane & 7) * 128), "s"(dump) : "memory");
}
__device__ __forceinline__ const GAS unsigned char* code_here() { u64 pc; asm volatile("s_getpc_b64 %0" : "=s"(pc)); return (const GAS unsigned char*)(pc & ~127ull); }

#define DITH(t) ((((t) & 1) << 2) | ((t) & 2) | (((t) & 4) >> 2))
#define WINWAIT(x, t) do { __builtin_amdgcn_sched_barrier(0); asm volatile("s_cmp_gt_i32 %0, %2\n\ts_cbranch_scc1 2f\n\ts_cmp_gt_i32 %0, %1\n\ts_cbranch_scc1 1f\n\ts_waitcnt vmcnt(3)\n1:\n\ts_waitcnt vmcnt(4)\n" \
    "2:\n\ts_cmp_gt_i32 %0, %3\n\ts_cbranch_scc1 3f\n\ts_waitcnt vmcnt(5)\n3:\n\ts_waitcnt vmcnt(6)" :: "s"(x), "i"(7 - DITH(t)), "i"(15 - DITH(t)), "i"(23 - DITH(t)) : "memory", "scc"); } while (0)

template <int SLEEP> __device__ __forceinline__ void wait_time(u64 t0, unsigned ticks) {
#pragma nounroll
  while (__builtin_amdgcn_s_memrealtime() - t0 < ticks) __builtin_amdgcn_s_sleep(SLEEP);
}
__device__ __forceinline__ void next_slot() {
  const unsigned n0 = (unsigned)__builtin_amdgcn_s_memrealtime(), left = SLOT - n0 % SLOT;
#pragma nounroll
  while ((unsigned)__builtin_amdgcn_s_memrealtime() - n0 < left) __builtin_amdgcn_s_sleep(1);
  asm volatile("" ::: "memory");
}
template <int SLEEP> __device__ __forceinline__ void lds_wait_ge(LAS unsigned* p, unsigned target) {
#pragma nounroll
  for (unsigned spins = 0; spins < 32 * SPIN_CAP && (unsigned)__builtin_amdgcn_readfirstlane(__hip_atomic_load(p, RLX_WG)) < target; ++spins) __builtin_amdgcn_s_sleep(SLEEP);
  asm volatile("" ::: "memory");
}
__device__ __forceinline__ void lds_signal(LAS unsigned* p, int lane) {
  asm volatile("s_waitcnt lgkmcnt(0)" ::: "memory");
  if (lane == 0) __builtin_amdgcn_atomic_inc32(p, 0xffffffffu, __ATOMIC_RELAXED, "workgroup");
}
__device__ __forceinline__ void hsync(LAS unsigned* misc, unsigned n, int lane) { lds_signal(misc + M_HSYNC, lane); lds_wait_ge<2>(misc + M_HSYNC, 4 * n); }
template <int N, int AUX> __device__ __forceinline__ void poll(u32x4 (&g)[N], rsrc_t rws, int voff, int stride, int soff, unsigned tagA, unsigned tagB) {
#pragma nounroll
  for (unsigned spins = 0; spins < SPIN_CAP; ++spins) {
    asm volatile("" ::: "memory");
    bool ok = true;
#pragma unroll
    for (int i = 0; i < N; ++i) g[i] = bld<AUX>(rws, voff + i * stride, soff);
#pragma unroll
    for (int i = 0; i < N; ++i) ok &= (g[i][1] == tagA) & (g[i][3] == tagB);
    if (__all(ok)) break;
    __builtin_amdgcn_s_sleep(2);
  }
  __builtin_amdgcn_fence(__ATOMIC_ACQUIRE, "wavefront");
  asm volatile("" ::: "memory");
}
__device__ __forceinline__ void cp_publish(int k, LAS unsigned* misc, rsrc_t rws, int bid, unsigned tagA, int lane) {
  if ((((bid >> 3) + k) & 15) == 0) {
    asm volatile("" ::: "memory");
    const u32x4 wp = *(LAS u32x4*)(misc + M_PH);
    const unsigned ph = wp[0] + (((int)(wp[1] - wp[0]) + (int)(wp[2] - wp[0]) + (int)(wp[3] - wp[0])) >> 2);
    if (lane < 8) __builtin_amdgcn_raw_buffer_store_b64(u32x2{ph, tagA}, rws, lane * 8, WS_PH + k * 2048 + ((bid & 7) + ((bid >> 7) << 3)) * 64, SC1);
  }
}
__device__ __forceinline__ unsigned cp_mean(int k, rsrc_t rws, unsigned ref, unsigned tagA, int lane) {
  asm volatile("" ::: "memory");
  const u32x2 g = bld2<SC1>(rws, (lane & 15) * 64, WS_PH + k * 2048);
  const bool ok = g[1] == tagA;
  const float d = wave_sum(ok ? (float)(int)(g[0] - ref) : 0.f), n = wave_sum(ok ? 1.f : 0.f);
  return __builtin_amdgcn_readfirstlane(n >= 32.f ? ref + (int)(d / n) : ref);
}
__device__ __forceinline__ void cp_consume(int k, LAS unsigned* misc, rsrc_t rws, unsigned tagA, int lane) {
  const unsigned a = cp_mean(k, rws, __hip_atomic_load(misc + M_ANCH, RLX_WG), tagA, lane);
  if (lane == 0) __hip_atomic_store(misc + M_ANCH, a, RLX_WG);
}

__device__ __forceinline__ void q_partial(const u32x4 (&w)[4], const u32x2 (&xv)[NB], LAS float* part, int lane) {
  float a[NB][8];
#pragma unroll
  for (int j = 0; j < 4; ++j) {
    const unsigned p0 = plo(w[1][j], w[0][j]), p1 = phi(w[1][j], w[0][j]), p2 = plo(w[3][j], w[2][j]), p3 = phi(w[3][j], w[2][j]);
#pragma unroll
    for (int bb = 0; bb < NB; ++bb) { a[bb][2 * j] = dot2(p2, xv[bb][1], dot2(p0, xv[bb][0], 0.f)); a[bb][2 * j + 1] = dot2(p3, xv[bb][1], dot2(p1, xv[bb][0], 0.f)); }
  }
  float f[8];
#pragma unroll
  for (int j = 0; j < 8; ++j) f[j] = fold16(fold32(a[0][j], a[2][j]), fold32(a[1][j], a[3][j]));
#pragma unroll
  for (int j = 0; j < 4; ++j) f[j] = foldd<XOR8>(f[j], f[j + 4], lane & 8);
  *(LAS f32x4*)(part + (lane >> 4) * 64 + (lane & 7) * 8 + ((lane >> 3) & 1) * 4) = f32x4{f[0], f[1], f[2], f[3]};
}
__device__ __forceinline__ void q_fetch(u32x2 (&p)[4], rsrc_t rws, int br, int b, int hq, int lane) {
#pragma unroll
  for (int i = 0; i < 4; ++i) p[i] = bld2<SC1NT>(rws, lane * 8 + i * 16384, WS_QP + br * 262144 + b * 65536 + hq * 512);
}
__device__ __forceinline__ unsigned q_sum(const u32x2 (&p)[4]) {
  const f32x2 s = (__builtin_bit_cast(f32x2, p[0]) + __builtin_bit_cast(f32x2, p[1])) + (__builtin_bit_cast(f32x2, p[2]) + __builtin_bit_cast(f32x2, p[3]));
  return cvtpk(s[0], s[1]);
}
__device__ __forceinline__ void gqa_step(const u32x4 (&k)[4], const u32x4 (&v)[4], const u32x4 (&q)[4], float (&m)[4], float (&l)[4], float (&acc)[4][8]) {
#pragma unroll
  for (int g = 0; g < 4; ++g) {
    float s[4];
#pragma unroll
    for (int t = 0; t < 4; ++t) s[t] = dot2(k[t][3], q[g][3], dot2(k[t][2], q[g][2], dot2(k[t][1], q[g][1], dot2(k[t][0], q[g][0], 0.f))));
#pragma unroll
    for (int t = 0; t < 4; t += 2) {
      const unsigned r1 = cvtpk(row16_sum(s[t]), row16_sum(s[t + 1]));
      const unsigned r2 = cvtpk(bflo(r1) * SCALE, bfhi(r1) * SCALE);
      s[t] = bflo(r2); s[t + 1] = bfhi(r2);
    }
    const float mn = fmaxf(fmaxf(m[g], fmaxf(s[0], s[1])), fmaxf(s[2], s[3]));
    const float al = ex(m[g] - mn);
    m[g] = mn;
    float ls = l[g] * al;
#pragma unroll
    for (int j = 0; j < 8; ++j) acc[g][j] *= al;
#pragma unroll
    for (int t = 0; t < 4; t += 2) {
      const float p0 = ex(s[t] - mn), p1 = ex(s[t + 1] - mn);
      ls += p0 + p1;
      const unsigned w = cvtpk(p0, p1);
#pragma unroll
      for (int j = 0; j < 4; ++j) {
        acc[g][2 * j] = dot2(v[t + 1][j], w >> 16, dot2(v[t][j], w & 0xffffu, acc[g][2 * j]));
        acc[g][2 * j + 1] = dot2(v[t + 1][j], w & 0xffff0000u, dot2(v[t][j], w << 16, acc[g][2 * j + 1]));
      }
    }
    l[g] = ls;
  }
}
template <int C> struct Proj {
  static __device__ __forceinline__ void run(float& y0, float& y1, float& y2, float& y3, const unsigned (&w0)[64], const unsigned (&w1)[64], const unsigned (&ob)[4]) {
#define DOTB(y, o, w) "v_dot2c_f32_bf16_dpp " y ", " o ", " w " row_newbcast:%16 row_mask:0xf bank_mask:0xf\n\t"
    asm volatile(DOTB("%0", "%4", "%8") DOTB("%1", "%4", "%12") DOTB("%2", "%5", "%9") DOTB("%3", "%5", "%13") DOTB("%0", "%6", "%10") DOTB("%1", "%6", "%14") DOTB("%2", "%7", "%11") DOTB("%3", "%7", "%15")
                 : "+v"(y0), "+v"(y1), "+v"(y2), "+v"(y3)
                 : "v"(ob[0]), "v"(ob[1]), "v"(ob[2]), "v"(ob[3]), "v"(w0[4 * C]), "v"(w0[4 * C + 1]), "v"(w0[4 * C + 2]), "v"(w0[4 * C + 3]),
                   "v"(w1[4 * C]), "v"(w1[4 * C + 1]), "v"(w1[4 * C + 2]), "v"(w1[4 * C + 3]), "i"(C));
#undef DOTB
    if constexpr (C + 1 < 16) Proj<C + 1>::run(y0, y1, y2, y3, w0, w1, ob);
  }
};

__global__ void __launch_bounds__(512, 2) k_fused(const u16* __restrict__ x, const u16* __restrict__ ka, const u16* __restrict__ va, const u16* __restrict__ wqa, const u16* __restrict__ woa,
                                                  const u16* __restrict__ kb, const u16* __restrict__ vb, const u16* __restrict__ wqb, const u16* __restrict__ wob,
                                                  const u16* __restrict__ gw, u16* __restrict__ out, unsigned char* __restrict__ ws) {
  __shared__ __attribute__((aligned(1024))) unsigned char smem[L_BYTES];
  LAS unsigned char* L = (LAS unsigned char*)smem;
  LAS unsigned* misc = (LAS unsigned*)(L + L_MISC);
  LAS float* miscf = (LAS float*)(L + L_MISC);
  const unsigned lds0 = (unsigned)(size_t)L;
  const int tid = threadIdx.x, lane = tid & 63, wave = __builtin_amdgcn_readfirstlane(tid >> 6), r = lane >> 4, c = lane & 15;
  const int bid = blockIdx.x, xg = bid & 7, idx = bid >> 3, b = idx & 3, sp = idx >> 2;
  const int side = sp & 1, h = xg * 4 + (idx >> 3), jp = h * 2 + side;
  asm volatile("" :: "s"(x), "s"(ka), "s"(va), "s"(wqa), "s"(woa), "s"(kb), "s"(vb), "s"(wqb), "s"(wob), "s"(gw), "s"(out), "s"(ws));

  if (wave >= 4) {
    __builtin_amdgcn_s_setprio(1);
    const int sw = wave - 4, voff = lane * 16;
    if (tid == 256) {
      const unsigned a0 = (unsigned)__builtin_amdgcn_s_memrealtime() + TK0;
      *(LAS u32x2*)(misc + M_ANCH) = u32x2{a0, 0u}; *(LAS u32x4*)(misc + M_QRDY) = u32x4{0u, 0u, 0u, 0u}; *(LAS u32x4*)(misc + M_PH) = u32x4{a0, a0, a0, a0};
    }
    if (sw == 3 && idx < 5) l2_pin(code_here() + idx * 1024, lds0 + L_DUMP, lane);
    LDS_BARRIER();
    {
      const size_t o = ((size_t)(b * 8 + xg) * SK + (SK - WIN) + sp * 64 + sw * 16) * DH;
      const GAS unsigned char* pk = (const GAS unsigned char*)(kb + o) + voff;
      const GAS unsigned char* pv = (const GAS unsigned char*)(vb + o) + voff;
#pragma unroll 4
      for (int i = 0; i < 16; ++i) { CAPWAIT(CAP0); glds16<NT>(pk + (((i >> 2) + xg) & 3) * 131072 + (i & 3) * 1024, lds0 + L_BIG + sw * 4096 + (i >> 2) * 16384 + (i & 3) * 1024); }
#pragma unroll 4
      for (int i = 0; i < 16; ++i) { CAPWAIT(CAP0); glds16<NT>(pv + (((i >> 2) + xg) & 3) * 131072 + (i & 3) * 1024, lds0 + L_BIG + 65536 + sw * 4096 + (i >> 2) * 16384 + (i & 3) * 1024); }
    }
    {
      const size_t o = (size_t)(h * 128 + (side * 4 + b) * 16 + sw * 4) * DM;
#pragma unroll
      for (int i = 0; i < 8; ++i) { CAPWAIT(i < 4 ? CAP0 + i : 7); l2_pin((const GAS unsigned char*)((i < 4 ? wob : woa) + o) + (i & 3) * 1024, lds0 + L_DUMP, lane); }
    }
    lds_signal(misc + M_BDMA, lane);
    const u16* kp = ka + (size_t)(b * NH + h) * SK * DH;
    const u16* vp = va + (size_t)(b * NH + h) * SK * DH;
#define SOFF(i) ((((((i) + h) & (NS - 1)) * 2 + side) << 15) + sw * 2048)
#define LOAD(dst, t) dst = bld<NT>(rn, voff + ((t) & 1) * 1024, on + ((t) >> 1) * 8192); __builtin_amdgcn_sched_barrier(0)
#define QK(v) dot2(v[3], q[3], dot2(v[2], q[2], dot2(v[1], q[1], dot2(v[0], q[0], 0.f))))
#define SCORES(i)        \
      _Pragma("unroll") for (int j = 0; j < 4; ++j) s[j] = foldd<XOR7>(s[j], s[j + 4], lane & 4); \
      _Pragma("unroll") for (int j = 0; j < 2; ++j) s[j] = foldd<XOR2>(s[j], s[j + 2], lane & 2); \
      s[0] = foldd<XOR1>(s[0], s[1], lane & 1); \
      s[0] += dpp<XOR8>(s[0]);                                              \
      const unsigned sb = cvtpk(rbf(s[0]) * SCALE, 0.f);                    \
      mx = fmaxf(mx, bflo(sb)); \
      *(LAS u16*)(L + L_SC + (i) * 256 + sw * 64 + r * 16 + (c & 7) * 2) = (u16)sb
#define SAMPLE() now = __builtin_amdgcn_s_memrealtime(); anch = __hip_atomic_load(misc + M_ANCH, RLX_WG); __builtin_amdgcn_sched_barrier(0)
#define PACE(i)          \
      asm volatile("" : "+s"(now)); \
      const unsigned ph = (unsigned)now - (i) * TAU; \
      const bool eg = (i) >= 2 * NS - END_STEPS; \
      const int xw = max((eg ? XCENTER_END : XCENTER) + (((int)(ph - __builtin_amdgcn_readfirstlane(anch)) * (eg ? XGAIN_END : XGAIN)) >> 8), eg ? XMIN_END : XMIN); \
      __hip_atomic_store(misc + M_PH + sw, ph, RLX_WG)
    u32x4 d[8], q;
    u64 now; unsigned anch;
    float mx = -INFINITY;
    {
      const rsrc_t rn = mk_rsrc(kp, SK * 256);
      u32x4 e[8];
      float s[8];
      int on = SOFF(0);
#pragma unroll
      for (int t = 0; t < 8; ++t) { CAPWAIT(t < 4 ? CAP0 + 4 - t : CAP0); LOAD(e[t], t); }
      on = SOFF(1);
#pragma unroll
      for (int t = 0; t < 8; ++t) {
        if (t != 4) CAPWAIT(CAP0);
        LOAD(d[t], t);
        if (t == 4) { lds_wait_ge<2>(misc + M_QRDY, 1); q = *(LAS u32x4*)(L + L_QA + c * 16); asm volatile("" : "+v"(q)); }
        if (t >= 4) { s[2 * t - 8] = QK(e[2 * t - 8]); s[2 * t - 7] = QK(e[2 * t - 7]); asm volatile("" : "+v"(s[2 * t - 8]), "+v"(s[2 * t - 7])); }
      }
      SAMPLE();
      SCORES(0);
    }
#pragma nounroll
    for (int i = 1; i < NS; ++i) {
      PACE(i);
      const rsrc_t rn = mk_rsrc(i + 1 < NS ? kp : vp, SK * 256);
      const int on = SOFF(i + 1), xe = i + 1 < NS ? xw : xw + 16;
      float s[8];
#pragma unroll
      for (int t = 0; t < 8; ++t) { s[t] = QK(d[t]); asm volatile("" : "+v"(s[t])); WINWAIT(t < 6 ? xw : xe, t); LOAD(d[t], t); }
      SAMPLE();
      SCORES(i);
    }
    mx = xrow_max(row16_max(mx));
    if (lane == 0) miscf[M_WMAX + sw] = mx;
    lds_signal(misc + M_KDONE, lane);
#define PIN8(a) asm volatile("" : "+v"(a[0]), "+v"(a[1]), "+v"(a[2]), "+v"(a[3]), "+v"(a[4]), "+v"(a[5]), "+v"(a[6]), "+v"(a[7]))
    float acc[8];
#pragma unroll
    for (int j = 0; j < 8; ++j) acc[j] = 0.f;
    u32x4 scn = *(LAS u32x4*)(L + L_SC + sw * 64 + r * 16);
#pragma nounroll
    for (int i = 0; i < NS; ++i) {
      u32x4 sc = scn;
      asm volatile("" : "+v"(sc));
      PACE(NS + i);
      if (sw == 0) __hip_atomic_store(misc + M_STEP, (unsigned)i, RLX_WG);
      scn = *(LAS u32x4*)(L + L_SC + ((i + 1) & (NS - 1)) * 256 + sw * 64 + r * 16);
      const rsrc_t rn = mk_rsrc(vp, i + 1 < NS ? SK * 256 : 0);
      const int on = SOFF(i + 1), xv = i + 1 < NS ? xw : xw + 99;
#pragma unroll
      for (int t = 0; t < 8; t += 2) {
        const unsigned w = cvtpk(ex(bflo(sc[t >> 1]) - mx), ex(bfhi(sc[t >> 1]) - mx));
#pragma unroll
        for (int j = 0; j < 4; ++j) { acc[2 * j] = dot2(d[t][j], w & 0xffffu, acc[2 * j]); acc[2 * j + 1] = dot2(d[t][j], w << 16, acc[2 * j + 1]); }
        PIN8(acc); WINWAIT(xv, t); LOAD(d[t], t);
#pragma unroll
        for (int j = 0; j < 4; ++j) { acc[2 * j] = dot2(d[t + 1][j], w >> 16, acc[2 * j]); acc[2 * j + 1] = dot2(d[t + 1][j], w & 0xffff0000u, acc[2 * j + 1]); }
        PIN8(acc); WINWAIT(xv, t + 1); LOAD(d[t + 1], t + 1);
      }
      SAMPLE();
    }
    *(LAS f32x2*)(L + L_REC + sw * 512 + c * 32 + r * 8) = fold_rows(acc);
    asm volatile("" :: "s"(now), "v"(scn), "v"(anch) : "memory");
    LDS_BARRIER();
    return;
  }

  __builtin_amdgcn_s_setprio(2);
  const int hw = wave;
  const rsrc_t rws = mk_rsrc(ws, WS_BYTES);
  const u64 t0 = __builtin_amdgcn_s_memrealtime();
  LAS float* tmp = (LAS float*)(L + L_TMP);
  u64 epoch = 0;
  const int band = xg >> 1, cg = (xg & 1) * 32 + idx;
  {
    const rsrc_t rx = mk_rsrc(x, NB * DM * 2), rwa = mk_rsrc(wqa, DM * 4096 * 2), rwb = mk_rsrc(wqb, DM * 4096 * 2);
    const int row0 = band * 128 + hw * 32, vo = (lane >> 3) * 4 * 8192 + (lane & 7) * 16;
    u32x4 wA[4], wB[4]; u32x2 xv[NB];
#pragma unroll
    for (int i = 0; i < 4; ++i) wA[i] = bld<NT>(rwa, vo, (row0 + i) * 8192 + cg * 128);
#pragma unroll
    for (int bb = 0; bb < NB; ++bb) xv[bb] = bld2<0>(rx, (lane >> 3) * 8, (bb * DM + row0) * 2);
    if (hw == 0) epoch = __hip_atomic_load((GAS u64*)(ws + WS_EPOCH), RLX_AGENT);
    __builtin_amdgcn_sched_barrier(0);
#pragma unroll
    for (int i = 0; i < 4; ++i) wB[i] = bld<NT>(rwb, vo, (row0 + i) * 8192 + cg * 128);
    __builtin_amdgcn_sched_barrier(0);
    LDS_BARRIER();
    asm volatile("" : "+v"(wA[0]));
    __builtin_amdgcn_sched_barrier(0);
    q_partial(wA, xv, tmp + hw * 256, lane);
    asm volatile("" : "+v"(epoch));
    epoch += 1;
    if (tid == 0) { misc[M_TAGA] = (unsigned)epoch; misc[M_TAGB] = (unsigned)(epoch >> 32) ^ ((unsigned)epoch * 0x9E3779B1u); }
    q_partial(wB, xv, tmp + 1024 + hw * 256, lane);
  }
  hsync(misc, 1, lane);
  const unsigned tagA = misc[M_TAGA], tagB = misc[M_TAGB];
  if (hw == 0) {
#pragma unroll
    for (int br = 0; br < 2; ++br) {
      LAS float* part = tmp + br * 1024 + lane * 4;
      const f32x4 s = (*(LAS f32x4*)part + *(LAS f32x4*)(part + 256)) + (*(LAS f32x4*)(part + 512) + *(LAS f32x4*)(part + 768));
      bst<SC1NT>(__builtin_bit_cast(u32x4, s), rws, (lane >> 4) * 65536 + (lane & 15) * 16, WS_QP + br * 262144 + band * 16384 + cg * 256);
    }
    DRAIN();
    if (lane < 4) bst<SC1>(u32x4{0u, tagA, 0u, tagB}, rws, lane * 16, WS_QF + (cg * 4 + band) * 64);
    const u32x4 xv = bld<0>(mk_rsrc(x, NB * DM * 2), lane * 16, b * DM * 2), gv = bld<0>(mk_rsrc(gw, DM * 2), lane * 16, 0);
    float s = 0.f;
#pragma unroll
    for (int j = 0; j < 4; ++j) { const unsigned p = cvtpk(bflo(xv[j]) * bflo(gv[j]), bfhi(xv[j]) * bfhi(gv[j])); s += bflo(p); s += bfhi(p); }
    s = rbf(wave_sum(s));
    if (lane == 0) miscf[M_GATE] = rbf(1.f / (1.f + __expf(-s)));
  }
  {
    wait_time<4>(t0, T_Q);
    u32x4 f[1];
    poll<1, SC1>(f, rws, (lane & 7) * 64, 0, WS_QF + (xg * 4 + hw) * 512, tagA, tagB);
    u32x2 pb[4];
    if (hw == (h & 3)) {
      u32x2 pa[4];
      q_fetch(pa, rws, 0, b, h, lane);
      q_fetch(pb, rws, 1, b, xg * 4 + hw, lane);
      *(LAS unsigned*)(L + L_QA + lane * 4) = q_sum(pa);
      lds_signal(misc + M_QRDY, lane);
    } else q_fetch(pb, rws, 1, b, xg * 4 + hw, lane);
    *(LAS unsigned*)(L + L_QB + hw * 256 + lane * 4) = q_sum(pb);
    lds_wait_ge<2>(misc + M_BDMA, 4);
  }
  __builtin_amdgcn_s_setprio(0);
  hsync(misc, 2, lane);
  {
    u32x4 q[4];
#pragma unroll
    for (int g = 0; g < 4; ++g) q[g] = *(LAS u32x4*)(L + L_QB + g * 256 + c * 16);
    float m[4], l[4], acc[4][8];
#pragma unroll
    for (int g = 0; g < 4; ++g) { m[g] = -INFINITY; l[g] = 0.f;
#pragma unroll
      for (int j = 0; j < 8; ++j) acc[g][j] = 0.f; }
#pragma nounroll
    for (int s = 0; s < 4; ++s) {
      u32x4 k[4], v[4];
#pragma unroll
      for (int t = 0; t < 4; ++t) { k[t] = *(LAS u32x4*)(L + L_BIG + (hw * 16 + s * 4 + t) * 1024 + lane * 16); v[t] = *(LAS u32x4*)(L + L_BIG + 65536 + (hw * 16 + s * 4 + t) * 1024 + lane * 16); }
      gqa_step(k, v, q, m, l, acc);
    }
#pragma unroll
    for (int g = 0; g < 4; ++g) {
      const float M = xrow_max(m[g]), w = ex(m[g] - M), Ls = xrow_sum(l[g] * w);
#pragma unroll
      for (int j = 0; j < 8; ++j) acc[g][j] *= w;
      LAS float* rec = tmp + (g * 4 + hw) * REC_F;
      *(LAS f32x2*)(rec + 4 + c * 8 + r * 2) = fold_rows(acc[g]);
      if (lane == 0) *(LAS f32x2*)rec = f32x2{M, Ls};
    }
  }
  hsync(misc, 3, lane);
  {
    LAS float* rec = tmp + hw * 4 * REC_F;
    f32x2 ml[4], av[4];
#pragma unroll
    for (int i = 0; i < 4; ++i) { ml[i] = *(LAS f32x2*)(rec + i * REC_F); av[i] = *(LAS f32x2*)(rec + i * REC_F + 4 + lane * 2); }
    const float M = fmaxf(fmaxf(ml[0][0], ml[1][0]), fmaxf(ml[2][0], ml[3][0]));
    float Ls = 0.f, o0 = 0.f, o1 = 0.f;
#pragma unroll
    for (int i = 0; i < 4; ++i) { const float wt = ex(ml[i][0] - M); Ls = fmaf(wt, ml[i][1], Ls); o0 = fmaf(wt, av[i][0], o0); o1 = fmaf(wt, av[i][1], o1); }
    *(LAS unsigned*)(L + L_PUB + hw * 256 + lane * 4) = cvtpk(o0 / Ls, o1 / Ls);
    if (lane == 0) *(LAS f32x2*)(L + L_PUB + 1024 + hw * 8) = f32x2{M, Ls};
  }
  hsync(misc, 4, lane);
  if (hw == 0) {
    const int pi = (b * 8 + xg) * 8 + sp;
    wait_time<4>(t0, T_PB);
    next_slot();
    bst<SC1NT>(*(LAS u32x4*)(L + L_PUB + lane * 16), rws, lane * 16, WS_PBP + pi * 1024);
    const u32x2 ml = *(LAS u32x2*)(L + L_PUB + 1024 + (lane & 3) * 8);
    DRAIN();
    next_slot();
    if (lane < 4) bst<SC1NT>(u32x4{ml[0], tagA, ml[1], tagB}, rws, lane * 16, WS_PBM + pi * 64);
  }
  if (hw == 3) cp_publish(0, misc, rws, bid, tagA, lane);
  wait_time<4>(t0, T_W);
  if (hw == 3) cp_consume(0, misc, rws, tagA, lane);
  {
    const GAS unsigned char* src = (const GAS unsigned char*)(woa + (size_t)(h * 128 + hw * 2) * DM) + lane * 16;
#pragma nounroll
    for (int i0 = 0; i0 < 32; i0 += 4) {
#pragma unroll
      for (int i = i0; i < i0 + 4; ++i) { const int o = (i >> 1) * 8192 + (i & 1) * 1024; CAPWAIT(2); glds16<SC1>(src + o, lds0 + L_BIG + hw * 2048 + o); }
      if (hw == 3) { if (i0 == 12) cp_publish(1, misc, rws, bid, tagA, lane); else if (i0 == 20) cp_consume(1, misc, rws, tagA, lane); }
      if (hw == 0 && i0 == 12) {
        u32x4 ml[1];
        poll<1, SC1NT>(ml, rws, (lane >> 3) * 64, 0, WS_PBM + (b * 8 + xg) * 512 + (h & 3) * 16, tagA, tagB);
        const u32x4 av = bld<SC1NT>(rws, (lane >> 3) * 1024 + (lane & 7) * 16, WS_PBP + (b * 8 + xg) * 8192 + (h & 3) * 256 + side * 128);
        const float pm = __uint_as_float(ml[0][0]), M = xrow_max(fmaxf(pm, dpp<XOR8>(pm)));
        const float wt = ex(pm - M) * __uint_as_float(ml[0][2]), Ls = xrow_sum(wt + dpp<XOR8>(wt));
        float o[8];
#pragma unroll
        for (int j = 0; j < 4; ++j) { o[2 * j] = wt * bflo(av[j]); o[2 * j + 1] = wt * bfhi(av[j]); }
        const f32x2 f = fold_rows(o);
        *(LAS unsigned*)(L + L_PUB + (lane & 7) * 16 + r * 4) = cvtpk((f[0] + dpp<XOR8>(f[0])) / Ls, (f[1] + dpp<XOR8>(f[1])) / Ls);
      }
    }
    DRAIN();
    hsync(misc, 5, lane);
    if (hw == 3) cp_publish(2, misc, rws, bid, tagA, lane);
  }
  unsigned w0[64], w1[64];
#pragma unroll
  for (int p = 0; p < 64; ++p) {
    const unsigned a = *(LAS unsigned*)(L + L_BIG + p * 2048 + tid * 4), bb = *(LAS unsigned*)(L + L_BIG + p * 2048 + 1024 + tid * 4);
    w0[p] = plo(bb, a); w1[p] = phi(bb, a);
    asm volatile("" : "+v"(w0[p]), "+v"(w1[p]));
  }
  hsync(misc, 6, lane);
  unsigned zpk;
  {
    const GAS unsigned char* src = (const GAS unsigned char*)(wob + (size_t)(h * 128 + side * 64 + hw * 2) * DM) + lane * 16;
    if (hw == 3) cp_consume(2, misc, rws, tagA, lane);
#pragma unroll 4
    for (int i = 0; i < 16; ++i) { const int o = (i >> 1) * 8192 + (i & 1) * 1024; CAPWAIT(2); glds16<SC1>(src + o, lds0 + L_BIG + hw * 2048 + o); }
    DRAIN();
    hsync(misc, 7, lane);
    float z0 = 0.f, z1 = 0.f;
#pragma unroll 4
    for (int p = 0; p < 32; ++p) {
      const unsigned a = *(LAS unsigned*)(L + L_BIG + p * 2048 + tid * 4), bb = *(LAS unsigned*)(L + L_BIG + p * 2048 + 1024 + tid * 4), op = *(LAS unsigned*)(L + L_PUB + p * 4);
      z0 = dot2(plo(bb, a), op, z0); z1 = dot2(phi(bb, a), op, z1);
    }
    zpk = cvtpk(z0, z1);
    asm volatile("" : "+v"(zpk));
  }
  {
    lds_wait_ge<32>(misc + M_KDONE, 4);
    const f32x4 wm = *(LAS f32x4*)(miscf + M_WMAX);
    const float M = fmaxf(fmaxf(wm[0], wm[1]), fmaxf(wm[2], wm[3]));
    float ls = 0.f;
#pragma unroll
    for (int i = 0; i < 2; ++i) {
      const u32x4 sc = *(LAS u32x4*)(L + L_SC + hw * 2048 + i * 1024 + lane * 16);
#pragma unroll
      for (int j = 0; j < 4; ++j) ls += ex(bflo(sc[j]) - M) + ex(bfhi(sc[j]) - M);
    }
    ls = wave_sum(ls);
    if (lane == 0) miscf[M_LSUM + hw] = ls;
    hsync(misc, 8, lane);
    if (hw == 0) {
      const f32x4 lw = *(LAS f32x4*)(miscf + M_LSUM);
      const float Ls = (lw[0] + lw[1]) + (lw[2] + lw[3]);
      if (lane < 4) bst<SC1NT>(u32x4{__float_as_uint(M), tagA, __float_as_uint(Ls), tagB}, rws, lane * 16, WS_ST + (b * 64 + jp) * 64);
      wait_time<32>(__builtin_amdgcn_s_memrealtime(), T_ST);
      u32x4 o[1];
      poll<1, SC1NT>(o, rws, 0, 0, WS_ST + (b * 64 + (jp ^ 1)) * 64, tagA, tagB);
      const float Mo = __uint_as_float(o[0][0]), MM = fmaxf(M, Mo);
      if (lane < 4) miscf[M_CNORM + lane] = ex(miscf[M_WMAX + lane] - MM) / (ex(M - MM) * Ls + ex(Mo - MM) * __uint_as_float(o[0][2]));
      if (bid == 0) {
        u32x4 all[4];
        wait_time<32>(__builtin_amdgcn_s_memrealtime(), T_ST);
        poll<4, SC1>(all, rws, lane * 64, 4096, WS_ST, tagA, tagB);
        if (lane == 0) __hip_atomic_store((GAS u64*)(ws + WS_EPOCH), epoch, RLX_AGENT);
      }
    } else if (hw == 3) {
      int k = 3, state = 0, rho = 0, rprev = RHO_MAX, have = 0, TP = 0;
      unsigned P = __builtin_amdgcn_readfirstlane(__hip_atomic_load(misc + M_ANCH, RLX_WG));
#pragma nounroll
      for (unsigned spins = 0; spins < SPIN_CAP && (unsigned)__builtin_amdgcn_readfirstlane(__hip_atomic_load(misc + M_STEP, RLX_WG)) < NS - 3; ++spins) {
        const int t = (int)(__builtin_amdgcn_s_memrealtime() - t0), tk = T_CP0 + (k - 3) * T_CP;
        if (state == 0 && t >= tk && k < NCP) { if (t < tk + T_CPLAG / 2) cp_publish(k, misc, rws, bid, tagA, lane); state = 1; }
        else if (state == 1 && t >= tk + T_CPLAG) {
          const unsigned Pe = P + (((tk - TP) * rho) >> 10), Pn = cp_mean(k, rws, Pe, tagA, lane);
          if (have) { const int rn = min(max((int)(Pn - P) * 1024 / T_CP, RHO_MIN), RHO_MAX); rho = min(rn, rprev); rprev = rn; }
          have = have || Pn != Pe; P = Pn; TP = tk; state = 0; ++k;
        }
        if (lane == 0) __hip_atomic_store(misc + M_ANCH, P + (((t - TP) * rho) >> 10), RLX_WG);
        __builtin_amdgcn_s_sleep(16);
      }
    }
  }
  int yoff = (tid >> 2) * 4096 + (tid & 3) * 16, goff = lane * 64 + hw * 16;
  const float gate = miscf[M_GATE];
  float gsel = lane < 32 ? gate : rbf(1.f - gate);
  asm volatile("" : "+v"(yoff), "+v"(goff), "+v"(gsel));
  if (hw == 1 && idx < 3) { l2_pin(code_here() + idx * 1024, lds0 + L_DUMP, lane); DRAIN(); }
  if (hw == 2) {
    if (lane == 0) __builtin_amdgcn_raw_buffer_store_b32(0u, mk_rsrc(out, NB * DM * 2), 0, (b * DM + jp * 8 + hw * 2) * 2, 0);
    DRAIN();
  }
  LDS_BARRIER();
  const unsigned yanch = misc[M_ANCH];
  {
    const f32x4 cn = *(LAS f32x4*)(miscf + M_CNORM);
    f32x4 rl[4], rh[4];
#pragma unroll
    for (int w = 0; w < 4; ++w) { rl[w] = *(LAS f32x4*)(L + L_REC + w * 512 + c * 32); rh[w] = *(LAS f32x4*)(L + L_REC + w * 512 + c * 32 + 16); }
    __builtin_amdgcn_sched_barrier(0);
    f32x4 lo = cn[0] * rl[0], hi = cn[0] * rh[0];
#pragma unroll
    for (int w = 1; w < 4; ++w) { lo += cn[w] * rl[w]; hi += cn[w] * rh[w]; }
    unsigned ob[4] = {cvtpk(lo[0], lo[1]), cvtpk(lo[2], lo[3]), cvtpk(hi[0], hi[1]), cvtpk(hi[2], hi[3])};
    float y0 = 0.f, y1 = 0.f, y2 = 0.f, y3 = 0.f;
    asm volatile("s_nop 1" : "+v"(ob[0]), "+v"(ob[1]), "+v"(ob[2]), "+v"(ob[3]), "+v"(y0), "+v"(y1), "+v"(y2), "+v"(y3));
    Proj<0>::run(y0, y1, y2, y3, w0, w1, ob);
    asm volatile("s_nop 2" : "+v"(y0), "+v"(y1), "+v"(y2), "+v"(y3));
    const unsigned ypk = cvtpk(y0 + y2, y1 + y3);
    bst<SC1>(u32x4{plo(zpk, ypk), tagA, phi(zpk, ypk), tagB}, rws, yoff, WS_YG + b * 64 * 4096 + jp * 64);
  }
  {
    {
      const u64 tp = __builtin_amdgcn_s_memrealtime();
      const int dl = min(max((int)(__builtin_amdgcn_readfirstlane(yanch) + T_Y - (unsigned)tp), Y_MIN), Y_MAX);
#pragma nounroll
      while ((int)(__builtin_amdgcn_s_memrealtime() - tp) < dl) {}
    }
    u32x4 g[1];
    poll<1, SC1>(g, rws, goff, 0, WS_YG + (b * 64 + jp) * 4096, tagA, tagB);
    const float u = row16_sum(fold16(fold32(bflo(g[0][0]), bfhi(g[0][0])), fold32(bflo(g[0][2]), bfhi(g[0][2]))));
    const float t = rbf(rbf(u) * gsel);
    const unsigned res = cvtpk(fold32(t, t), 0.f);
    if ((lane & 47) == 0) __builtin_amdgcn_raw_buffer_store_b16((u16)res, mk_rsrc(out, NB * DM * 2), r * 2, (b * DM + jp * 8 + hw * 2) * 2, 0);
  }
}

extern "C" void kernel_launch(void* const* d_in, const int* in_sizes, int n_in, void* d_out, int out_size, void* d_ws, size_t ws_size, hipStream_t stream) {
  k_fused<<<256, 512, 0, stream>>>((const u16*)d_in[0], (const u16*)d_in[1], (const u16*)d_in[2], (const u16*)d_in[3], (const u16*)d_in[4], (const u16*)d_in[6], (const u16*)d_in[7],
                                   (const u16*)d_in[8], (const u16*)d_in[9], (const u16*)d_in[10], (u16*)d_out, (unsigned char*)d_ws);
}
```
